```python
import torch

def _softmax(x, axis=-1):
    return torch.softmax(x, dim=axis)
def _gelu(x):
    return torch.nn.functional.gelu(x, approximate="tanh")
def _silu(x):
    return torch.nn.functional.silu(x)
def _sigmoid(x):
    return torch.sigmoid(x)
def _layernorm(x, axis=-1, eps=1e-5):
    m = x.mean(dim=axis, keepdim=True)
    v = x.var(dim=axis, keepdim=True, unbiased=False)
    return (x - m) / torch.sqrt(v + eps)
def _scatter_add(tgt, idx, src, axis=0):
    return tgt.clone().index_add_(axis, idx, src)
def _segment_sum(src, seg, n):
    out = torch.zeros((n,) + src.shape[1:], dtype=src.dtype)
    return out.index_add_(0, seg, src)
def _pad(x, pw):
    flat = tuple(v for pair in reversed(pw) for v in pair)
    return torch.nn.functional.pad(x, flat)

kernel_name = 'parallel_sum__linear_attn_block__glu_gated_bilinear__prompt'

def setup_inputs():

    _w = (lambda a: (lambda t: t.to(torch.bfloat16) if t.is_floating_point() else t)(torch.as_tensor(a)))
    def _A_setup():
        B, S, D = 1, 2048, 640
        return {
            "x": _w((lambda *s: torch.randn(*s, dtype=torch.bfloat16) * 0.05)(B, S, D)),
            "wqkv": _w((lambda *s: torch.randn(*s, dtype=torch.bfloat16) * 0.05)(D, 3 * D)),
            "wo": _w((lambda *s: torch.randn(*s, dtype=torch.bfloat16) * 0.05)(D, D)),
        }
    _A_in = _A_setup()

    def _B_setup():
        B, S, D, F = 1, 2048, 640, 1536
        return {
            "x": _w((lambda *s: torch.randn(*s, dtype=torch.bfloat16) * 0.05)(B, S, D)),
            "w_gate": _w((lambda *s: torch.randn(*s, dtype=torch.bfloat16) * 0.2)(D, F) * (D ** -0.5)),
            "w_up": _w((lambda *s: torch.randn(*s, dtype=torch.bfloat16) * 0.2)(D, F) * (D ** -0.5)),
            "w_down": _w((lambda *s: torch.randn(*s, dtype=torch.bfloat16) * 0.2)(F, D) * (F ** -0.5)),
        }
    _B_in = _B_setup()

    return {
        "A_x": _A_in["x"],
        "A_wqkv": _A_in["wqkv"],
        "A_wo": _A_in["wo"],
        "B_x": _B_in["x"],
        "B_w_gate": _B_in["w_gate"],
        "B_w_up": _B_in["w_up"],
        "B_w_down": _B_in["w_down"],
    }

def reference(A_x, A_wqkv, A_wo, B_x, B_w_gate, B_w_up, B_w_down):
    def _A_ref(x, wqkv, wo):
        B, S, D = x.shape
        H = 32; Dh = D // H
        qkv = (lambda x, *d: x.permute(*d))((x @ wqkv).reshape(B, S, 3, H, Dh), 2, 0, 3, 1, 4)
        q, k, v = qkv[0], qkv[1], qkv[2]
        phi = lambda t: torch.where(t > 0, t + 1, torch.exp(t)) + 1e-6
        pq, pk = phi(q), phi(k)
        kv = torch.einsum("bhsd,bhse->bhde", pk, v)
        z = torch.einsum("bhsd->bhd", pk)
        num = torch.einsum("bhqd,bhde->bhqe", pq, kv)
        den = torch.einsum("bhqd,bhd->bhq", pq, z)[..., None] + 1e-6
        o = num / den
        return (lambda x, *d: x.permute(*d))(o, 0, 2, 1, 3).reshape(B, S, D) @ wo

    def _B_ref(x, w_gate, w_up, w_down):
        g = torch.einsum("bsd,df->bsf", x, w_gate)
        u = torch.einsum("bsd,df->bsf", x, w_up)
        h = g * u
        return torch.einsum("bsf,fd->bsd", h, w_down)

    x = A_x
    xn = _layernorm(x, axis=-1)
    return x + _A_ref(xn, A_wqkv, A_wo) + _B_ref(xn, B_w_gate, B_w_up, B_w_down)
```

```cpp
#include <hip/hip_runtime.h>
#include <hip/hip_bf16.h>
typedef __hip_bfloat16 bf16;
constexpr int S=2048, D=640, F=1536, H=32, DH=20;
__device__ __forceinline__ float b2f(bf16 v){return __bfloat162float(v);}
__global__ void ln_k(const bf16* x, float* xn){
  int r=blockIdx.x; int t=threadIdx.x; __shared__ float red[256];
  float s=0; for(int i=t;i<D;i+=256) s+=b2f(x[r*D+i]); red[t]=s; __syncthreads();
  for(int o=128;o>0;o>>=1){ if(t<o) red[t]+=red[t+o]; __syncthreads(); }
  float m=red[0]/D; __syncthreads();
  s=0; for(int i=t;i<D;i+=256){ float d=b2f(x[r*D+i])-m; s+=d*d;} red[t]=s; __syncthreads();
  for(int o=128;o>0;o>>=1){ if(t<o) red[t]+=red[t+o]; __syncthreads(); }
  float rs=rsqrtf(red[0]/D+1e-5f);
  for(int i=t;i<D;i+=256) xn[r*D+i]=(b2f(x[r*D+i])-m)*rs;
}
__global__ void gemm_k(const float* A, const bf16* W, float* C, int K, int N){
  int n=blockIdx.x*256+threadIdx.x; int r=blockIdx.y; if(n>=N) return;
  float s=0; for(int k=0;k<K;k++) s+=A[r*K+k]*b2f(W[k*N+n]); C[r*N+n]=s;
}
__device__ float phi(float t){ return (t>0? t+1.f: expf(t))+1e-6f; }
__global__ void kv_k(const float* qkv, float* kv, float* z){
  int h=blockIdx.x; int t=threadIdx.x; if(t>=420) return;
  if(t<400){ int d=t/20,e=t%20; float s=0; for(int i=0;i<S;i++) s+=phi(qkv[i*1920+640+h*20+d])*qkv[i*1920+1280+h*20+e]; kv[h*400+t]=s; }
  else { int d=t-400; float s=0; for(int i=0;i<S;i++) s+=phi(qkv[i*1920+640+h*20+d]); z[h*20+d]=s; }
}
__global__ void o_k(const float* qkv,const float* kv,const float* z,float* o){
  int r=blockIdx.x; for(int c=threadIdx.x;c<D;c+=256){ int h=c/20,e=c%20; float num=0,den=0;
    for(int d=0;d<20;d++){ float p=phi(qkv[r*1920+h*20+d]); num+=p*kv[h*400+d*20+e]; den+=p*z[h*20+d]; }
    o[r*D+c]=num/(den+1e-6f);} }
__global__ void mul_k(const float* g,const float* u,float* h,int n){int i=blockIdx.x*256+threadIdx.x; if(i<n) h[i]=g[i]*u[i];}
__global__ void fin_k(const bf16* x,const float* a,const float* b,bf16* out,int n){int i=blockIdx.x*256+threadIdx.x; if(i<n) out[i]=__float2bfloat16(b2f(x[i])+a[i]+b[i]);}
extern "C" void kernel_launch(void* const* d_in, const int* in_sizes, int n_in, void* d_out, int out_size, void* d_ws, size_t ws_size, hipStream_t stream){
  const bf16* x=(const bf16*)d_in[0]; const bf16* wqkv=(const bf16*)d_in[1]; const bf16* wo=(const bf16*)d_in[2];
  const bf16* wg=(const bf16*)d_in[4]; const bf16* wu=(const bf16*)d_in[5]; const bf16* wd=(const bf16*)d_in[6];
  float* ws=(float*)d_ws; float* xn=ws; float* qkv=xn+S*D; float* g=qkv+S*1920; float* u=g+S*F; float* h=u+S*F; float* o=h+S*F; float* a=o+S*D; float* b=a+S*D; float* kv=b+S*D; float* z=kv+H*400;
  ln_k<<<S,256,0,stream>>>(x,xn);
  gemm_k<<<dim3((1920+255)/256,S),256,0,stream>>>(xn,wqkv,qkv,D,1920);
  gemm_k<<<dim3(F/256,S),256,0,stream>>>(xn,wg,g,D,F);
  gemm_k<<<dim3(F/256,S),256,0,stream>>>(xn,wu,u,D,F);
  mul_k<<<S*F/256,256,0,stream>>>(g,u,h,S*F);
  kv_k<<<H,448,0,stream>>>(qkv,kv,z);
  o_k<<<S,256,0,stream>>>(qkv,kv,z,o);
  gemm_k<<<dim3(3,S),256,0,stream>>>(o,wo,a,D,D);
  gemm_k<<<dim3(3,S),256,0,stream>>>(h,wd,b,F,D);
  fin_k<<<S*D/256,256,0,stream>>>(x,a,b,(bf16*)d_out,S*D);
}
```

```cpp
#include <hip/hip_runtime.h>
#include <hip/hip_bf16.h>
#include <stdint.h>

typedef __attribute__((ext_vector_type(8))) __bf16 bf8;
typedef __attribute__((ext_vector_type(2))) __bf16 bf2;
typedef __attribute__((ext_vector_type(4))) short s4;
typedef __attribute__((ext_vector_type(2))) float f2;
typedef __attribute__((ext_vector_type(4))) float f4;
typedef __attribute__((ext_vector_type(16))) float f16v;
typedef __attribute__((ext_vector_type(2))) unsigned u2;
typedef __attribute__((ext_vector_type(4))) unsigned u4;
#define LDSP __attribute__((address_space(3)))
#define GLBP __attribute__((address_space(1)))
#define DI __device__ __forceinline__

constexpr int S = 2048, D = 640, F = 1536;
constexpr int NCOL = 4992;
constexpr int NCSP = 5;
constexpr size_t WS_STATS = 0;
constexpr size_t WS_CS    = WS_STATS + 2048 * 8;
constexpr size_t WS_KVP   = 131072;
constexpr size_t WS_H     = 1 << 20;
constexpr size_t WS_Q     = WS_H + (size_t)S * F * 2;
constexpr size_t WS_O     = WS_Q + (size_t)S * D * 2;

constexpr int SLOT = 52 * 1024;
constexpr int LDS_BYTES = 160 * 1024;
constexpr int AUX = 3 * SLOT;

DI void glds16(const char* g, LDSP char* l) {
  __builtin_amdgcn_global_load_lds((const GLBP void*)g, (LDSP void*)l, 16, 0, 0);
}
DI u4 lds_r128(LDSP char* p) { return *(LDSP u4*)p; }
DI u2 lds_tr(LDSP char* p) { return __builtin_bit_cast(u2, __builtin_amdgcn_ds_read_tr16_b64_v4i16((LDSP s4*)p)); }
DI f16v mfma32(u4 a, u4 b, f16v c) { return __builtin_amdgcn_mfma_f32_32x32x16_bf16(__builtin_bit_cast(bf8, a), __builtin_bit_cast(bf8, b), c, 0, 0, 0); }
DI f4 mfma16(u4 a, u4 b, f4 c) { return __builtin_amdgcn_mfma_f32_16x16x32_bf16(__builtin_bit_cast(bf8, a), __builtin_bit_cast(bf8, b), c, 0, 0, 0); }
DI unsigned pk2(float lo, float hi) { f2 v = {lo, hi}; return __builtin_bit_cast(unsigned, __builtin_convertvector(v, bf2)); }
DI float blo(unsigned v) { return __uint_as_float(v << 16); }
DI float bhi(unsigned v) { return __uint_as_float(v & 0xffff0000u); }
DI float phi(float t) {
  float e = __builtin_amdgcn_exp2f(fminf(t * 1.44269504088896f, 0.f));
  return fmaxf(t + 1e-6f, 1e-6f) + e;
}
#define BAR() __builtin_amdgcn_s_barrier()
#define WAITV(n) asm volatile("s_waitcnt vmcnt(" #n ")" ::: "memory")
#define WAITL0() asm volatile("s_waitcnt lgkmcnt(0)" ::: "memory")
#define SCHED0() __builtin_amdgcn_sched_barrier(0)

struct Params {
  const char* x; const char* wqkv; const char* wo; const char* wg; const char* wu; const char* wd;
  char* out; char* ws;
};

__global__ __launch_bounds__(256) void k0_stats(Params p) {
  __shared__ float red[4][128];
  const int t = threadIdx.x, lane = t & 63, w = t >> 6, b = blockIdx.x;
  {
    const int row = b * 8 + w * 2 + (lane >> 5);
    const char* src = p.x + (size_t)row * 1280 + (lane & 31) * 8;
    u2 v[5];
#pragma unroll
    for (int i = 0; i < 5; i++) v[i] = *(const u2*)(src + i * 256);
    float s = 0;
#pragma unroll
    for (int i = 0; i < 5; i++) s += (blo(v[i][0]) + bhi(v[i][0])) + (blo(v[i][1]) + bhi(v[i][1]));
#pragma unroll
    for (int o = 1; o < 32; o <<= 1) s += __shfl_xor(s, o);
    const float mu = s * (1.f / 640.f);
    float q = 0;
#pragma unroll
    for (int i = 0; i < 5; i++) {
      float a = blo(v[i][0]) - mu, c = bhi(v[i][0]) - mu, d = blo(v[i][1]) - mu, e = bhi(v[i][1]) - mu;
      q += a * a + c * c + d * d + e * e;
    }
#pragma unroll
    for (int o = 1; o < 32; o <<= 1) q += __shfl_xor(q, o);
    const float rs = rsqrtf(q * (1.f / 640.f) + 1e-5f);
    if ((lane & 31) == 0) { f2 r = {mu, rs}; *(f2*)(p.ws + WS_STATS + row * 8) = r; }
  }
  if (b < 195) {
    const int cu = b % 39, kp = b / 39;
    const char* base; int stride;
    if (cu < 15) { base = p.wqkv + cu * 256; stride = 3840; }
    else if (cu < 27) { base = p.wg + (cu - 15) * 256; stride = 3072; }
    else { base = p.wu + (cu - 27) * 256; stride = 3072; }
    const char* src = base + (size_t)(kp * 128 + (t >> 4)) * stride + (t & 15) * 16;
    u4 v[8];
#pragma unroll
    for (int i = 0; i < 8; i++) v[i] = *(const u4*)(src + (size_t)i * 16 * stride);
    float s[8];
#pragma unroll
    for (int j = 0; j < 8; j++) s[j] = 0;
#pragma unroll
    for (int i = 0; i < 8; i++)
#pragma unroll
      for (int j = 0; j < 4; j++) { s[2 * j] += blo(v[i][j]); s[2 * j + 1] += bhi(v[i][j]); }
#pragma unroll
    for (int j = 0; j < 8; j++) { s[j] += __shfl_xor(s[j], 16); s[j] += __shfl_xor(s[j], 32); }
    if (lane < 16)
#pragma unroll
      for (int j = 0; j < 8; j++) red[w][lane * 8 + j] = s[j];
    __syncthreads();
    if (t < 128) ((float*)(p.ws + WS_CS))[kp * NCOL + cu * 128 + t] = red[0][t] + red[1][t] + red[2][t] + red[3][t];
  }
}

template <int TYPE>
DI void k1_loader(const Params& p, LDSP char* sm, int lw, int lane, int row0, int cg, int rot) {
  constexpr int NX = TYPE == 0 ? 5 : 8, NW = 13 - NX;
  constexpr int XB = TYPE == 0 ? 160 * 128 : 256 * 128;
  unsigned ox[NX], ow[NW];
#pragma unroll
  for (int i = 0; i < NX; i++) {
    const int r = (lw * NX + i) * 8 + (lane >> 3);
    ox[i] = (unsigned)((row0 + r) * 1280 + (((lane & 7) ^ ((r >> 1) & 7)) << 4));
  }
  const char* wbase; unsigned wstep;
  if (TYPE == 0) {
    const int isU = lw >> 1;
    wbase = (isU ? p.wu : p.wg) + cg * 256; wstep = 64 * 3072;
#pragma unroll
    for (int i = 0; i < NW; i++) {
      const int r = ((lw & 1) * 8 + i) * 4 + (lane >> 4);
      ow[i] = (unsigned)(r * 3072 + (((lane & 15) ^ ((r & 3) << 2) ^ (isU << 1)) << 4));
    }
  } else {
    wbase = p.wqkv + (TYPE == 1 ? cg * 320 : 1280 + cg * 160); wstep = 64 * 3840;
#pragma unroll
    for (int i = 0; i < NW; i++) {
      const int e = (lw * NW + i) * 64 + lane, r = e / 20, pos = e - r * 20;
      int cb = pos * 16;
      if (TYPE == 2) cb = pos < 10 ? pos * 16 : (pos < 12 ? 1280 + (pos - 2) * 16 : 1280 + (pos - 12) * 16);
      ow[i] = (unsigned)(r * 3840 + cb);
    }
  }
  auto issue = [&](int s, int slot) {
    int ks = s + rot; if (ks >= 10) ks -= 10;
    const char* xb = p.x + ks * 128;
    const char* wb = wbase + (size_t)ks * wstep;
    LDSP char* dx = sm + slot * SLOT + lw * NX * 1024;
    LDSP char* dw = sm + slot * SLOT + XB + lw * NW * 1024;
#pragma unroll
    for (int i = 0; i < NX; i++) glds16(xb + ox[i], dx + i * 1024);
#pragma unroll
    for (int i = 0; i < NW; i++) glds16(wb + ow[i], dw + i * 1024);
  };
  issue(0, 0); issue(1, 1);
  WAITV(13); BAR();
  int slot = 2;
#pragma unroll 1
  for (int s = 0; s < 8; s++) {
    issue(s + 2, slot); slot = slot == 2 ? 0 : slot + 1;
    WAITV(13); BAR();
  }
  WAITV(0); BAR();
}

template <int TYPE>
DI void k1_main(LDSP char* sm, int w, int lane, f16v (&acc)[10]) {
  constexpr int NXF = TYPE == 0 ? 5 : 2, NWF = 7 - NXF;
  constexpr int XB = TYPE == 0 ? 160 * 128 : 256 * 128;
  const int r = lane & 31, hh = lane >> 5, blk = (lane >> 4) & 1, q = (lane & 15) >> 2, pp = lane & 3;
  unsigned xa[4];
#pragma unroll
  for (int k = 0; k < 4; k++) xa[k] = (unsigned)(((TYPE == 0 ? 0 : w * 64) + r) * 128 + (((2 * k + hh) ^ ((r >> 1) & 7)) << 4));
  unsigned wa[2];
  if (TYPE == 0) {
#pragma unroll
    for (int ft = 0; ft < 2; ft++)
      wa[ft] = (unsigned)(XB + blk * 16384 + (8 * hh + q) * 256 + (((w * 4 + ft * 2 + (pp >> 1)) ^ (q << 2) ^ (blk << 1)) << 4) + (pp & 1) * 8);
  } else {
    wa[0] = (unsigned)(XB + (8 * hh + q) * 320 + blk * 32 + pp * 8); wa[1] = 0;
  }
  u4 xf[2][NXF], wf[2][NWF];
  auto rd = [&](int buf, LDSP char* base, int k) {
#pragma unroll
    for (int i = 0; i < NWF; i++) {
      u2 lo, hi;
      if (TYPE == 0) { lo = lds_tr(base + wa[i] + k * 4096); hi = lds_tr(base + wa[i] + k * 4096 + 1024); }
      else { lo = lds_tr(base + wa[0] + k * 5120 + i * 64); hi = lds_tr(base + wa[0] + k * 5120 + i * 64 + 1280); }
      wf[buf][i] = u4{lo[0], lo[1], hi[0], hi[1]};
    }
#pragma unroll
    for (int i = 0; i < NXF; i++) xf[buf][i] = lds_r128(base + xa[k] + i * 4096);
  };
  auto mm = [&](int buf) {
#pragma unroll
    for (int i = 0; i < NXF; i++)
#pragma unroll
      for (int j = 0; j < NWF; j++) {
        if (TYPE == 2) acc[i * NWF + j] = mfma32(xf[buf][i], wf[buf][j], acc[i * NWF + j]);
        else acc[i * NWF + j] = mfma32(wf[buf][j], xf[buf][i], acc[i * NWF + j]);
      }
  };
  auto inter = [&]() {
    constexpr int NR = NXF + 2 * NWF;
#pragma unroll
    for (int i = 0; i < 6; i++) {
      __builtin_amdgcn_sched_group_barrier(0x8, 1, 0);
      __builtin_amdgcn_sched_group_barrier(0x100, (NR + 5) / 6, 0);
    }
    __builtin_amdgcn_sched_group_barrier(0x8, 4, 0);
  };
  BAR();
  rd(0, sm, 0);
  int slot = 0;
#pragma unroll 1
  for (int s = 0; s < 9; s++) {
    LDSP char* base = sm + slot * SLOT;
    slot = slot == 2 ? 0 : slot + 1;
    SCHED0();
    rd(1, base, 1); mm(0); inter(); SCHED0();
    rd(0, base, 2); mm(1); inter(); SCHED0();
    rd(1, base, 3); mm(0); inter(); SCHED0();
    WAITL0(); BAR(); SCHED0();
    rd(0, sm + slot * SLOT, 0); mm(1); inter(); SCHED0();
  }
  {
    LDSP char* base = sm + slot * SLOT;
    SCHED0();
    rd(1, base, 1); mm(0); inter(); SCHED0();
    rd(0, base, 2); mm(1); inter(); SCHED0();
    rd(1, base, 3); mm(0); inter(); SCHED0();
    mm(1); SCHED0();
  }
}

DI void k1_tile(int b, int& type, int& rt, int& cg) {
  const int c = b & 7, j = b >> 3, a = c & 1, q = c >> 1;
  if (j < 12) { rt = 4 * a + j / 3; const int m = j % 3; if (m == 0) { type = 1; cg = q; } else { type = 2; cg = 2 * q + m - 1; } }
  else { const int n = j - 12 + (a ? 19 : 0); if (!a && j == 31) { type = 3; rt = cg = 0; } else { type = 0; rt = n / 3; cg = 3 * q + n % 3; } }
}

template <int TYPE>
DI void k1_load_aux(const Params& p, LDSP char* sm, int t, int row0, int cg) {
  constexpr int NT = TYPE == 0 ? 160 : 256, NC = TYPE == 0 ? 256 : 160;
  if (t < NT) *(LDSP f2*)(sm + AUX + t * 8) = *(const f2*)(p.ws + WS_STATS + (row0 + t) * 8);
  if (t < NC) {
    int col;
    if (TYPE == 0) col = 1920 + (t >> 7) * 1536 + cg * 128 + (t & 127);
    else if (TYPE == 1) col = cg * 160 + t;
    else col = t < 80 ? 640 + cg * 80 + t : (t < 96 ? 1280 + cg * 80 + t - 16 : 1280 + cg * 80 + t - 96);
    const float* cs = (const float*)(p.ws + WS_CS) + col;
    float s = 0;
#pragma unroll
    for (int i = 0; i < NCSP; i++) s += cs[i * NCOL];
    *(LDSP float*)(sm + AUX + 2048 + t * 4) = s;
  }
}

template <int TYPE>
DI void k1_body(const Params& p, LDSP char* sm, int rt, int cg, int rot) {
  const int tid = threadIdx.x, lane = tid & 63, w = __builtin_amdgcn_readfirstlane(tid >> 6);
  const int row0 = TYPE == 0 ? min(rt * 160, S - 160) : rt * 256;
  const int hh = lane >> 5, r = lane & 31;
  if (w >= 4) {
    k1_load_aux<TYPE>(p, sm, tid - 256, row0, cg);
    asm volatile("s_waitcnt vmcnt(0) lgkmcnt(0)" ::: "memory");
    k1_loader<TYPE>(p, sm, w - 4, lane, row0, cg, rot);
    if (TYPE == 2) { BAR(); BAR(); }
  } else {
    f16v acc[10];
#pragma unroll
    for (int i = 0; i < 10; i++)
#pragma unroll
      for (int j = 0; j < 16; j++) acc[i][j] = 0.f;
    k1_main<TYPE>(sm, w, lane, acc);
    if (TYPE == 0) {
      f4 cs[2][4];
#pragma unroll
      for (int ft = 0; ft < 2; ft++)
#pragma unroll
        for (int qd = 0; qd < 4; qd++)
          cs[ft][qd] = *(LDSP f4*)(sm + AUX + 2048 + ((qd >> 1) * 128 + w * 32 + ft * 16 + (qd & 1) * 8 + hh * 4) * 4);
#pragma unroll
      for (int tt = 0; tt < 5; tt++) {
        const float mu = (*(LDSP f2*)(sm + AUX + (tt * 32 + r) * 8))[0];
        const int tok = row0 + tt * 32 + r;
        char* dst = p.ws + WS_H + (size_t)(((tok >> 3) * 48 + cg * 4 + w) * 4 + hh) * 128 + (tok & 7) * 16;
#pragma unroll
        for (int ft = 0; ft < 2; ft++) {
          const f16v a = acc[tt * 2 + ft];
          float h[8];
#pragma unroll
          for (int j = 0; j < 8; j++) h[j] = (a[j] - mu * cs[ft][j >> 2][j & 3]) * (a[8 + j] - mu * cs[ft][2 + (j >> 2)][j & 3]);
          *(u4*)(dst + ft * 256) = u4{pk2(h[0], h[1]), pk2(h[2], h[3]), pk2(h[4], h[5]), pk2(h[6], h[7])};
        }
      }
    } else if (TYPE == 1) {
#pragma unroll
      for (int tt = 0; tt < 2; tt++) {
        const f2 st = *(LDSP f2*)(sm + AUX + (w * 64 + tt * 32 + r) * 8);
        const int tok = row0 + w * 64 + tt * 32 + r;
        char* dst = p.ws + WS_Q + (size_t)(((tok >> 3) * 20 + cg * 5) * 4 + hh) * 128 + (tok & 7) * 16;
#pragma unroll
        for (int ft = 0; ft < 5; ft++) {
          const f16v a = acc[tt * 5 + ft];
          float v[16];
#pragma unroll
          for (int qd = 0; qd < 4; qd++) {
            const f4 cs = *(LDSP f4*)(sm + AUX + 2048 + (ft * 32 + qd * 8 + hh * 4) * 4);
#pragma unroll
            for (int j = 0; j < 4; j++) v[qd * 4 + j] = (a[qd * 4 + j] - st[0] * cs[j]) * st[1];
          }
          *(u4*)(dst + ft * 512) = u4{pk2(v[0], v[1]), pk2(v[2], v[3]), pk2(v[4], v[5]), pk2(v[6], v[7])};
          *(u4*)(dst + ft * 512 + 256) = u4{pk2(v[8], v[9]), pk2(v[10], v[11]), pk2(v[12], v[13]), pk2(v[14], v[15])};
        }
      }
    } else {
      float cs[5];
#pragma unroll
      for (int ct = 0; ct < 5; ct++) cs[ct] = *(LDSP float*)(sm + AUX + 2048 + (ct * 32 + r) * 4);
      const bool isk = r < 16;
      const float kmul = isk ? 1.f : 0.f, kadd = isk ? 1e-6f : 0.f, klo = isk ? 1e-6f : -__builtin_inff();
      u4 fr[4][5];
#pragma unroll
      for (int tt = 0; tt < 2; tt++)
#pragma unroll
        for (int s = 0; s < 2; s++) {
          float mu[8], rs[8];
#pragma unroll
          for (int hq = 0; hq < 2; hq++) {
            LDSP char* sp = sm + AUX + (w * 64 + tt * 32 + s * 16 + hq * 8 + hh * 4) * 8;
            const f4 a = *(LDSP f4*)sp, b = *(LDSP f4*)(sp + 16);
            mu[hq * 4] = a[0]; rs[hq * 4] = a[1]; mu[hq * 4 + 1] = a[2]; rs[hq * 4 + 1] = a[3];
            mu[hq * 4 + 2] = b[0]; rs[hq * 4 + 2] = b[1]; mu[hq * 4 + 3] = b[2]; rs[hq * 4 + 3] = b[3];
          }
#pragma unroll
          for (int ct = 0; ct < 5; ct++) {
            float v[8];
#pragma unroll
            for (int j = 0; j < 8; j++) {
              float t = (acc[tt * 5 + ct][s * 8 + j] - mu[j] * cs[ct]) * rs[j];
              if (ct < 2) t = phi(t);
              else if (ct == 2) t = fmaf(__builtin_amdgcn_exp2f(fminf(t * 1.44269504088896f, 0.f)), kmul, fmaxf(t + kadd, klo));
              v[j] = t;
            }
            fr[tt * 2 + s][ct] = u4{pk2(v[0], v[1]), pk2(v[2], v[3]), pk2(v[4], v[5]), pk2(v[6], v[7])};
          }
          SCHED0();
        }
      f16v z[8];
#pragma unroll
      for (int i = 0; i < 8; i++)
#pragma unroll
        for (int j = 0; j < 16; j++) z[i][j] = 0.f;
#pragma unroll
      for (int g = 0; g < 4; g++) {
          u4 mb;
#pragma unroll
          for (int j = 0; j < 4; j++) mb[j] = isk ? 0x3f803f80u : fr[g][2][j];
          z[0] = mfma32(fr[g][0], fr[g][3], z[0]);
          z[1] = mfma32(fr[g][0], fr[g][4], z[1]);
          z[2] = mfma32(fr[g][0], mb, z[2]);
          z[3] = mfma32(fr[g][1], fr[g][3], z[3]);
          z[4] = mfma32(fr[g][1], fr[g][4], z[4]);
          z[5] = mfma32(fr[g][1], mb, z[5]);
          z[6] = mfma32(fr[g][2], fr[g][4], z[6]);
          z[7] = mfma32(fr[g][2], mb, z[7]);
        }
      SCHED0();
      BAR();
      LDSP char* dst = sm + w * 32768 + lane * 16;
#pragma unroll
      for (int i = 0; i < 8; i++)
#pragma unroll
        for (int qd = 0; qd < 4; qd++) {
          const bool need = (i == 1) ? qd >= 2 : (i == 3) ? qd == 0 : (i >= 6) ? qd < 2 : true;
          if (need) *(LDSP f4*)(dst + (i * 4 + qd) * 1024) = f4{z[i][qd * 4], z[i][qd * 4 + 1], z[i][qd * 4 + 2], z[i][qd * 4 + 3]};
        }
      WAITL0(); BAR();
    }
  }
  if (TYPE == 2 && tid < 420) {
    int d0, vt, col;
    if (tid < 400) {
      const int hl = tid / 100, rem = tid - hl * 100, dg = rem / 20, e = rem - dg * 20, eg = hl * 20 + e;
      d0 = hl * 20 + dg * 4; vt = eg >> 5; col = vt == 2 ? eg - 48 : (eg & 31);
    } else { d0 = (tid - 400) * 4; vt = 2; col = 0; }
    const int kt = d0 >> 5, ti = kt == 2 ? 5 + vt : kt * 3 + vt;
    LDSP char* src = sm + (ti * 4 + ((d0 & 31) >> 3)) * 1024 + (((d0 >> 2) & 1) * 32 + col) * 16;
    const f4 s = (*(LDSP f4*)src + *(LDSP f4*)(src + 32768)) + (*(LDSP f4*)(src + 65536) + *(LDSP f4*)(src + 98304));
    *(f4*)(p.ws + WS_KVP + (size_t)((rt * 8 + cg) * 448 + tid) * 16) = s;
  }
}

__global__ __launch_bounds__(512, 2) void k1_gemm(Params p) {
  extern __shared__ __attribute__((aligned(16))) char smem[];
  LDSP char* sm = (LDSP char*)smem;
  int type, rt, cg;
  k1_tile(blockIdx.x, type, rt, cg);
  const int rot = 0;
  if (type == 0) k1_body<0>(p, sm, rt, cg, rot);
  else if (type == 1) k1_body<1>(p, sm, rt, cg, rot);
  else if (type == 2) k1_body<2>(p, sm, rt, cg, rot);
}

__global__ __launch_bounds__(512) void k15_attn(Params p) {
  __shared__ __attribute__((aligned(16))) char smem[25 * 1024 + 256];
  LDSP char* sm = (LDSP char*)smem;
  const int tid = threadIdx.x, lane = tid & 63, w = __builtin_amdgcn_readfirstlane(tid >> 6);
  const int hg = blockIdx.x & 7, tg = blockIdx.x >> 3;
  for (int i = tid; i < 1600; i += 512) *(LDSP u4*)(sm + i * 16) = u4{0, 0, 0, 0};
  u2 pv = {0, 0};
  if (tid < 420) {
    f4 s = {0, 0, 0, 0};
#pragma unroll
    for (int i = 0; i < 8; i++) s += *(const f4*)(p.ws + WS_KVP + (size_t)((i * 8 + hg) * 448 + tid) * 16);
    pv = u2{pk2(s[0], s[1]), pk2(s[2], s[3])};
    if (tid >= 400) *(LDSP u2*)(sm + 25600 + (tid - 400) * 8) = pv;
  }
  __syncthreads();
  if (tid < 400) {
    const int hl = tid / 100, rem = tid - hl * 100, dg = rem / 20, e = rem - dg * 20, eg = hl * 20 + e, d0 = hl * 20 + dg * 4;
    LDSP char* dst = sm + ((eg >> 4) * 5 + (d0 >> 4)) * 1024 + (((d0 >> 2) & 1) * 32 + (eg & 15)) * 16 + ((d0 >> 3) & 1) * 8;
    *(LDSP u2*)dst = pv;
    *(LDSP u2*)(dst + 256) = *(LDSP u2*)(sm + 25600 + (d0 >> 2) * 8);
  }
  __syncthreads();
  if (w < 4) {
    const int tt = w & 1, part = w >> 1, hh = lane >> 5;
    const int tok = tg * 64 + tt * 32 + (lane & 31);
    const size_t rowoff = (size_t)(tok >> 3) * 20 * 512 + hh * 128 + (tok & 7) * 16;
    u4 pq[4];
#pragma unroll
    for (int i = 0; i < 4; i++) {
      const int T = min(part * 2 + i, 4);
      const u4 v = *(const u4*)(p.ws + WS_Q + rowoff + (hg * 5 + T) * 256);
#pragma unroll
      for (int j = 0; j < 4; j++) pq[i][j] = pk2(phi(blo(v[j])), phi(bhi(v[j])));
    }
    f16v zero;
#pragma unroll
    for (int j = 0; j < 16; j++) zero[j] = 0.f;
    auto fin = [&](int T, const f16v& a) {
      float o[8];
#pragma unroll
      for (int j = 0; j < 8; j++) o[j] = a[j] * __builtin_amdgcn_rcpf(a[8 + j] + 1e-6f);
      *(u4*)(p.ws + WS_O + rowoff + (hg * 5 + T) * 256) = u4{pk2(o[0], o[1]), pk2(o[2], o[3]), pk2(o[4], o[5]), pk2(o[6], o[7])};
    };
    auto A = [&](int T, int ks) { return lds_r128(sm + (T * 5 + ks) * 1024 + lane * 16); };
    if (part == 0) {
      f16v a0 = mfma32(A(0, 0), pq[0], zero); a0 = mfma32(A(0, 1), pq[1], a0);
      f16v a1 = mfma32(A(1, 0), pq[0], zero); a1 = mfma32(A(1, 1), pq[1], a1); a1 = mfma32(A(1, 2), pq[2], a1);
      f16v a2 = mfma32(A(2, 1), pq[1], zero); a2 = mfma32(A(2, 2), pq[2], a2); a2 = mfma32(A(2, 3), pq[3], a2);
      fin(0, a0); fin(1, a1); fin(2, a2);
    } else {
      f16v a3 = mfma32(A(3, 2), pq[0], zero); a3 = mfma32(A(3, 3), pq[1], a3); a3 = mfma32(A(3, 4), pq[2], a3);
      f16v a4 = mfma32(A(4, 3), pq[1], zero); a4 = mfma32(A(4, 4), pq[2], a4);
      fin(3, a3); fin(4, a4);
    }
  }
}

constexpr int SLOT2 = 19 * 1024, NSLOT2 = 8;
__global__ __launch_bounds__(512, 2) void k2_gemm(Params p) {
  extern __shared__ __attribute__((aligned(16))) char smem[];
  LDSP char* sm = (LDSP char*)smem;
  const int tid = threadIdx.x, lane = tid & 63, w = __builtin_amdgcn_readfirstlane(tid >> 6);
  const int b = blockIdx.x, n = (b & 7) * 32 + (b >> 3);
  if (n >= 250) return;
  const int rt = n / 10, cg = n - rt * 10;
  const int oct0 = rt * 10 + min(rt, 6), noct = rt < 6 ? 11 : 10;
  if (w >= 4) {
    const int lw = w - 4;
    if (lw < 2) {
      const int o0 = lw * 5, cnt = lw == 0 ? 5 : noct - 5;
      auto issue = [&](int s, int slot) {
        const char* src = s < 24 ? p.ws + WS_H + (size_t)(oct0 + o0) * 24576 + s * 1024 : p.ws + WS_O + (size_t)(oct0 + o0) * 10240 + (s - 24) * 1024;
        const unsigned st = s < 24 ? 24576 : 10240;
        LDSP char* d = sm + slot * SLOT2 + o0 * 1024;
#pragma unroll
        for (int i = 0; i < 5; i++) glds16(src + i * st + lane * 16, d + i * 1024);
        if (cnt == 6) glds16(src + 5 * st + lane * 16, d + 5 * 1024);
      };
      for (int s = 0; s < 7; s++) issue(s, s);
      if (cnt == 6) WAITV(36); else WAITV(30);
      BAR();
#pragma unroll 1
      for (int s = 0; s < 27; s++) {
        issue(s + 7, (s + 7) & 7);
        if (cnt == 6) WAITV(36); else WAITV(30);
        BAR();
      }
      if (cnt == 6) { WAITV(30); BAR(); WAITV(24); BAR(); WAITV(18); BAR(); WAITV(12); BAR(); WAITV(6); BAR(); WAITV(0); BAR(); }
      else { WAITV(25); BAR(); WAITV(20); BAR(); WAITV(15); BAR(); WAITV(10); BAR(); WAITV(5); BAR(); WAITV(0); BAR(); }
    } else {
      unsigned off[4];
#pragma unroll
      for (int i = 0; i < 4; i++) {
        const int r = ((lw - 2) * 4 + i) * 8 + (lane >> 3);
        off[i] = (unsigned)(r * 1280 + cg * 128 + (((lane & 7) ^ (((r >> 1) & 3) << 1)) << 4));
      }
      auto issue = [&](int s, int slot) {
        const char* src = s < 24 ? p.wd + (size_t)s * 64 * 1280 : p.wo + (size_t)(s - 24) * 64 * 1280;
        LDSP char* d = sm + slot * SLOT2 + 11 * 1024 + (lw - 2) * 4096;
#pragma unroll
        for (int i = 0; i < 4; i++) glds16(src + off[i], d + i * 1024);
      };
      for (int s = 0; s < 7; s++) issue(s, s);
      WAITV(24); BAR();
#pragma unroll 1
      for (int s = 0; s < 27; s++) {
        issue(s + 7, (s + 7) & 7);
        WAITV(24); BAR();
      }
      WAITV(20); BAR(); WAITV(16); BAR(); WAITV(12); BAR(); WAITV(8); BAR(); WAITV(4); BAR(); WAITV(0); BAR();
    }
    BAR(); BAR();
  } else {
    const int t = lane & 15, g = lane >> 4, q = (lane & 15) >> 2, pp = lane & 3;
    const unsigned aa = (unsigned)((t >> 3) * 1024 + g * 128 + (t & 7) * 16);
    const int br = (g >> 1) * 16 + (g & 1) * 4 + q;
    const unsigned ba = (unsigned)(11 * 1024 + br * 128 + ((w ^ ((br >> 1) & 3)) << 5) + pp * 8);
    float r2[6];
#pragma unroll
    for (int i = 0; i < 6; i++) {
      const int tok = min(oct0 * 8 + i * 16 + t, S - 1);
      const float rs = (*(const f2*)(p.ws + WS_STATS + tok * 8))[1];
      r2[i] = rs * rs;
    }
    f4 acc[6];
#pragma unroll
    for (int i = 0; i < 6; i++) acc[i] = f4{0, 0, 0, 0};
    u4 af[2][6], bfr[2];
    auto rd = [&](int buf, LDSP char* base, int k) {
      const u2 lo = lds_tr(base + ba + k * 4096), hi = lds_tr(base + ba + k * 4096 + 1024);
      bfr[buf] = u4{lo[0], lo[1], hi[0], hi[1]};
#pragma unroll
      for (int i = 0; i < 6; i++) af[buf][i] = lds_r128(base + aa + i * 2048 + k * 512);
    };
    auto mm = [&](int buf) {
#pragma unroll
      for (int i = 0; i < 6; i++) acc[i] = mfma16(bfr[buf], af[buf][i], acc[i]);
    };
    BAR();
    rd(0, sm, 0);
#pragma unroll 1
    for (int s = 0; s < 34; s++) {
      if (s == 24) {
#pragma unroll
        for (int i = 0; i < 6; i++) acc[i] *= r2[i];
      }
      SCHED0();
      rd(1, sm + (s & 7) * SLOT2, 1); mm(0); SCHED0();
      if (s < 33) {
        WAITL0(); BAR(); SCHED0();
        rd(0, sm + ((s + 1) & 7) * SLOT2, 0);
      }
      mm(1); SCHED0();
    }
    BAR();
#pragma unroll
    for (int i = 0; i < 6; i++) *(LDSP f4*)(sm + (i * 16 + t) * 272 + w * 64 + g * 16) = acc[i];
    WAITL0(); BAR();
  }
  for (int i = tid; i < noct * 64; i += 512) {
    const int row = i >> 3, c = i & 7;
    const size_t go = (size_t)(oct0 * 8 + row) * 1280 + cg * 128 + c * 16;
    const u4 xv = *(const u4*)(p.x + go);
    const f4 a = *(LDSP f4*)(sm + row * 272 + c * 32), bb = *(LDSP f4*)(sm + row * 272 + c * 32 + 16);
    *(u4*)(p.out + go) = u4{pk2(blo(xv[0]) + a[0], bhi(xv[0]) + a[1]), pk2(blo(xv[1]) + a[2], bhi(xv[1]) + a[3]),
                            pk2(blo(xv[2]) + bb[0], bhi(xv[2]) + bb[1]), pk2(blo(xv[3]) + bb[2], bhi(xv[3]) + bb[3])};
  }
}

extern "C" void kernel_launch(void* const* d_in, const int* in_sizes, int n_in, void* d_out, int out_size,
                              void* d_ws, size_t ws_size, hipStream_t stream) {
  Params p;
  p.x = (const char*)d_in[0]; p.wqkv = (const char*)d_in[1]; p.wo = (const char*)d_in[2];
  p.wg = (const char*)d_in[4]; p.wu = (const char*)d_in[5]; p.wd = (const char*)d_in[6];
  p.out = (char*)d_out; p.ws = (char*)d_ws;
  (void)hipFuncSetAttribute((const void*)k1_gemm, hipFuncAttributeMaxDynamicSharedMemorySize, LDS_BYTES);
  (void)hipFuncSetAttribute((const void*)k2_gemm, hipFuncAttributeMaxDynamicSharedMemorySize, LDS_BYTES);
  k0_stats<<<256, 256, 0, stream>>>(p);
  k1_gemm<<<256, 512, LDS_BYTES, stream>>>(p);
  k15_attn<<<256, 512, 0, stream>>>(p);
  k2_gemm<<<256, 512, LDS_BYTES, stream>>>(p);
}
```

```cpp
#include <hip/hip_runtime.h>
#include <hip/hip_bf16.h>
#include <stdint.h>

typedef __attribute__((ext_vector_type(8))) __bf16 bf8;
typedef __attribute__((ext_vector_type(2))) __bf16 bf2;
typedef __attribute__((ext_vector_type(4))) short s4;
typedef __attribute__((ext_vector_type(2))) float f2;
typedef __attribute__((ext_vector_type(4))) float f4;
typedef __attribute__((ext_vector_type(16))) float f16v;
typedef __attribute__((ext_vector_type(2))) unsigned u2;
typedef __attribute__((ext_vector_type(4))) unsigned u4;
typedef unsigned long long u64;
typedef __amdgpu_buffer_rsrc_t rsrc_t;
#define LDSP __attribute__((address_space(3)))
#define GLBP __attribute__((address_space(1)))
#define DI __device__ __forceinline__

constexpr int S = 2048, D = 640, F = 1536;
constexpr int NCOL = 4992;
constexpr unsigned WS_GEN = 0;
constexpr unsigned WS_F0 = 4096;
constexpr unsigned WS_FH = 8192;
constexpr unsigned WS_FKV = 12288;
constexpr unsigned WS_FO = 16384;
constexpr unsigned WS_STATS = 32768;
constexpr unsigned WS_CS = 65536;
constexpr unsigned WS_KVP = 262144;
constexpr unsigned WS_H = 1 << 20;
constexpr unsigned WS_O = WS_H + S * F * 2;
constexpr unsigned WS_END = WS_O + S * D * 2;

constexpr int SLOT = 52 * 1024;
constexpr int LDS_BYTES = 160 * 1024;
constexpr int AUX = 3 * SLOT;
constexpr int IMG = 54 * 1024;
constexpr int SC1 = 16;

DI rsrc_t mkrs(const void* b, unsigned n) { return __builtin_amdgcn_make_buffer_rsrc((void*)b, (short)0, (int)n, 0x00020000); }
DI void blds16(rsrc_t rs, unsigned voff, unsigned soff, LDSP char* l) {
  __builtin_amdgcn_raw_ptr_buffer_load_lds(rs, (LDSP void*)l, 16, voff, soff, 0, 0);
}
template <int AUXB> DI u4 bld128(rsrc_t rs, unsigned voff, unsigned soff) { return __builtin_amdgcn_raw_buffer_load_b128(rs, voff, soff, AUXB); }
template <int AUXB> DI u2 bld64(rsrc_t rs, unsigned voff, unsigned soff) { return __builtin_amdgcn_raw_buffer_load_b64(rs, voff, soff, AUXB); }
template <int AUXB> DI unsigned bld32(rsrc_t rs, unsigned voff, unsigned soff) { return __builtin_amdgcn_raw_buffer_load_b32(rs, voff, soff, AUXB); }
DI void bst128(u4 v, rsrc_t rs, unsigned voff, unsigned soff) { __builtin_amdgcn_raw_buffer_store_b128(v, rs, voff, soff, SC1); }
DI void bst64(u2 v, rsrc_t rs, unsigned voff, unsigned soff) { __builtin_amdgcn_raw_buffer_store_b64(v, rs, voff, soff, SC1); }
DI u4 lds_r128(LDSP char* p) { return *(LDSP u4*)p; }
DI u2 lds_tr(LDSP char* p) { return __builtin_bit_cast(u2, __builtin_amdgcn_ds_read_tr16_b64_v4i16((LDSP s4*)p)); }
DI f16v mfma32(u4 a, u4 b, f16v c) { return __builtin_amdgcn_mfma_f32_32x32x16_bf16(__builtin_bit_cast(bf8, a), __builtin_bit_cast(bf8, b), c, 0, 0, 0); }
DI unsigned pk2(float lo, float hi) { f2 v = {lo, hi}; return __builtin_bit_cast(unsigned, __builtin_convertvector(v, bf2)); }
DI float blo(unsigned v) { return __uint_as_float(v << 16); }
DI float bhi(unsigned v) { return __uint_as_float(v & 0xffff0000u); }
DI float exp2c(float u) { return __builtin_amdgcn_fmed3f(__builtin_amdgcn_exp2f(u), 0.f, 1.f); }
DI u64 ld_flag(const char* ws, unsigned off) { return __hip_atomic_load((const GLBP u64*)(ws + off), __ATOMIC_RELAXED, __HIP_MEMORY_SCOPE_AGENT); }
DI void st_flag(char* ws, unsigned off, u64 v) { __hip_atomic_store((GLBP u64*)(ws + off), v, __ATOMIC_RELAXED, __HIP_MEMORY_SCOPE_AGENT); }
#define BAR() __builtin_amdgcn_s_barrier()
#define WAITV(n) asm volatile("s_waitcnt vmcnt(" #n ")" ::: "memory")
#define WAITL0() asm volatile("s_waitcnt lgkmcnt(0)" ::: "memory")
#define SCHED0() __builtin_amdgcn_sched_barrier(0)
#define FENCE() do { asm volatile("" ::: "memory"); __builtin_amdgcn_sched_barrier(0); } while (0)
constexpr float LOG2E = 1.44269504088896f;

#define STAMP(i)
#define STAMPR(i)

struct Params {
  const char* x; const char* wqkv; const char* wo; const char* wg; const char* wu; const char* wd;
  char* out; char* ws;
};

DI void wait_flag(const char* ws, unsigned off, u64 tag, u64 first) {
  u64 v = first;
#pragma unroll 1
  for (int it = 0; it < (1 << 18); it++) {
    if (__all(v == tag)) break;
    __builtin_amdgcn_s_sleep(1);
    v = ld_flag(ws, off);
  }
  FENCE();
}
DI u64 lds_tag(LDSP char* sm) {
  const u2 t = *(LDSP u2*)(sm + AUX + 3072);
  return (u64)(unsigned)__builtin_amdgcn_readfirstlane(t[0]) | ((u64)(unsigned)__builtin_amdgcn_readfirstlane(t[1]) << 32);
}

struct P0 { u2 xs[5]; u4 cw[10]; };
DI void p0_load(const Params& p, P0& d, int lw, int lane, bool hascs) {
  const int b = blockIdx.x;
  const rsrc_t rx = mkrs(p.x, S * D * 2);
  const unsigned xo = (unsigned)((b * 8 + lw * 2 + (lane >> 5)) * 1280 + (lane & 31) * 8);
#pragma unroll
  for (int i = 0; i < 5; i++) d.xs[i] = bld64<0>(rx, xo + i * 256, 0);
  if (hascs) {
    const int task = lw * 256 + b, kp = task / 78, cu = task - kp * 78;
    const char* base = cu < 30 ? p.wqkv + cu * 128 : (cu < 54 ? p.wg + (cu - 30) * 128 : p.wu + (cu - 54) * 128);
    const unsigned stride = cu < 30 ? 3840 : 3072;
    const rsrc_t rw = mkrs(base, 640 * stride - 127);
    const unsigned wo = (unsigned)(kp * 80 + (lane >> 3)) * stride + (lane & 7) * 16;
#pragma unroll
    for (int i = 0; i < 10; i++) d.cw[i] = bld128<0>(rw, wo, i * 8 * stride);
  }
}
DI void p0_finish(const Params& p, P0& d, rsrc_t rws, int lw, int lane, bool hascs) {
  const int b = blockIdx.x;
#pragma unroll
  for (int i = 0; i < 5; i++) asm volatile("" : "+v"(d.xs[i]));
  float s = 0;
#pragma unroll
  for (int i = 0; i < 5; i++) s += (blo(d.xs[i][0]) + bhi(d.xs[i][0])) + (blo(d.xs[i][1]) + bhi(d.xs[i][1]));
#pragma unroll
  for (int o = 1; o < 32; o <<= 1) s += __shfl_xor(s, o);
  const float mu = s * (1.f / 640.f);
  float q = 0;
#pragma unroll
  for (int i = 0; i < 5; i++) {
    const float a = blo(d.xs[i][0]) - mu, c = bhi(d.xs[i][0]) - mu, e = blo(d.xs[i][1]) - mu, g = bhi(d.xs[i][1]) - mu;
    q += a * a + c * c + e * e + g * g;
  }
#pragma unroll
  for (int o = 1; o < 32; o <<= 1) q += __shfl_xor(q, o);
  const float rs = rsqrtf(q * (1.f / 640.f) + 1e-5f);
  if ((lane & 31) == 0) bst64(u2{__float_as_uint(mu), __float_as_uint(rs)}, rws, (b * 8 + lw * 2 + (lane >> 5)) * 8, WS_STATS);
  if (hascs) {
    const int task = lw * 256 + b, kp = task / 78, cu = task - kp * 78;
#pragma unroll
    for (int i = 0; i < 10; i++) asm volatile("" : "+v"(d.cw[i]));
    float c[8];
#pragma unroll
    for (int j = 0; j < 8; j++) c[j] = 0;
#pragma unroll
    for (int i = 0; i < 10; i++)
#pragma unroll
      for (int j = 0; j < 4; j++) { c[2 * j] += blo(d.cw[i][j]); c[2 * j + 1] += bhi(d.cw[i][j]); }
    float e[4];
#pragma unroll
    for (int j = 0; j < 4; j++) {
      const float keep = lane < 32 ? c[j] : c[4 + j], give = lane < 32 ? c[4 + j] : c[j];
      e[j] = keep + __shfl_xor(give, 32);
      e[j] += __shfl_xor(e[j], 16);
      e[j] += __shfl_xor(e[j], 8);
    }
    if ((lane & 24) == 0)
      bst128(u4{__float_as_uint(e[0]), __float_as_uint(e[1]), __float_as_uint(e[2]), __float_as_uint(e[3])}, rws,
             (kp * NCOL + cu * 64 + (lane & 7) * 8 + (lane >> 5) * 4) * 4, WS_CS);
  }
}

template <int TYPE>
DI void k1_main(LDSP char* sm, int w, int lane, f16v (&acc)[10]) {
  constexpr int NXF = TYPE == 0 ? 5 : 2, NWF = 7 - NXF;
  constexpr int XB = TYPE == 0 ? 160 * 128 : 256 * 128;
  const int r = lane & 31, hh = lane >> 5, blk = (lane >> 4) & 1, q = (lane & 15) >> 2, pp = lane & 3;
  unsigned xa[4];
#pragma unroll
  for (int k = 0; k < 4; k++) xa[k] = (unsigned)(((TYPE == 0 ? 0 : w * 64) + r) * 128 + (((2 * k + hh) ^ ((r >> 1) & 7)) << 4));
  unsigned wa[2];
  if (TYPE == 0) {
#pragma unroll
    for (int ft = 0; ft < 2; ft++)
      wa[ft] = (unsigned)(XB + blk * 16384 + (8 * hh + q) * 256 + (((w * 4 + ft * 2 + (pp >> 1)) ^ (q << 2) ^ (blk << 1)) << 4) + (pp & 1) * 8);
  } else {
    wa[0] = (unsigned)(XB + (8 * hh + q) * 320 + blk * 32 + pp * 8); wa[1] = 0;
  }
  u4 xf[2][NXF], wf[2][NWF];
  auto rd = [&](int buf, LDSP char* base, int k) {
#pragma unroll
    for (int i = 0; i < NWF; i++) {
      u2 lo, hi;
      if (TYPE == 0) { lo = lds_tr(base + wa[i] + k * 4096); hi = lds_tr(base + wa[i] + k * 4096 + 1024); }
      else { lo = lds_tr(base + wa[0] + k * 5120 + i * 64); hi = lds_tr(base + wa[0] + k * 5120 + i * 64 + 1280); }
      wf[buf][i] = u4{lo[0], lo[1], hi[0], hi[1]};
    }
#pragma unroll
    for (int i = 0; i < NXF; i++) xf[buf][i] = lds_r128(base + xa[k] + i * 4096);
  };
  auto mm = [&](int buf) {
#pragma unroll
    for (int i = 0; i < NXF; i++)
#pragma unroll
      for (int j = 0; j < NWF; j++) {
        if (TYPE == 2) acc[i * NWF + j] = mfma32(xf[buf][i], wf[buf][j], acc[i * NWF + j]);
        else acc[i * NWF + j] = mfma32(wf[buf][j], xf[buf][i], acc[i * NWF + j]);
      }
  };
  auto inter = [&]() {
    constexpr int NR = NXF + 2 * NWF;
#pragma unroll
    for (int i = 0; i < 6; i++) {
      __builtin_amdgcn_sched_group_barrier(0x8, 1, 0);
      __builtin_amdgcn_sched_group_barrier(0x100, (NR + 5) / 6, 0);
    }
    __builtin_amdgcn_sched_group_barrier(0x8, 4, 0);
  };
  BAR();
  rd(0, sm, 0);
  int slot = 0;
#pragma unroll 1
  for (int s = 0; s < 9; s++) {
    LDSP char* base = sm + slot * SLOT;
    slot = slot == 2 ? 0 : slot + 1;
    SCHED0();
    rd(1, base, 1); mm(0); inter(); SCHED0();
    rd(0, base, 2); mm(1); inter(); SCHED0();
    rd(1, base, 3); mm(0); inter(); SCHED0();
    WAITL0(); BAR(); SCHED0();
    rd(0, sm + slot * SLOT, 0); mm(1); inter(); SCHED0();
  }
  SCHED0();
  rd(1, sm, 1); mm(0); inter(); SCHED0();
  rd(0, sm, 2); mm(1); inter(); SCHED0();
  rd(1, sm, 3); mm(0); inter(); SCHED0();
  mm(1); SCHED0();
}

template <int TYPE>
DI void k1_mfma(const Params& p, LDSP char* sm, rsrc_t rws, int w, int lane, int rt, int cg, int row0) {
  const int hh = lane >> 5, r = lane & 31;
  f16v acc[10];
#pragma unroll
  for (int i = 0; i < 10; i++)
#pragma unroll
    for (int j = 0; j < 16; j++) acc[i][j] = 0.f;
  k1_main<TYPE>(sm, w, lane, acc);
  STAMP(3);
  if (TYPE == 0) {
    f4 cs[2][4];
#pragma unroll
    for (int ft = 0; ft < 2; ft++)
#pragma unroll
      for (int qd = 0; qd < 4; qd++)
        cs[ft][qd] = *(LDSP f4*)(sm + AUX + 2048 + ((qd >> 1) * 128 + w * 32 + ft * 16 + (qd & 1) * 8 + hh * 4) * 4);
    const unsigned dst = (unsigned)(((((row0 + r) >> 3) * 48 + cg * 4 + w) * 4 + hh) * 128 + (r & 7) * 16);
#pragma unroll
    for (int tt = 0; tt < 5; tt++) {
      const float mu = (*(LDSP f2*)(sm + AUX + (tt * 32 + r) * 8))[0];
      if (tt == 0 && rt == 12) continue;
#pragma unroll
      for (int ft = 0; ft < 2; ft++) {
        const f16v a = acc[tt * 2 + ft];
        float h[8];
#pragma unroll
        for (int j = 0; j < 8; j++) h[j] = (a[j] - mu * cs[ft][j >> 2][j & 3]) * (a[8 + j] - mu * cs[ft][2 + (j >> 2)][j & 3]);
        bst128(u4{pk2(h[0], h[1]), pk2(h[2], h[3]), pk2(h[4], h[5]), pk2(h[6], h[7])}, rws, dst + ft * 256, WS_H + tt * 4 * 24576);
      }
    }
    STAMP(4);
    WAITV(0); BAR();
    if (threadIdx.x == 0) st_flag(p.ws, WS_FH + (rt * 16 + cg) * 8, lds_tag(sm));
  } else if (TYPE == 1) {
    u4 pq[2][10];
#pragma unroll
    for (int tt = 0; tt < 2; tt++) {
      const f2 st = *(LDSP f2*)(sm + AUX + (w * 64 + tt * 32 + r) * 8);
      const float rl = st[1] * LOG2E;
#pragma unroll
      for (int ft = 0; ft < 5; ft++) {
        const f16v a = acc[tt * 5 + ft];
        float v[16];
#pragma unroll
        for (int qd = 0; qd < 4; qd++) {
          const f4 cs = *(LDSP f4*)(sm + AUX + 2048 + (ft * 32 + qd * 8 + hh * 4) * 4);
#pragma unroll
          for (int j = 0; j < 4; j++) {
            const float y = a[qd * 4 + j] - st[0] * cs[j];
            v[qd * 4 + j] = fmaxf(fmaf(y, st[1], 1e-6f), 1e-6f) + exp2c(y * rl);
          }
        }
        pq[tt][ft * 2] = u4{pk2(v[0], v[1]), pk2(v[2], v[3]), pk2(v[4], v[5]), pk2(v[6], v[7])};
        pq[tt][ft * 2 + 1] = u4{pk2(v[8], v[9]), pk2(v[10], v[11]), pk2(v[12], v[13]), pk2(v[14], v[15])};
      }
    }
    STAMP(4);
    SCHED0(); BAR(); SCHED0();
    STAMP(5);
    const unsigned dst = (unsigned)((((row0 + w * 64 + r) >> 3) * 20 + cg * 5) * 512 + hh * 128 + (r & 7) * 16);
#pragma unroll
    for (int hg = 0; hg < 2; hg++)
#pragma unroll
      for (int T = 0; T < 5; T++) {
        const int k0 = T < 2 ? 0 : T - 1, k1 = T == 0 ? 1 : (T == 4 ? 4 : T + 1);
        u4 af[3];
#pragma unroll
        for (int k = k0; k <= k1; k++) af[k - k0] = lds_r128(sm + IMG + (hg * 25 + T * 5 + k) * 1024 + lane * 16);
#pragma unroll
        for (int tt = 0; tt < 2; tt++) {
          f16v a;
#pragma unroll
          for (int j = 0; j < 16; j++) a[j] = 0.f;
#pragma unroll
          for (int k = k0; k <= k1; k++) a = mfma32(af[k - k0], pq[tt][hg * 5 + k], a);
          const float d0 = __builtin_amdgcn_rcpf(a[8] + 1e-6f), d1 = __builtin_amdgcn_rcpf(a[12] + 1e-6f);
          bst128(u4{pk2(a[0] * d0, a[1] * d0), pk2(a[2] * d0, a[3] * d0), pk2(a[4] * d1, a[5] * d1), pk2(a[6] * d1, a[7] * d1)},
                 rws, dst + (hg * 5 + T) * 256, WS_O + tt * 4 * 10240);
        }
      }
    STAMP(6);
    WAITV(0); BAR();
    if (threadIdx.x == 0) st_flag(p.ws, WS_FO + (rt * 16 + cg) * 8, lds_tag(sm));
  } else {
    float cs[5];
#pragma unroll
    for (int ct = 0; ct < 5; ct++) cs[ct] = *(LDSP float*)(sm + AUX + 2048 + (ct * 32 + r) * 4);
    const bool isk = r < 16;
    const float kmul = isk ? 1.f : 0.f, kadd = isk ? 1e-6f : 0.f, klo = isk ? 1e-6f : -__builtin_inff();
    u4 fr[4][5];
#pragma unroll
    for (int g = 0; g < 4; g++) {
      float mu[8], rs[8];
#pragma unroll
      for (int hq = 0; hq < 2; hq++) {
        LDSP char* sp = sm + AUX + (w * 64 + g * 16 + hq * 8 + hh * 4) * 8;
        const f4 a = *(LDSP f4*)sp, b = *(LDSP f4*)(sp + 16);
        mu[hq * 4] = a[0]; rs[hq * 4] = a[1]; mu[hq * 4 + 1] = a[2]; rs[hq * 4 + 1] = a[3];
        mu[hq * 4 + 2] = b[0]; rs[hq * 4 + 2] = b[1]; mu[hq * 4 + 3] = b[2]; rs[hq * 4 + 3] = b[3];
      }
#pragma unroll
      for (int ct = 0; ct < 5; ct++) {
        float v[8];
#pragma unroll
        for (int j = 0; j < 8; j++) {
          const float y = acc[(g >> 1) * 5 + ct][(g & 1) * 8 + j] - mu[j] * cs[ct];
          if (ct < 2) v[j] = fmaxf(fmaf(y, rs[j], 1e-6f), 1e-6f) + exp2c(y * (rs[j] * LOG2E));
          else if (ct == 2) v[j] = fmaf(exp2c(y * (rs[j] * LOG2E)), kmul, fmaxf(fmaf(y, rs[j], kadd), klo));
          else v[j] = y * rs[j];
        }
        fr[g][ct] = u4{pk2(v[0], v[1]), pk2(v[2], v[3]), pk2(v[4], v[5]), pk2(v[6], v[7])};
      }
      SCHED0();
    }
    f16v z[8];
#pragma unroll
    for (int i = 0; i < 8; i++)
#pragma unroll
      for (int j = 0; j < 16; j++) z[i][j] = 0.f;
#pragma unroll
    for (int g = 0; g < 4; g++) {
      u4 mb;
#pragma unroll
      for (int j = 0; j < 4; j++) mb[j] = isk ? 0x3f803f80u : fr[g][2][j];
      z[0] = mfma32(fr[g][0], fr[g][3], z[0]);
      z[1] = mfma32(fr[g][0], fr[g][4], z[1]);
      z[2] = mfma32(fr[g][0], mb, z[2]);
      z[3] = mfma32(fr[g][1], fr[g][3], z[3]);
      z[4] = mfma32(fr[g][1], fr[g][4], z[4]);
      z[5] = mfma32(fr[g][1], mb, z[5]);
      z[6] = mfma32(fr[g][2], fr[g][4], z[6]);
      z[7] = mfma32(fr[g][2], mb, z[7]);
    }
    unsigned pa[3], pm[3];
#pragma unroll
    for (int vt = 0; vt < 3; vt++) {
      const int e = vt == 2 ? 48 + r : vt * 32 + r, he = e / 20;
      int ee = e - he * 20; unsigned m = 31u << (5 * he);
      if (vt == 2 && isk) { ee = 20; m = r == 0 ? 0xfffffu : 0u; }
      pa[vt] = (unsigned)(w * 8192 + ee * 16 + hh * 336); pm[vt] = m >> hh;
    }
    SCHED0();
#pragma unroll
    for (int i = 0; i < 8; i++)
#pragma unroll
      for (int qd = 0; qd < 4; qd++) {
        const int kt = i / 3, vt = i < 6 ? i % 3 : i - 5, c = kt * 8 + qd * 2;
        const bool need = (i == 1) ? qd >= 2 : (i == 3) ? qd == 0 : (i >= 6) ? qd < 2 : true;
        if (need && (pm[vt] & (1u << c)))
          *(LDSP f4*)(sm + pa[vt] + c * 336) = f4{z[i][qd * 4], z[i][qd * 4 + 1], z[i][qd * 4 + 2], z[i][qd * 4 + 3]};
      }
    STAMP(4);
    WAITL0(); BAR();
#pragma unroll
    for (int i = 0; i < 2; i++) {
      const int t = threadIdx.x + i * 256;
      if (t < 420) {
        LDSP char* src = sm + t * 16;
        const f4 s = (*(LDSP f4*)src + *(LDSP f4*)(src + 8192)) + (*(LDSP f4*)(src + 16384) + *(LDSP f4*)(src + 24576));
        bst128(__builtin_bit_cast(u4, s), rws, t * 16, WS_KVP + (rt * 8 + cg) * 7168);
      }
    }
    asm volatile("s_waitcnt vmcnt(0) lgkmcnt(0)" ::: "memory"); BAR();
    if (threadIdx.x == 0) st_flag(p.ws, WS_FKV + (cg * 8 + rt) * 8, lds_tag(sm));
  }
}

DI void p2_out(const Params& p, LDSP char* sm, int item, u4 xv, unsigned go) {
  const int row = item >> 3, c = item & 7;
  LDSP char* src = sm + (row >> 3) * 8192 + (row & 7) * 256;
  const unsigned c0 = ((2 * c) ^ (row & 7)) << 4, c1 = c0 ^ 16;
  f4 a = *(LDSP f4*)(src + c0), b = *(LDSP f4*)(src + c1);
#pragma unroll
  for (int i = 1; i < 4; i++) { a += *(LDSP f4*)(src + i * 2048 + c0); b += *(LDSP f4*)(src + i * 2048 + c1); }
  *(GLBP u4*)(p.out + go) = u4{pk2(blo(xv[0]) + a[0], bhi(xv[0]) + a[1]), pk2(blo(xv[1]) + a[2], bhi(xv[1]) + a[3]),
                               pk2(blo(xv[2]) + b[0], bhi(xv[2]) + b[1]), pk2(blo(xv[3]) + b[2], bhi(xv[3]) + b[3])};
}

constexpr int PD = 8;
DI void p2_mfma(const Params& p, LDSP char* sm, rsrc_t rws, int w, int lane, int row2, int cg2) {
  const int hh = lane >> 5, r = lane & 31, blk = (lane >> 4) & 1, q = (lane & 15) >> 2, pp = lane & 3;
  const u64 tag = lds_tag(sm);
  const unsigned fh = WS_FH + (((row2 + ((lane >> 2) & 1) * 95) / 160) * 16 + 3 * w + min(lane & 3, 2)) * 8;
  const unsigned fo = WS_FO + (((row2 + (lane & 1) * 95) >> 8) * 16 + w) * 8;
  float r2[3];
  wait_flag(p.ws, fh, tag, ld_flag(p.ws, fh));
  STAMP(8);
  const unsigned oct = (unsigned)(row2 + r) >> 3, in = hh * 128 + (r & 7) * 16;
  const unsigned ah = oct * 24576 + w * 6144 + in, ao = oct * 10240 + w * 2560 + in;
  u4 af[PD][3], bf[2][2];
  auto lda = [&](int i) {
#pragma unroll
    for (int tt = 0; tt < 3; tt++) {
      if (i < 24) af[i % PD][tt] = bld128<SC1>(rws, ah + (i & 15) * 256, WS_H + tt * 98304 + (i >> 4) * 4096);
      else af[i % PD][tt] = bld128<SC1>(rws, ao + (i - 24) * 256, WS_O + tt * 40960);
    }
  };
  LDSP char* ba[2][3];
#pragma unroll
  for (int ct = 0; ct < 2; ct++)
#pragma unroll
    for (int sg = 0; sg < 3; sg++) {
      ba[ct][sg] = sm + sg * 49152 + w * 2048 + (4 * hh + q) * 128 + (((ct * 4 + blk * 2 + (pp >> 1)) ^ ((q >> 1) << 2)) << 4) + (pp & 1) * 8;
      asm volatile("" : "+v"(ba[ct][sg]));
    }
  auto ldb = [&](int i) {
    const int sl = 17 - (i < 18 ? i : i - 18);
#pragma unroll
    for (int ct = 0; ct < 2; ct++) {
      LDSP char* base = ba[ct][sl / 6] + (sl % 6) * 8192;
      const u2 lo = lds_tr(base), hi = lds_tr(base + 1024);
      bf[i & 1][ct] = u4{lo[0], lo[1], hi[0], hi[1]};
    }
  };
  f16v acc[6];
#pragma unroll
  for (int i = 0; i < 6; i++)
#pragma unroll
    for (int j = 0; j < 16; j++) acc[i][j] = 0.f;
#pragma unroll
  for (int i = 0; i < PD; i++) lda(i);
  ldb(0);
  u64 ofl = 0;
  constexpr int OCHK = 20;
#pragma unroll
  for (int i = 0; i < 34; i++) {
    SCHED0();
    if (i == 7 || i == 15 || i == 23) { WAITL0(); BAR(); SCHED0(); }
    if (i == OCHK) {
      wait_flag(p.ws, fo, tag, ofl);
      STAMP(9);
#pragma unroll
      for (int j = 24; j < OCHK + PD; j++) lda(j);
      SCHED0();
    }
    if (i == 24) {
#pragma unroll
      for (int tt = 0; tt < 3; tt++) { acc[tt * 2] *= r2[tt] * r2[tt]; acc[tt * 2 + 1] *= r2[tt] * r2[tt]; }
      SCHED0();
    }
    if (i + 1 < 34) ldb(i + 1);
#pragma unroll
    for (int tt = 0; tt < 3; tt++)
#pragma unroll
      for (int ct = 0; ct < 2; ct++) acc[tt * 2 + ct] = mfma32(bf[i & 1][ct], af[i % PD][tt], acc[tt * 2 + ct]);
    SCHED0();
    if (i + PD < 24 || (i >= OCHK && i + PD < 34)) lda(i + PD);
    if (i + PD == 24) {
#pragma unroll
      for (int tt = 0; tt < 3; tt++) r2[tt] = __uint_as_float(bld32<SC1>(rws, (row2 + tt * 32 + r) * 8 + 4, WS_STATS));
      ofl = ld_flag(p.ws, fo);
    }
  }
  STAMP(10);
  const int item = 512 + threadIdx.x;
  const unsigned go = (unsigned)((row2 + (item >> 3)) * 1280 + cg2 * 128 + (item & 7) * 16);
  const u4 xv = *(const GLBP u4*)(p.x + go);
#pragma unroll
  for (int tt = 0; tt < 3; tt++)
#pragma unroll
    for (int ct = 0; ct < 2; ct++)
#pragma unroll
      for (int qd = 0; qd < 4; qd++)
        *(LDSP f4*)(sm + (tt * 4 + (r >> 3)) * 8192 + w * 2048 + (r & 7) * 256 + (((ct * 8 + qd * 2 + hh) ^ (r & 7)) << 4)) =
            f4{acc[tt * 2 + ct][qd * 4], acc[tt * 2 + ct][qd * 4 + 1], acc[tt * 2 + ct][qd * 4 + 2], acc[tt * 2 + ct][qd * 4 + 3]};
  WAITL0(); BAR();
  p2_out(p, sm, item, xv, go);
}

DI void loader(const Params& p, LDSP char* sm, rsrc_t rws, int lw, int lane, int type, int rt, int cg, int row0, int row2, int cg2) {
  const int b = blockIdx.x, t = lw * 64 + lane;
  const bool gu = type == 0;
  unsigned vo[13];
  {
    const int nx = gu ? 5 : 8;
#pragma unroll
    for (int i = 0; i < 13; i++) {
      const int rx_ = (lw * nx + i) * 8 + (lane >> 3);
      const unsigned xo = (unsigned)((row0 + rx_) * 1280 + (((lane & 7) ^ ((rx_ >> 1) & 7)) << 4));
      const int rg = ((lw & 1) * 8 + i - 5) * 4 + (lane >> 4);
      const unsigned go = (unsigned)(rg * 3072 + (((lane & 15) ^ ((rg & 3) << 2) ^ ((lw >> 1) << 1)) << 4));
      const int e = (lw * 5 + i - 8) * 64 + lane, rq = e / 20, pos = e - rq * 20;
      const int cb = (type == 1 || pos < 10) ? pos * 16 : (pos < 12 ? 1280 + (pos - 2) * 16 : 1280 + (pos - 12) * 16);
      const unsigned qo = (unsigned)(rq * 3840 + cb);
      vo[i] = i < 5 ? xo : (i < 8 ? (gu ? go : xo) : (gu ? go : qo));
    }
  }
  const char* wbase = gu ? ((lw >> 1) ? p.wu : p.wg) + cg * 256 : p.wqkv + (type == 1 ? cg * 320 : 1280 + cg * 160);
  const unsigned wlen = gu ? 640 * 3072 - cg * 256 : 640 * 3840 - (type == 1 ? cg * 320 : 1280 + cg * 160);
  const unsigned wstep = gu ? 64 * 3072 : 64 * 3840, mstep = gu ? wstep : 128;
  const rsrc_t rx = mkrs(p.x, S * D * 2), rw = mkrs(wbase, wlen), rm = mkrs(gu ? wbase : p.x, gu ? wlen : S * D * 2);
  const int dx = gu ? lw * 5120 : lw * 8192, dm = gu ? 20480 + lw * 8192 : dx + 5120, dw = gu ? dm + 3072 : 32768 + lw * 5120;
  auto issue = [&](int s) {
    LDSP char* d = sm + (s % 3) * SLOT;
#pragma unroll
    for (int i = 0; i < 5; i++) blds16(rx, vo[i], s * 128, d + dx + i * 1024);
#pragma unroll
    for (int i = 0; i < 3; i++) blds16(rm, vo[5 + i], s * mstep, d + dm + i * 1024);
#pragma unroll
    for (int i = 0; i < 5; i++) blds16(rw, vo[8 + i], s * wstep, d + dw + i * 1024);
  };
  const bool hascs = lw * 256 + b < 624;
  P0 d0;
  issue(0); issue(1);
  WAITV(13); BAR();
  issue(2); FENCE(); p0_load(p, d0, lw, lane, hascs);
  if (hascs) WAITV(28); else WAITV(18);
  BAR();
  issue(3);
  if (hascs) WAITV(28); else WAITV(18);
  BAR();
  issue(4); WAITV(13); BAR();
  issue(5); FENCE(); p0_finish(p, d0, rws, lw, lane, hascs);
  WAITV(13); BAR();
  issue(6); WAITV(13); BAR();
  const u64 tag = lds_tag(sm);
  if (t == 0) st_flag(p.ws, WS_F0 + b * 8, tag);
  issue(7); WAITV(13); BAR();
  issue(8); WAITV(13); BAR();
  issue(9); FENCE();
  u64 f0[4];
#pragma unroll
  for (int i = 0; i < 4; i++) f0[i] = ld_flag(p.ws, WS_F0 + (i * 64 + lane) * 8);
  WAITV(17); BAR();
#pragma unroll
  for (int i = 0; i < 4; i++) wait_flag(p.ws, WS_F0 + (i * 64 + lane) * 8, tag, f0[i]);
  const u2 stv = bld64<SC1>(rws, min(row0 + t, S - 1) * 8, WS_STATS);
  float csv[8];
  {
    const int tc = gu ? t : min(t, 159);
    const int col = gu ? 1920 + (tc >> 7) * 1536 + cg * 128 + (tc & 127)
                       : (type == 1 ? cg * 160 + tc : (tc < 80 ? 640 + cg * 80 + tc : (tc < 96 ? 1280 + cg * 80 + tc - 16 : 1280 + cg * 80 + tc - 96)));
#pragma unroll
    for (int i = 0; i < 8; i++) csv[i] = __uint_as_float(bld32<SC1>(rws, col * 4, WS_CS + i * NCOL * 4));
  }
  const rsrc_t rd_ = mkrs(p.wd, F * D * 2), ro_ = mkrs(p.wo, D * D * 2);
  const unsigned bo = (unsigned)((lane >> 3) * 1280 + cg2 * 128 + (((lane & 7) ^ (((lane >> 4) & 1) << 2)) << 4));
  auto bdma = [&](rsrc_t rs, int i) {
#pragma unroll
    for (int h = 0; h < 2; h++) blds16(rs, bo, (lw * 384 + i * 16 + h * 8) * 1280, sm + (17 - i) * 8192 + lw * 2048 + h * 1024);
  };
  FENCE();
  {
    const rsrc_t rz = mkrs(p.wd, type == 1 ? 0 : F * D * 2);
#pragma unroll
    for (int i = 5; i < 11; i++) bdma(rz, i);
  }
  FENCE();
  *(LDSP u2*)(sm + IMG + t * 8) = u2{0, 0};
  *(LDSP u2*)(sm + AUX + t * 8) = stv;
  *(LDSP float*)(sm + AUX + 2048 + t * 4) = ((csv[0] + csv[1]) + (csv[2] + csv[3])) + ((csv[4] + csv[5]) + (csv[6] + csv[7]));
  if (type == 1) WAITV(0);
  WAITV(12); WAITL0(); BAR();
  STAMP(3);
  if (type == 1) {
    const unsigned fk = WS_FKV + (cg * 16 + (lane & 15)) * 8;
    wait_flag(p.ws, fk, tag, ld_flag(p.ws, fk));
    STAMP(4);
    u4 pv[4][8];
#pragma unroll
    for (int m = 0; m < 4; m++) {
      const int g = min(t + m * 256, 839), hg = g >= 420, off = hg * 7168 + (g - hg * 420) * 16;
#pragma unroll
      for (int i = 0; i < 8; i++) pv[m][i] = bld128<SC1>(rws, off, WS_KVP + (i * 8 + cg * 2) * 7168);
    }
#pragma unroll
    for (int m = 0; m < 4; m++) {
      const int g = min(t + m * 256, 839), hg = g >= 420, idx = g - hg * 420, dq = idx / 21, ee = idx - dq * 21, hl = dq / 5;
      f4 s = __builtin_bit_cast(f4, pv[m][0]);
#pragma unroll
      for (int i = 1; i < 8; i++) s += __builtin_bit_cast(f4, pv[m][i]);
      const u2 v = u2{pk2(s[0], s[1]), pk2(s[2], s[3])};
      LDSP char* dst = sm + IMG + hg * 25600 + (dq >> 2) * 1024 + (dq & 1) * 512 + ((dq >> 1) & 1) * 8;
      if (ee < 20) {
        const int eg = hl * 20 + ee;
        *(LDSP u2*)(dst + (eg >> 4) * 5120 + (eg & 15) * 16) = v;
      } else {
#pragma unroll
        for (int k = 0; k < 5; k++) {
          const int eg = hl * 20 + k * 4;
          *(LDSP u2*)(dst + (eg >> 4) * 5120 + (eg & 15) * 16 + 256) = v;
        }
      }
    }
    WAITL0(); BAR();
    BAR();
    return;
  }
#pragma unroll
  for (int i = 0; i < 5; i++) bdma(rd_, i);
  FENCE();
  u4 br[32];
#pragma unroll
  for (int i = 0; i < 16; i++) {
    const int st = 18 + (i >> 1);
    br[i] = st < 24 ? bld128<0>(rd_, bo, (lw * 384 + st * 16 + (i & 1) * 8) * 1280) : bld128<0>(ro_, bo, (lw * 160 + (st - 24) * 16 + (i & 1) * 8) * 1280);
  }
  WAITV(16);
  if (type == 2) BAR();
  BAR();
#pragma unroll
  for (int i = 11; i < 18; i++) bdma(rd_, i);
  FENCE();
#pragma unroll
  for (int i = 16; i < 32; i++) br[i] = bld128<0>(ro_, bo, (lw * 160 + ((i >> 1) - 6) * 16 + (i & 1) * 8) * 1280);
  WAITV(16); BAR();
#pragma unroll
  for (int i = 0; i < 16; i++) *(LDSP u4*)(sm + (17 - (i >> 1)) * 8192 + lw * 2048 + (i & 1) * 1024 + lane * 16) = br[i];
  WAITL0(); BAR();
#pragma unroll
  for (int i = 16; i < 32; i++) *(LDSP u4*)(sm + (17 - (i >> 1)) * 8192 + lw * 2048 + (i & 1) * 1024 + lane * 16) = br[i];
  WAITL0(); BAR();
  unsigned go[2]; u4 xv[2];
#pragma unroll
  for (int i = 0; i < 2; i++) {
    const int item = i * 256 + t;
    go[i] = (unsigned)((row2 + (item >> 3)) * 1280 + cg2 * 128 + (item & 7) * 16);
    xv[i] = *(const GLBP u4*)(p.x + go[i]);
  }
  BAR();
#pragma unroll
  for (int i = 0; i < 2; i++) p2_out(p, sm, i * 256 + t, xv[i], go[i]);
}

__global__ __launch_bounds__(512, 2) void fused_kernel(Params p) {
  extern __shared__ __attribute__((aligned(16))) char smem[];
  LDSP char* sm = (LDSP char*)smem;
  const int lane = threadIdx.x & 63, w = __builtin_amdgcn_readfirstlane(threadIdx.x >> 6), b = blockIdx.x;
  STAMPR(0); STAMP(1);
  int type, rt, cg, n2;
  {
    const int c = b & 7, j = b >> 3, a = c & 1, q = c >> 1;
    if (j < 12) {
      rt = 4 * a + j / 3; const int m = j % 3;
      if (m == 0) { type = 1; cg = q; } else { type = 2; cg = 2 * q + m - 1; }
      n2 = (j / 3) * 2 + m - 1;
    } else {
      const int n = j - 12 + (a ? 19 : 0);
      type = (!a && j == 31) ? 3 : 0; rt = n / 3; cg = 3 * q + n % 3;
      n2 = j - 4;
    }
    n2 = max(n2 + q * 55 + a * 27, 0);
  }
  const int row0 = type == 0 ? min(rt * 160, S - 160) : rt * 256;
  const int rt2 = n2 / 10, cg2 = n2 - rt2 * 10, row2 = min(rt2 * 96, S - 96);
  const rsrc_t rws = mkrs(p.ws, WS_END);
  if (type == 3) {
    if (w < 4) return;
    const u64 tag = ld_flag(p.ws, WS_GEN) + 1;
    const bool hascs = (w - 4) * 256 + b < 624;
    P0 d0;
    p0_load(p, d0, w - 4, lane, hascs);
    p0_finish(p, d0, rws, w - 4, lane, hascs);
    WAITV(0); BAR();
    if (w != 4) return;
    if (lane == 0) st_flag(p.ws, WS_F0 + b * 8, tag);
    if (b != 248) return;
#pragma unroll 1
    for (int i = 0; i < 4; i++) wait_flag(p.ws, WS_F0 + (i * 64 + lane) * 8, tag, tag - 1);
    if (lane == 0) st_flag(p.ws, WS_GEN, tag);
    return;
  }
  if (w >= 4) {
    loader(p, sm, rws, w - 4, lane, type, rt, cg, row0, row2, cg2);
  } else {
    if (threadIdx.x == 0) {
      const u64 tag = ld_flag(p.ws, WS_GEN) + 1;
      *(LDSP u2*)(sm + AUX + 3072) = u2{(unsigned)tag, (unsigned)(tag >> 32)};
    }
    WAITL0();
    if (type == 0) k1_mfma<0>(p, sm, rws, w, lane, rt, cg, row0);
    else if (type == 1) k1_mfma<1>(p, sm, rws, w, lane, rt, cg, row0);
    else k1_mfma<2>(p, sm, rws, w, lane, rt, cg, row0);
    STAMP(7);
    if (type != 1) p2_mfma(p, sm, rws, w, lane, row2, cg2);
  }
  STAMP(11); STAMPR(2);
}

extern "C" void kernel_launch(void* const* d_in, const int* in_sizes, int n_in, void* d_out, int out_size,
                              void* d_ws, size_t ws_size, hipStream_t stream) {
  Params p;
  p.x = (const char*)d_in[0]; p.wqkv = (const char*)d_in[1]; p.wo = (const char*)d_in[2];
  p.wg = (const char*)d_in[4]; p.wu = (const char*)d_in[5]; p.wd = (const char*)d_in[6];
  p.out = (char*)d_out; p.ws = (char*)d_ws;
  (void)hipFuncSetAttribute((const void*)fused_kernel, hipFuncAttributeMaxDynamicSharedMemorySize, LDS_BYTES);
  fused_kernel<<<256, 512, LDS_BYTES, stream>>>(p);
}
```

```cpp
#include <hip/hip_runtime.h>
#include <hip/hip_bf16.h>
#include <stdint.h>

typedef __attribute__((ext_vector_type(8))) __bf16 bf8;
typedef __attribute__((ext_vector_type(2))) __bf16 bf2;
typedef __attribute__((ext_vector_type(4))) short s4;
typedef __attribute__((ext_vector_type(2))) float f2;
typedef __attribute__((ext_vector_type(4))) float f4;
typedef __attribute__((ext_vector_type(16))) float f16v;
typedef __attribute__((ext_vector_type(2))) unsigned u2;
typedef __attribute__((ext_vector_type(4))) unsigned u4;
typedef unsigned long long u64;
typedef __amdgpu_buffer_rsrc_t rsrc_t;
#define LDSP __attribute__((address_space(3)))
#define GLBP __attribute__((address_space(1)))
#define DI __device__ __forceinline__

constexpr int S = 2048, D = 640, F = 1536;
constexpr int NCOL = 4992;
constexpr unsigned WS_GEN = 0;
constexpr unsigned WS_F0 = 4096;
constexpr unsigned WS_FH = 8192;
constexpr unsigned WS_FKV = 12288;
constexpr unsigned WS_FO = 16384;
constexpr unsigned WS_STATS = 32768;
constexpr unsigned WS_CS = 65536;
constexpr unsigned WS_KVP = 262144;
constexpr unsigned WS_H = 1 << 20;
constexpr unsigned WS_O = WS_H + S * F * 2;
constexpr unsigned WS_END = WS_O + S * D * 2;

constexpr int SLOT = 52 * 1024;
constexpr int LDS_BYTES = 160 * 1024;
constexpr int AUX = 3 * SLOT;
constexpr int IMG = 54 * 1024;
constexpr int SC1 = 16;

DI rsrc_t mkrs(const void* b, unsigned n) { return __builtin_amdgcn_make_buffer_rsrc((void*)b, (short)0, (int)n, 0x00020000); }
DI void blds16(rsrc_t rs, unsigned voff, unsigned soff, LDSP char* l) {
  __builtin_amdgcn_raw_ptr_buffer_load_lds(rs, (LDSP void*)l, 16, voff, soff, 0, 0);
}
template <int AUXB> DI u4 bld128(rsrc_t rs, unsigned voff, unsigned soff) { return __builtin_amdgcn_raw_buffer_load_b128(rs, voff, soff, AUXB); }
template <int AUXB> DI u2 bld64(rsrc_t rs, unsigned voff, unsigned soff) { return __builtin_amdgcn_raw_buffer_load_b64(rs, voff, soff, AUXB); }
template <int AUXB> DI unsigned bld32(rsrc_t rs, unsigned voff, unsigned soff) { return __builtin_amdgcn_raw_buffer_load_b32(rs, voff, soff, AUXB); }
DI void bst128(u4 v, rsrc_t rs, unsigned voff, unsigned soff) { __builtin_amdgcn_raw_buffer_store_b128(v, rs, voff, soff, SC1); }
DI void bst64(u2 v, rsrc_t rs, unsigned voff, unsigned soff) { __builtin_amdgcn_raw_buffer_store_b64(v, rs, voff, soff, SC1); }
DI u4 lds_r128(LDSP char* p) { return *(LDSP u4*)p; }
DI u2 lds_tr(LDSP char* p) { return __builtin_bit_cast(u2, __builtin_amdgcn_ds_read_tr16_b64_v4i16((LDSP s4*)p)); }
DI f16v mfma32(u4 a, u4 b, f16v c) { return __builtin_amdgcn_mfma_f32_32x32x16_bf16(__builtin_bit_cast(bf8, a), __builtin_bit_cast(bf8, b), c, 0, 0, 0); }
DI unsigned pk2(float lo, float hi) { f2 v = {lo, hi}; return __builtin_bit_cast(unsigned, __builtin_convertvector(v, bf2)); }
DI float blo(unsigned v) { return __uint_as_float(v << 16); }
DI float bhi(unsigned v) { return __uint_as_float(v & 0xffff0000u); }
DI float exp2c(float u) { return __builtin_amdgcn_fmed3f(__builtin_amdgcn_exp2f(u), 0.f, 1.f); }
constexpr float LOG2E = 1.44269504088896f, LN2 = 0.693147180559945f;
DI unsigned phi2(f2 u) {
  const f2 t = u * LN2 + 1e-6f, e = {exp2c(u[0]), exp2c(u[1])};
  const f2 v = f2{fmaxf(t[0], 1e-6f), fmaxf(t[1], 1e-6f)} + e;
  return pk2(v[0], v[1]);
}
DI u64 ld_flag(const char* ws, unsigned off) { return __hip_atomic_load((const GLBP u64*)(ws + off), __ATOMIC_RELAXED, __HIP_MEMORY_SCOPE_AGENT); }
DI void st_flag(char* ws, unsigned off, u64 v) { __hip_atomic_store((GLBP u64*)(ws + off), v, __ATOMIC_RELAXED, __HIP_MEMORY_SCOPE_AGENT); }
#define BAR() __builtin_amdgcn_s_barrier()
#define WAITV(n) asm volatile("s_waitcnt vmcnt(" #n ")" ::: "memory")
#define WAITL0() asm volatile("s_waitcnt lgkmcnt(0)" ::: "memory")
#define SCHED0() __builtin_amdgcn_sched_barrier(0)
#define FENCE() do { asm volatile("" ::: "memory"); __builtin_amdgcn_sched_barrier(0); } while (0)

#define STAMP(i)
#define STAMPR(i)

struct Params {
  const char* x; const char* wqkv; const char* wo; const char* wg; const char* wu; const char* wd;
  char* out; char* ws;
};

DI void wait_flag(const char* ws, unsigned off, u64 tag, u64 first) {
  u64 v = first;
#pragma unroll 1
  for (int it = 0; it < (1 << 18); it++) {
    if (__all(v == tag)) break;
    __builtin_amdgcn_s_sleep(1);
    v = ld_flag(ws, off);
  }
  FENCE();
}
DI u64 lds_tag(LDSP char* sm) {
  const u2 t = *(LDSP u2*)(sm + AUX + 3072);
  return (u64)(unsigned)__builtin_amdgcn_readfirstlane(t[0]) | ((u64)(unsigned)__builtin_amdgcn_readfirstlane(t[1]) << 32);
}

struct P0 { u2 xs[5]; u4 cw[10]; };
DI void p0_load(const Params& p, P0& d, int lw, int lane, bool hascs) {
  const int b = blockIdx.x;
  const rsrc_t rx = mkrs(p.x, S * D * 2);
  const unsigned xo = (unsigned)((b * 8 + lw * 2 + (lane >> 5)) * 1280 + (lane & 31) * 8);
#pragma unroll
  for (int i = 0; i < 5; i++) d.xs[i] = bld64<0>(rx, xo + i * 256, 0);
  if (hascs) {
    const int task = lw * 256 + b, kp = task / 78, cu = task - kp * 78;
    const char* base = cu < 30 ? p.wqkv + cu * 128 : (cu < 54 ? p.wg + (cu - 30) * 128 : p.wu + (cu - 54) * 128);
    const unsigned stride = cu < 30 ? 3840 : 3072;
    const rsrc_t rw = mkrs(base, 640 * stride - 127);
    const unsigned wo = (unsigned)(kp * 80 + (lane >> 3)) * stride + (lane & 7) * 16;
#pragma unroll
    for (int i = 0; i < 10; i++) d.cw[i] = bld128<0>(rw, wo, i * 8 * stride);
  }
}
DI void p0_finish(const Params& p, P0& d, rsrc_t rws, int lw, int lane, bool hascs) {
  const int b = blockIdx.x;
#pragma unroll
  for (int i = 0; i < 5; i++) asm volatile("" : "+v"(d.xs[i]));
  float s = 0;
#pragma unroll
  for (int i = 0; i < 5; i++) s += (blo(d.xs[i][0]) + bhi(d.xs[i][0])) + (blo(d.xs[i][1]) + bhi(d.xs[i][1]));
#pragma unroll
  for (int o = 1; o < 32; o <<= 1) s += __shfl_xor(s, o);
  const float mu = s * (1.f / 640.f);
  float q = 0;
#pragma unroll
  for (int i = 0; i < 5; i++) {
    const float a = blo(d.xs[i][0]) - mu, c = bhi(d.xs[i][0]) - mu, e = blo(d.xs[i][1]) - mu, g = bhi(d.xs[i][1]) - mu;
    q += a * a + c * c + e * e + g * g;
  }
#pragma unroll
  for (int o = 1; o < 32; o <<= 1) q += __shfl_xor(q, o);
  const float rs = rsqrtf(q * (1.f / 640.f) + 1e-5f);
  if ((lane & 31) == 0) bst64(u2{__float_as_uint(mu), __float_as_uint(rs)}, rws, (b * 8 + lw * 2 + (lane >> 5)) * 8, WS_STATS);
  if (hascs) {
    const int task = lw * 256 + b, kp = task / 78, cu = task - kp * 78;
#pragma unroll
    for (int i = 0; i < 10; i++) asm volatile("" : "+v"(d.cw[i]));
    float c[8];
#pragma unroll
    for (int j = 0; j < 8; j++) c[j] = 0;
#pragma unroll
    for (int i = 0; i < 10; i++)
#pragma unroll
      for (int j = 0; j < 4; j++) { c[2 * j] += blo(d.cw[i][j]); c[2 * j + 1] += bhi(d.cw[i][j]); }
    float e[4];
#pragma unroll
    for (int j = 0; j < 4; j++) {
      const float keep = lane < 32 ? c[j] : c[4 + j], give = lane < 32 ? c[4 + j] : c[j];
      e[j] = keep + __shfl_xor(give, 32);
      e[j] += __shfl_xor(e[j], 16);
      e[j] += __shfl_xor(e[j], 8);
    }
    if ((lane & 24) == 0)
      bst128(u4{__float_as_uint(e[0]), __float_as_uint(e[1]), __float_as_uint(e[2]), __float_as_uint(e[3])}, rws,
             (kp * NCOL + cu * 64 + (lane & 7) * 8 + (lane >> 5) * 4) * 4, WS_CS);
  }
}

template <int TYPE>
DI void k1_main(LDSP char* sm, int w, int lane, f16v (&acc)[10]) {
  constexpr int NXF = TYPE == 0 ? 5 : 2, NWF = 7 - NXF;
  constexpr int XB = TYPE == 0 ? 160 * 128 : 256 * 128;
  const int r = lane & 31, hh = lane >> 5, blk = (lane >> 4) & 1, q = (lane & 15) >> 2, pp = lane & 3;
  unsigned xa[4];
#pragma unroll
  for (int k = 0; k < 4; k++) xa[k] = (unsigned)(((TYPE == 0 ? 0 : w * 64) + r) * 128 + (((2 * k + hh) ^ ((r >> 1) & 7)) << 4));
  unsigned wa[2];
  if (TYPE == 0) {
#pragma unroll
    for (int ft = 0; ft < 2; ft++)
      wa[ft] = (unsigned)(XB + blk * 16384 + (8 * hh + q) * 256 + (((w * 4 + ft * 2 + (pp >> 1)) ^ (q << 2) ^ (blk << 1)) << 4) + (pp & 1) * 8);
  } else {
    wa[0] = (unsigned)(XB + (8 * hh + q) * 320 + blk * 32 + pp * 8); wa[1] = 0;
  }
  u4 xf[2][NXF], wf[2][NWF];
  auto rd = [&](int buf, LDSP char* base, int k) {
#pragma unroll
    for (int i = 0; i < NWF; i++) {
      u2 lo, hi;
      if (TYPE == 0) { lo = lds_tr(base + wa[i] + k * 4096); hi = lds_tr(base + wa[i] + k * 4096 + 1024); }
      else { lo = lds_tr(base + wa[0] + k * 5120 + i * 64); hi = lds_tr(base + wa[0] + k * 5120 + i * 64 + 1280); }
      wf[buf][i] = u4{lo[0], lo[1], hi[0], hi[1]};
    }
#pragma unroll
    for (int i = 0; i < NXF; i++) xf[buf][i] = lds_r128(base + xa[k] + i * 4096);
  };
  auto mm = [&](int buf) {
#pragma unroll
    for (int i = 0; i < NXF; i++)
#pragma unroll
      for (int j = 0; j < NWF; j++) {
        if (TYPE == 2) acc[i * NWF + j] = mfma32(xf[buf][i], wf[buf][j], acc[i * NWF + j]);
        else acc[i * NWF + j] = mfma32(wf[buf][j], xf[buf][i], acc[i * NWF + j]);
      }
  };
  auto inter = [&]() {
    constexpr int NR = NXF + 2 * NWF;
#pragma unroll
    for (int i = 0; i < 6; i++) {
      __builtin_amdgcn_sched_group_barrier(0x8, 1, 0);
      __builtin_amdgcn_sched_group_barrier(0x100, (NR + 5) / 6, 0);
    }
    __builtin_amdgcn_sched_group_barrier(0x8, 4, 0);
  };
  BAR();
  rd(0, sm, 0);
  int slot = 0;
#pragma unroll 1
  for (int s = 0; s < 9; s++) {
    LDSP char* base = sm + slot * SLOT;
    slot = slot == 2 ? 0 : slot + 1;
    SCHED0();
    rd(1, base, 1); mm(0); inter(); SCHED0();
    rd(0, base, 2); mm(1); inter(); SCHED0();
    rd(1, base, 3); mm(0); inter(); SCHED0();
    WAITL0(); BAR(); SCHED0();
    rd(0, sm + slot * SLOT, 0); mm(1); inter(); SCHED0();
  }
  SCHED0();
  rd(1, sm, 1); mm(0); inter(); SCHED0();
  rd(0, sm, 2); mm(1); inter(); SCHED0();
  rd(1, sm, 3); mm(0); inter(); SCHED0();
  mm(1); SCHED0();
}

template <int TYPE>
DI void k1_mfma(const Params& p, LDSP char* sm, rsrc_t rws, int w, int lane, int rt, int cg, int row0) {
  const int hh = lane >> 5, r = lane & 31;
  f16v acc[10];
#pragma unroll
  for (int i = 0; i < 10; i++)
#pragma unroll
    for (int j = 0; j < 16; j++) acc[i][j] = 0.f;
  k1_main<TYPE>(sm, w, lane, acc);
  STAMP(3);
  if (TYPE == 0) {
    f4 cs[2][4];
#pragma unroll
    for (int ft = 0; ft < 2; ft++)
#pragma unroll
      for (int qd = 0; qd < 4; qd++)
        cs[ft][qd] = *(LDSP f4*)(sm + AUX + 2048 + ((qd >> 1) * 128 + w * 32 + ft * 16 + (qd & 1) * 8 + hh * 4) * 4);
    const unsigned dst = (unsigned)(((((row0 + r) >> 3) * 48 + cg * 4 + w) * 4 + hh) * 128 + (r & 7) * 16);
#pragma unroll
    for (int tt = 0; tt < 5; tt++) {
      const float mu = *(LDSP float*)(sm + AUX + (tt * 32 + r) * 4);
      if (tt == 0 && rt == 12) continue;
#pragma unroll
      for (int ft = 0; ft < 2; ft++) {
        const f16v a = acc[tt * 2 + ft];
        float h[8];
#pragma unroll
        for (int j = 0; j < 8; j++) h[j] = (a[j] - mu * cs[ft][j >> 2][j & 3]) * (a[8 + j] - mu * cs[ft][2 + (j >> 2)][j & 3]);
        bst128(u4{pk2(h[0], h[1]), pk2(h[2], h[3]), pk2(h[4], h[5]), pk2(h[6], h[7])}, rws, dst + ft * 256, WS_H + tt * 4 * 24576);
      }
    }
    STAMP(4);
    WAITV(0); BAR();
    if (threadIdx.x == 0) st_flag(p.ws, WS_FH + (rt * 16 + cg) * 8, lds_tag(sm));
  } else if (TYPE == 1) {
    u4 pq[2][10];
#pragma unroll
    for (int tt = 0; tt < 2; tt++) {
      const float mu = *(LDSP float*)(sm + AUX + (w * 64 + tt * 32 + r) * 4);
      const float rl = *(LDSP float*)(sm + AUX + 1024 + (w * 64 + tt * 32 + r) * 4) * LOG2E;
#pragma unroll
      for (int ft = 0; ft < 5; ft++) {
        const f16v a = acc[tt * 5 + ft];
#pragma unroll
        for (int qd = 0; qd < 4; qd++) {
          const f4 cs = *(LDSP f4*)(sm + AUX + 2048 + (ft * 32 + qd * 8 + hh * 4) * 4);
#pragma unroll
          for (int j = 0; j < 2; j++) {
            const f2 u = (f2{a[qd * 4 + 2 * j], a[qd * 4 + 2 * j + 1]} - f2{cs[2 * j], cs[2 * j + 1]} * mu) * rl;
            pq[tt][ft * 2 + (qd >> 1)][(qd & 1) * 2 + j] = phi2(u);
          }
        }
        asm volatile("" : "+v"(pq[tt][ft * 2]), "+v"(pq[tt][ft * 2 + 1]));
      }
    }
    STAMP(4);
    SCHED0(); BAR(); SCHED0();
    STAMP(5);
    const unsigned dst = (unsigned)((((row0 + w * 64 + r) >> 3) * 20 + cg * 5) * 512 + hh * 128 + (r & 7) * 16);
#pragma unroll
    for (int hg = 0; hg < 2; hg++)
#pragma unroll
      for (int T = 0; T < 5; T++) {
        const int k0 = T < 2 ? 0 : T - 1, k1 = T == 0 ? 1 : (T == 4 ? 4 : T + 1);
        u4 af[3];
#pragma unroll
        for (int k = k0; k <= k1; k++) af[k - k0] = lds_r128(sm + IMG + (hg * 25 + T * 5 + k) * 1024 + lane * 16);
#pragma unroll
        for (int tt = 0; tt < 2; tt++) {
          f16v a;
#pragma unroll
          for (int j = 0; j < 16; j++) a[j] = 0.f;
#pragma unroll
          for (int k = k0; k <= k1; k++) a = mfma32(af[k - k0], pq[tt][hg * 5 + k], a);
          const float d0 = __builtin_amdgcn_rcpf(a[8] + 1e-6f), d1 = __builtin_amdgcn_rcpf(a[12] + 1e-6f);
          bst128(u4{pk2(a[0] * d0, a[1] * d0), pk2(a[2] * d0, a[3] * d0), pk2(a[4] * d1, a[5] * d1), pk2(a[6] * d1, a[7] * d1)},
                 rws, dst + (hg * 5 + T) * 256, WS_O + tt * 4 * 10240);
        }
      }
    STAMP(6);
    WAITV(0); BAR();
    if (threadIdx.x == 0) st_flag(p.ws, WS_FO + (rt * 16 + cg) * 8, lds_tag(sm));
  } else {
    float cs[5];
#pragma unroll
    for (int ct = 0; ct < 5; ct++) cs[ct] = *(LDSP float*)(sm + AUX + 2048 + (ct * 32 + r) * 4);
    const bool isk = r < 16;
    const float kmul = isk ? 1.f : 0.f, kadd = isk ? 1e-6f : 0.f, klo = isk ? 1e-6f : -__builtin_inff();
    u4 fr[4][5];
#pragma unroll
    for (int g = 0; g < 4; g++) {
      f2 mu[4], rs[4], rl[4];
#pragma unroll
      for (int hq = 0; hq < 2; hq++) {
        LDSP char* sp = sm + AUX + (w * 64 + g * 16 + hq * 8 + hh * 4) * 4;
        const f4 a = *(LDSP f4*)sp, b = *(LDSP f4*)(sp + 1024);
        mu[hq * 2] = f2{a[0], a[1]}; mu[hq * 2 + 1] = f2{a[2], a[3]};
        rs[hq * 2] = f2{b[0], b[1]}; rs[hq * 2 + 1] = f2{b[2], b[3]};
      }
#pragma unroll
      for (int j = 0; j < 4; j++) rl[j] = rs[j] * LOG2E;
#pragma unroll
      for (int ct = 0; ct < 5; ct++) {
#pragma unroll
        for (int j = 0; j < 4; j++) {
          const f16v& a = acc[(g >> 1) * 5 + ct];
          const f2 y = f2{a[(g & 1) * 8 + 2 * j], a[(g & 1) * 8 + 2 * j + 1]} - mu[j] * cs[ct];
          if (ct < 2) fr[g][ct][j] = phi2(y * rl[j]);
          else if (ct == 2) {
            const f2 u = y * rl[j], t = y * rs[j] + kadd, e = {exp2c(u[0]), exp2c(u[1])};
            const f2 v = e * kmul + f2{fmaxf(t[0], klo), fmaxf(t[1], klo)};
            fr[g][ct][j] = pk2(v[0], v[1]);
          } else { const f2 v = y * rs[j]; fr[g][ct][j] = pk2(v[0], v[1]); }
        }
      }
      SCHED0();
    }
    f16v z[8];
#pragma unroll
    for (int i = 0; i < 8; i++)
#pragma unroll
      for (int j = 0; j < 16; j++) z[i][j] = 0.f;
#pragma unroll
    for (int g = 0; g < 4; g++) {
      u4 mb;
#pragma unroll
      for (int j = 0; j < 4; j++) mb[j] = isk ? 0x3f803f80u : fr[g][2][j];
      z[0] = mfma32(fr[g][0], fr[g][3], z[0]);
      z[1] = mfma32(fr[g][0], fr[g][4], z[1]);
      z[2] = mfma32(fr[g][0], mb, z[2]);
      z[3] = mfma32(fr[g][1], fr[g][3], z[3]);
      z[4] = mfma32(fr[g][1], fr[g][4], z[4]);
      z[5] = mfma32(fr[g][1], mb, z[5]);
      z[6] = mfma32(fr[g][2], fr[g][4], z[6]);
      z[7] = mfma32(fr[g][2], mb, z[7]);
    }
    unsigned pa[3], pm[3];
#pragma unroll
    for (int vt = 0; vt < 3; vt++) {
      const int e = vt == 2 ? 48 + r : vt * 32 + r, he = e / 20;
      int ee = e - he * 20; unsigned m = 31u << (5 * he);
      if (vt == 2 && isk) { ee = 20; m = r == 0 ? 0xfffffu : 0u; }
      pa[vt] = (unsigned)(w * 8192 + ee * 16 + hh * 336); pm[vt] = m >> hh;
    }
    SCHED0();
#pragma unroll
    for (int i = 0; i < 8; i++)
#pragma unroll
      for (int qd = 0; qd < 4; qd++) {
        const int kt = i / 3, vt = i < 6 ? i % 3 : i - 5, c = kt * 8 + qd * 2;
        const bool need = (i == 1) ? qd >= 2 : (i == 3) ? qd == 0 : (i >= 6) ? qd < 2 : true;
        if (need && (pm[vt] & (1u << c)))
          *(LDSP f4*)(sm + pa[vt] + c * 336) = f4{z[i][qd * 4], z[i][qd * 4 + 1], z[i][qd * 4 + 2], z[i][qd * 4 + 3]};
      }
    STAMP(4);
    WAITL0(); BAR();
#pragma unroll
    for (int i = 0; i < 2; i++) {
      const int t = threadIdx.x + i * 256;
      if (t < 420) {
        LDSP char* src = sm + t * 16;
        const f4 s = (*(LDSP f4*)src + *(LDSP f4*)(src + 8192)) + (*(LDSP f4*)(src + 16384) + *(LDSP f4*)(src + 24576));
        bst128(__builtin_bit_cast(u4, s), rws, t * 16, WS_KVP + (rt * 8 + cg) * 7168);
      }
    }
    asm volatile("s_waitcnt vmcnt(0) lgkmcnt(0)" ::: "memory"); BAR();
    if (threadIdx.x == 0) st_flag(p.ws, WS_FKV + (cg * 8 + rt) * 8, lds_tag(sm));
  }
}

DI void p2_out(const Params& p, LDSP char* sm, int item, u4 xv, unsigned go) {
  const int row = item >> 3, c = item & 7;
  LDSP char* src = sm + (row >> 3) * 8192 + (row & 7) * 256;
  const unsigned c0 = ((2 * c) ^ (row & 7)) << 4, c1 = c0 ^ 16;
  f4 a = *(LDSP f4*)(src + c0), b = *(LDSP f4*)(src + c1);
#pragma unroll
  for (int i = 1; i < 4; i++) { a += *(LDSP f4*)(src + i * 2048 + c0); b += *(LDSP f4*)(src + i * 2048 + c1); }
  *(GLBP u4*)(p.out + go) = u4{pk2(blo(xv[0]) + a[0], bhi(xv[0]) + a[1]), pk2(blo(xv[1]) + a[2], bhi(xv[1]) + a[3]),
                               pk2(blo(xv[2]) + b[0], bhi(xv[2]) + b[1]), pk2(blo(xv[3]) + b[2], bhi(xv[3]) + b[3])};
}

constexpr int PD = 8;
DI void p2_mfma(const Params& p, LDSP char* sm, rsrc_t rws, int w, int lane, int row2, int cg2) {
  const int hh = lane >> 5, r = lane & 31, blk = (lane >> 4) & 1, q = (lane & 15) >> 2, pp = lane & 3;
  const u64 tag = lds_tag(sm);
  const unsigned fh = WS_FH + (((row2 + ((lane >> 2) & 1) * 95) / 160) * 16 + 3 * w + min(lane & 3, 2)) * 8;
  const unsigned fo = WS_FO + (((row2 + (lane & 1) * 95) >> 8) * 16 + w) * 8;
  float r2[3];
  wait_flag(p.ws, fh, tag, ld_flag(p.ws, fh));
  STAMP(8);
  const unsigned oct = (unsigned)(row2 + r) >> 3, in = hh * 128 + (r & 7) * 16;
  const unsigned ah = oct * 24576 + w * 6144 + in, ao = oct * 10240 + w * 2560 + in;
  u4 af[PD][3], bf[2][2];
  auto lda = [&](int i) {
#pragma unroll
    for (int tt = 0; tt < 3; tt++) {
      if (i < 24) af[i % PD][tt] = bld128<SC1>(rws, ah + (i & 15) * 256, WS_H + tt * 98304 + (i >> 4) * 4096);
      else af[i % PD][tt] = bld128<SC1>(rws, ao + (i - 24) * 256, WS_O + tt * 40960);
    }
  };
  LDSP char* ba[2][3];
#pragma unroll
  for (int ct = 0; ct < 2; ct++)
#pragma unroll
    for (int sg = 0; sg < 3; sg++) {
      ba[ct][sg] = sm + sg * 49152 + w * 2048 + (4 * hh + q) * 128 + (((ct * 4 + blk * 2 + (pp >> 1)) ^ ((q >> 1) << 2)) << 4) + (pp & 1) * 8;
      asm volatile("" : "+v"(ba[ct][sg]));
    }
  auto ldb = [&](int i) {
    const int sl = 17 - (i < 18 ? i : i - 18);
#pragma unroll
    for (int ct = 0; ct < 2; ct++) {
      LDSP char* base = ba[ct][sl / 6] + (sl % 6) * 8192;
      const u2 lo = lds_tr(base), hi = lds_tr(base + 1024);
      bf[i & 1][ct] = u4{lo[0], lo[1], hi[0], hi[1]};
    }
  };
  f16v acc[6];
#pragma unroll
  for (int i = 0; i < 6; i++)
#pragma unroll
    for (int j = 0; j < 16; j++) acc[i][j] = 0.f;
#pragma unroll
  for (int i = 0; i < PD; i++) lda(i);
  ldb(0);
  u64 ofl = 0;
  constexpr int OCHK = 20;
#pragma unroll
  for (int i = 0; i < 34; i++) {
    SCHED0();
    if (i == 7 || i == 15 || i == 23) { STAMP(12 + i / 4); WAITL0(); BAR(); STAMP(13 + i / 4); SCHED0(); }
    if (i == OCHK) {
      STAMP(19);
      wait_flag(p.ws, fo, tag, ofl);
      STAMP(9);
#pragma unroll
      for (int j = 24; j < OCHK + PD; j++) lda(j);
      SCHED0();
    }
    if (i == 24) {
#pragma unroll
      for (int tt = 0; tt < 3; tt++) { acc[tt * 2] *= r2[tt] * r2[tt]; acc[tt * 2 + 1] *= r2[tt] * r2[tt]; }
      SCHED0();
    }
    if (i + 1 < 34) ldb(i + 1);
#pragma unroll
    for (int tt = 0; tt < 3; tt++)
#pragma unroll
      for (int ct = 0; ct < 2; ct++) acc[tt * 2 + ct] = mfma32(bf[i & 1][ct], af[i % PD][tt], acc[tt * 2 + ct]);
    SCHED0();
    if (i + PD < 24 || (i >= OCHK && i + PD < 34)) lda(i + PD);
    if (i + PD == 24) {
#pragma unroll
      for (int tt = 0; tt < 3; tt++) r2[tt] = __uint_as_float(bld32<SC1>(rws, (row2 + tt * 32 + r) * 8 + 4, WS_STATS));
      ofl = ld_flag(p.ws, fo);
    }
  }
  STAMP(10);
  const int item = 512 + threadIdx.x;
  const unsigned go = (unsigned)((row2 + (item >> 3)) * 1280 + cg2 * 128 + (item & 7) * 16);
  const u4 xv = *(const GLBP u4*)(p.x + go);
#pragma unroll
  for (int tt = 0; tt < 3; tt++)
#pragma unroll
    for (int ct = 0; ct < 2; ct++)
#pragma unroll
      for (int qd = 0; qd < 4; qd++)
        *(LDSP f4*)(sm + (tt * 4 + (r >> 3)) * 8192 + w * 2048 + (r & 7) * 256 + (((ct * 8 + qd * 2 + hh) ^ (r & 7)) << 4)) =
            f4{acc[tt * 2 + ct][qd * 4], acc[tt * 2 + ct][qd * 4 + 1], acc[tt * 2 + ct][qd * 4 + 2], acc[tt * 2 + ct][qd * 4 + 3]};
  WAITL0(); BAR();
  p2_out(p, sm, item, xv, go);
}

DI void loader(const Params& p, LDSP char* sm, rsrc_t rws, int lw, int lane, int type, int rt, int cg, int row0, int row2, int cg2) {
  const int b = blockIdx.x, t = lw * 64 + lane;
  const bool gu = type == 0;
  __builtin_amdgcn_s_setprio(3);
  unsigned vo[13];
  {
    const int x0 = gu ? lw * 5 : lw * 8;
    const unsigned xr = (unsigned)((row0 + x0 * 8 + (lane >> 3)) * 1280), xs = (unsigned)(((lane & 7) ^ (lane >> 4) ^ ((x0 & 1) << 2)) << 4);
#pragma unroll
    for (int i = 0; i < 8; i++) vo[i] = xr + (xs ^ ((i & 1) << 6)) + i * 10240;
    if (gu) {
      const unsigned g0 = (unsigned)(((lw & 1) * 32 + (lane >> 4)) * 3072 + (((lane & 15) ^ ((lane >> 4) << 2) ^ (lw & 2)) << 4));
#pragma unroll
      for (int i = 5; i < 13; i++) vo[i] = g0 + (i - 5) * 12288;
    } else {
#pragma unroll
      for (int i = 0; i < 5; i++) {
        const int e = i * 64 + lane, rq = (e * 205) >> 12, pos = e - rq * 20;
        const int adj = type == 1 ? 0 : (pos < 10 ? 0 : (pos < 12 ? 1248 : 1088));
        vo[8 + i] = (unsigned)((lw * 16 + rq) * 3840 + pos * 16 + adj);
      }
    }
  }
  const unsigned wofs = gu ? cg * 256 : (type == 1 ? cg * 320 : 1280 + cg * 160);
  const char* wbase = (gu ? ((lw >> 1) ? p.wu : p.wg) : p.wqkv) + wofs;
  const unsigned wstep = gu ? 64 * 3072 : 64 * 3840, wlen = wstep * 10 - wofs, mstep = gu ? wstep : 128;
  const rsrc_t rx = mkrs(p.x, S * D * 2), rw = mkrs(wbase, wlen), rm = mkrs(gu ? wbase : p.x, gu ? wlen : S * D * 2);
  const int dx = gu ? lw * 5120 : lw * 8192, dm = gu ? 20480 + lw * 8192 : dx + 5120, dw = gu ? dm + 3072 : 32768 + lw * 5120;
  auto issue = [&](int s) {
    LDSP char* d = sm + (s % 3) * SLOT;
#pragma unroll
    for (int i = 0; i < 5; i++) blds16(rx, vo[i], s * 128, d + dx + i * 1024);
#pragma unroll
    for (int i = 0; i < 3; i++) blds16(rm, vo[5 + i], s * mstep, d + dm + i * 1024);
#pragma unroll
    for (int i = 0; i < 5; i++) blds16(rw, vo[8 + i], s * wstep, d + dw + i * 1024);
    FENCE();
  };
  issue(0); issue(1);
  STAMP(12);
  const bool hascs = lw * 256 + b < 624;
  P0 d0;
  p0_load(p, d0, lw, lane, hascs);
  const rsrc_t rd_ = mkrs(p.wd, type == 1 ? 0 : F * D * 2), ro_ = mkrs(p.wo, type == 1 ? 0 : D * D * 2);
  const unsigned bo = (unsigned)((lane >> 3) * 1280 + cg2 * 128 + (((lane & 7) ^ (((lane >> 4) & 1) << 2)) << 4));
  auto bdma = [&](int i) {
#pragma unroll
    for (int h = 0; h < 2; h++) blds16(rd_, bo, (lw * 384 + i * 16 + h * 8) * 1280, sm + (17 - i) * 8192 + lw * 2048 + h * 1024);
  };
  u4 br[46];
  auto breg = [&](int i0, int i1) {
#pragma unroll
    for (int i = i0; i < i1; i++) {
      const int st = 11 + (i >> 1);
      br[i] = st < 24 ? bld128<0>(rd_, bo, (lw * 384 + st * 16 + (i & 1) * 8) * 1280) : bld128<0>(ro_, bo, (lw * 160 + (st - 24) * 16 + (i & 1) * 8) * 1280);
    }
    FENCE();
  };
  auto bput = [&](int i0, int i1) {
#pragma unroll
    for (int i = i0; i < i1; i++) {
      const int st = 11 + (i >> 1), sl = st < 18 ? st : st - 18;
      *(LDSP u4*)(sm + (17 - sl) * 8192 + lw * 2048 + (i & 1) * 1024 + lane * 16) = br[i];
    }
  };
  if (hascs) WAITV(28); else WAITV(18);
  BAR(); STAMP(13);
  issue(2);
  if (hascs) WAITV(28); else WAITV(18);
  BAR(); STAMP(14);
  issue(3); WAITV(13); BAR();
  issue(4); p0_finish(p, d0, rws, lw, lane, hascs);
  STAMP(15); WAITV(13); BAR();
  issue(5); breg(0, 4); WAITV(17); BAR();
  STAMP(16);
  const u64 tag = lds_tag(sm);
  if (t == 0) st_flag(p.ws, WS_F0 + b * 8, tag);
  FENCE();
  issue(6); breg(4, 8); WAITV(21); BAR();
  issue(7); breg(8, 12); WAITV(21); BAR();
  issue(8);
  u64 f0[4];
#pragma unroll
  for (int i = 0; i < 4; i++) f0[i] = ld_flag(p.ws, WS_F0 + (i * 64 + lane) * 8);
  WAITV(21); BAR(); STAMP(17);
  issue(9);
  if (!__all(f0[0] == tag && f0[1] == tag && f0[2] == tag && f0[3] == tag)) {
#pragma unroll 1
    for (int i = 0; i < 4; i++) wait_flag(p.ws, WS_F0 + (i * 64 + lane) * 8, tag, tag - 1);
  }
  FENCE();
  const u2 stv = bld64<SC1>(rws, min(row0 + t, S - 1) * 8, WS_STATS);
  float csv[8];
  {
    const int tc = gu ? t : min(t, 159);
    const int col = gu ? 1920 + (tc >> 7) * 1536 + cg * 128 + (tc & 127)
                       : (type == 1 ? cg * 160 + tc : cg * 80 + tc + (tc < 80 ? 640 : (tc < 96 ? 1264 : 1184)));
#pragma unroll
    for (int i = 0; i < 8; i++) csv[i] = __uint_as_float(bld32<SC1>(rws, col * 4, WS_CS + i * NCOL * 4));
  }
  FENCE();
  breg(12, 16);
  BAR(); STAMP(18);
#pragma unroll
  for (int i = 5; i < 11; i++) bdma(i);
  FENCE();
  breg(16, 24);
  *(LDSP u2*)(sm + IMG + t * 8) = u2{0, 0};
  *(LDSP unsigned*)(sm + AUX + t * 4) = stv[0];
  *(LDSP unsigned*)(sm + AUX + 1024 + t * 4) = stv[1];
  *(LDSP float*)(sm + AUX + 2048 + t * 4) = ((csv[0] + csv[1]) + (csv[2] + csv[3])) + ((csv[4] + csv[5]) + (csv[6] + csv[7]));
  if (type == 1) WAITV(0);
  WAITV(24); WAITL0(); BAR();
  STAMP(3);
  if (type == 1) {
    const unsigned fk = WS_FKV + (cg * 16 + (lane & 15)) * 8;
    wait_flag(p.ws, fk, tag, ld_flag(p.ws, fk));
    STAMP(4);
    u4 pv[4][8];
#pragma unroll
    for (int m = 0; m < 4; m++) {
      const int g = min(t + m * 256, 839), hg = g >= 420, off = hg * 7168 + (g - hg * 420) * 16;
      if (m < 3 || lw < 2) {
#pragma unroll
        for (int i = 0; i < 8; i++) pv[m][i] = bld128<SC1>(rws, off, WS_KVP + (i * 8 + cg * 2) * 7168);
      }
    }
    FENCE();
#pragma unroll
    for (int m = 0; m < 4; m++) {
      if (m < 3 || lw < 2) {
        const int g = min(t + m * 256, 839), hg = g >= 420, idx = g - hg * 420, dq = idx / 21, ee = idx - dq * 21, hl = dq / 5;
        f4 s = __builtin_bit_cast(f4, pv[m][0]);
#pragma unroll
        for (int i = 1; i < 8; i++) s += __builtin_bit_cast(f4, pv[m][i]);
        const u2 v = u2{pk2(s[0], s[1]), pk2(s[2], s[3])};
        LDSP char* dst = sm + IMG + hg * 25600 + (dq >> 2) * 1024 + (dq & 1) * 512 + ((dq >> 1) & 1) * 8;
        if (ee < 20) {
          const int eg = hl * 20 + ee;
          *(LDSP u2*)(dst + (eg >> 4) * 5120 + (eg & 15) * 16) = v;
        } else {
#pragma unroll
          for (int k = 0; k < 5; k++) {
            const int eg = hl * 20 + k * 4;
            *(LDSP u2*)(dst + (eg >> 4) * 5120 + (eg & 15) * 16 + 256) = v;
          }
        }
      }
    }
    WAITL0(); BAR();
    BAR();
    return;
  }
#pragma unroll
  for (int i = 0; i < 5; i++) bdma(i);
  FENCE();
  breg(24, 46);
  WAITV(22);
  STAMP(4);
  if (type == 2) BAR();
  BAR();
  bput(0, 14);
  WAITL0(); BAR();
  bput(14, 30);
  WAITL0(); BAR();
  bput(30, 46);
  WAITL0(); BAR();
  unsigned go[2]; u4 xv[2];
#pragma unroll
  for (int i = 0; i < 2; i++) {
    const int item = i * 256 + t;
    go[i] = (unsigned)((row2 + (item >> 3)) * 1280 + cg2 * 128 + (item & 7) * 16);
    xv[i] = *(const GLBP u4*)(p.x + go[i]);
  }
  BAR();
#pragma unroll
  for (int i = 0; i < 2; i++) p2_out(p, sm, i * 256 + t, xv[i], go[i]);
}

__global__ __launch_bounds__(512, 2) void fused_kernel(Params p) {
  extern __shared__ __attribute__((aligned(16))) char smem[];
  LDSP char* sm = (LDSP char*)smem;
  const int lane = threadIdx.x & 63, w = __builtin_amdgcn_readfirstlane(threadIdx.x >> 6), b = blockIdx.x;
  STAMPR(0); STAMP(1);
  int type, rt, cg, n2;
  {
    const int c = b & 7, j = b >> 3, a = c & 1, q = c >> 1;
    if (j < 12) {
      rt = 4 * a + j / 3; const int m = j % 3;
      if (m == 0) { type = 1; cg = q; } else { type = 2; cg = 2 * q + m - 1; }
      n2 = (j / 3) * 2 + m - 1;
    } else {
      const int n = j - 12 + (a ? 19 : 0);
      type = (!a && j == 31) ? 3 : 0; rt = n / 3; cg = 3 * q + n % 3;
      n2 = j - 4;
    }
    n2 = max(n2 + q * 55 + a * 27, 0);
  }
  const int row0 = type == 0 ? min(rt * 160, S - 160) : rt * 256;
  const int rt2 = n2 / 10, cg2 = n2 - rt2 * 10, row2 = min(rt2 * 96, S - 96);
  const rsrc_t rws = mkrs(p.ws, WS_END);
  if (type == 3) {
    if (w < 4) return;
    const u64 tag = ld_flag(p.ws, WS_GEN) + 1;
    const bool hascs = (w - 4) * 256 + b < 624;
    P0 d0;
    p0_load(p, d0, w - 4, lane, hascs);
    p0_finish(p, d0, rws, w - 4, lane, hascs);
    WAITV(0); BAR();
    if (w != 4) return;
    if (lane == 0) st_flag(p.ws, WS_F0 + b * 8, tag);
    if (b != 248) return;
#pragma unroll 1
    for (int i = 0; i < 4; i++) wait_flag(p.ws, WS_F0 + (i * 64 + lane) * 8, tag, tag - 1);
    if (lane == 0) st_flag(p.ws, WS_GEN, tag);
    return;
  }
  if (w >= 4) {
    loader(p, sm, rws, w - 4, lane, type, rt, cg, row0, row2, cg2);
  } else {
    if (threadIdx.x == 0) {
      const u64 tag = ld_flag(p.ws, WS_GEN) + 1;
      *(LDSP u2*)(sm + AUX + 3072) = u2{(unsigned)tag, (unsigned)(tag >> 32)};
    }
    WAITL0();
    if (type == 0) k1_mfma<0>(p, sm, rws, w, lane, rt, cg, row0);
    else if (type == 1) k1_mfma<1>(p, sm, rws, w, lane, rt, cg, row0);
    else k1_mfma<2>(p, sm, rws, w, lane, rt, cg, row0);
    STAMP(7);
    if (type != 1) p2_mfma(p, sm, rws, w, lane, row2, cg2);
  }
  STAMP(11); STAMPR(2);
}

extern "C" void kernel_launch(void* const* d_in, const int* in_sizes, int n_in, void* d_out, int out_size,
                              void* d_ws, size_t ws_size, hipStream_t stream) {
  Params p;
  p.x = (const char*)d_in[0]; p.wqkv = (const char*)d_in[1]; p.wo = (const char*)d_in[2];
  p.wg = (const char*)d_in[4]; p.wu = (const char*)d_in[5]; p.wd = (const char*)d_in[6];
  p.out = (char*)d_out; p.ws = (char*)d_ws;
  (void)hipFuncSetAttribute((const void*)fused_kernel, hipFuncAttributeMaxDynamicSharedMemorySize, LDS_BYTES);
  fused_kernel<<<256, 512, LDS_BYTES, stream>>>(p);
}
```

```cpp
#include <hip/hip_runtime.h>
#include <hip/hip_bf16.h>
#include <stdint.h>

typedef __attribute__((ext_vector_type(8))) __bf16 bf8;
typedef __attribute__((ext_vector_type(2))) __bf16 bf2;
typedef __attribute__((ext_vector_type(4))) short s4;
typedef __attribute__((ext_vector_type(2))) float f2;
typedef __attribute__((ext_vector_type(4))) float f4;
typedef __attribute__((ext_vector_type(16))) float f16v;
typedef __attribute__((ext_vector_type(2))) unsigned u2;
typedef __attribute__((ext_vector_type(4))) unsigned u4;
typedef unsigned long long u64;
typedef __amdgpu_buffer_rsrc_t rsrc_t;
#define LDSP __attribute__((address_space(3)))
#define GLBP __attribute__((address_space(1)))
#define DI __device__ __forceinline__

constexpr int S = 2048, D = 640, F = 1536;
constexpr int NCOL = 4992;
constexpr unsigned WS_GEN = 0;
constexpr unsigned WS_F0 = 4096;
constexpr unsigned WS_FH = 8192;
constexpr unsigned WS_FKV = 12288;
constexpr unsigned WS_FO = 16384;
constexpr unsigned WS_STATS = 32768;
constexpr unsigned WS_CS = 65536;
constexpr unsigned WS_KVP = 262144;
constexpr unsigned WS_H = 1 << 20;
constexpr unsigned WS_O = WS_H + S * F * 2;
constexpr unsigned WS_END = WS_O + S * D * 2;

constexpr int SLOT = 52 * 1024;
constexpr int LDS_BYTES = 160 * 1024;
constexpr int AUX = 3 * SLOT;
constexpr int IMG = 54 * 1024;
constexpr int SC1 = 16;
constexpr unsigned CODE_BYTES = 44 * 1024;

DI rsrc_t mkrs(const void* b, unsigned n) { return __builtin_amdgcn_make_buffer_rsrc((void*)b, (short)0, (int)n, 0x00020000); }
DI void blds16(rsrc_t rs, unsigned voff, unsigned soff, LDSP char* l) {
  __builtin_amdgcn_raw_ptr_buffer_load_lds(rs, (LDSP void*)l, 16, voff, soff, 0, 0);
}
template <int AUXB> DI u4 bld128(rsrc_t rs, unsigned voff, unsigned soff) { return __builtin_amdgcn_raw_buffer_load_b128(rs, voff, soff, AUXB); }
template <int AUXB> DI u2 bld64(rsrc_t rs, unsigned voff, unsigned soff) { return __builtin_amdgcn_raw_buffer_load_b64(rs, voff, soff, AUXB); }
template <int AUXB> DI unsigned bld32(rsrc_t rs, unsigned voff, unsigned soff) { return __builtin_amdgcn_raw_buffer_load_b32(rs, voff, soff, AUXB); }
DI void bst128(u4 v, rsrc_t rs, unsigned voff, unsigned soff) { __builtin_amdgcn_raw_buffer_store_b128(v, rs, voff, soff, SC1); }
DI void bst64(u2 v, rsrc_t rs, unsigned voff, unsigned soff) { __builtin_amdgcn_raw_buffer_store_b64(v, rs, voff, soff, SC1); }
DI u4 lds_r128(LDSP char* p) { return *(LDSP u4*)p; }
DI u2 lds_tr(LDSP char* p) { return __builtin_bit_cast(u2, __builtin_amdgcn_ds_read_tr16_b64_v4i16((LDSP s4*)p)); }
DI f16v mfma32(u4 a, u4 b, f16v c) { return __builtin_amdgcn_mfma_f32_32x32x16_bf16(__builtin_bit_cast(bf8, a), __builtin_bit_cast(bf8, b), c, 0, 0, 0); }
DI unsigned pk2(float lo, float hi) { f2 v = {lo, hi}; return __builtin_bit_cast(unsigned, __builtin_convertvector(v, bf2)); }
DI float blo(unsigned v) { return __uint_as_float(v << 16); }
DI float bhi(unsigned v) { return __uint_as_float(v & 0xffff0000u); }
DI float exp2c(float u) { return __builtin_amdgcn_fmed3f(__builtin_amdgcn_exp2f(u), 0.f, 1.f); }
constexpr float LOG2E = 1.44269504088896f, LN2 = 0.693147180559945f;
DI unsigned phi2(f2 u) {
  const f2 t = u * LN2 + 1e-6f, e = {exp2c(u[0]), exp2c(u[1])};
  const f2 v = f2{fmaxf(t[0], 1e-6f), fmaxf(t[1], 1e-6f)} + e;
  return pk2(v[0], v[1]);
}
DI u64 ld_flag(const char* ws, unsigned off) { return __hip_atomic_load((const GLBP u64*)(ws + off), __ATOMIC_RELAXED, __HIP_MEMORY_SCOPE_AGENT); }
DI void st_flag(char* ws, unsigned off, u64 v) { __hip_atomic_store((GLBP u64*)(ws + off), v, __ATOMIC_RELAXED, __HIP_MEMORY_SCOPE_AGENT); }
#define BAR() __builtin_amdgcn_s_barrier()
#define WAITV(n) asm volatile("s_waitcnt vmcnt(" #n ")" ::: "memory")
#define WAITL0() asm volatile("s_waitcnt lgkmcnt(0)" ::: "memory")
#define SCHED0() __builtin_amdgcn_sched_barrier(0)
#define FENCE() do { asm volatile("" ::: "memory"); __builtin_amdgcn_sched_barrier(0); } while (0)

#define STAMP(i)
#define STAMPR(i)

struct Params {
  const char* x; const char* wqkv; const char* wo; const char* wg; const char* wu; const char* wd;
  char* out; char* ws;
};

DI void wait_flag(const char* ws, unsigned off, u64 tag, u64 first) {
  u64 v = first;
#pragma unroll 1
  for (int it = 0; it < (1 << 18); it++) {
    if (__all(v == tag)) break;
    __builtin_amdgcn_s_sleep(1);
    v = ld_flag(ws, off);
  }
  FENCE();
}
DI u64 lds_tag(LDSP char* sm) {
  const u2 t = *(LDSP u2*)(sm + AUX + 3072);
  return (u64)(unsigned)__builtin_amdgcn_readfirstlane(t[0]) | ((u64)(unsigned)__builtin_amdgcn_readfirstlane(t[1]) << 32);
}

template <int CTL> DI float dppadd(float v) { return v + __int_as_float(__builtin_amdgcn_update_dpp(0, __float_as_int(v), CTL, 0xf, 0xf, true)); }
DI float swap16add(float v) { const u2 t = __builtin_amdgcn_permlane16_swap(__float_as_uint(v), __float_as_uint(v), false, false); return __uint_as_float(t[0]) + __uint_as_float(t[1]); }
DI float sum32(float v) { return swap16add(dppadd<0x121>(dppadd<0x122>(dppadd<0x124>(dppadd<0x128>(v))))); }
constexpr unsigned EMPTY = 0xffffffffu;

struct P0 { u2 xs[5]; u4 cw[10]; };
DI void p0_load(const Params& p, P0& d, int lw, int lane, bool hascs) {
  const int b = blockIdx.x;
  const rsrc_t rx = mkrs(p.x, S * D * 2);
  const unsigned xo = (unsigned)((b * 8 + lw * 2 + (lane >> 5)) * 1280 + (lane & 31) * 8);
#pragma unroll
  for (int i = 0; i < 5; i++) d.xs[i] = bld64<0>(rx, xo + i * 256, 0);
  if (hascs) {
    const int task = lw * 256 + b, kp = task / 78, cu = task - kp * 78;
    const char* base = cu < 30 ? p.wqkv + cu * 128 : (cu < 54 ? p.wg + (cu - 30) * 128 : p.wu + (cu - 54) * 128);
    const unsigned stride = cu < 30 ? 3840 : 3072;
    const rsrc_t rw = mkrs(base, 640 * stride - 127);
    const unsigned wo = (unsigned)(kp * 80 + (lane >> 3)) * stride + (lane & 7) * 16;
#pragma unroll
    for (int i = 0; i < 10; i++) d.cw[i] = bld128<0>(rw, wo, i * 8 * stride);
  }
}
DI void p0_finish(const Params& p, P0& d, rsrc_t rws, int lw, int lane, bool hascs) {
  const int b = blockIdx.x;
  if (lw * 64 + lane < 105) bst128(u4{EMPTY, EMPTY, EMPTY, EMPTY}, rws, ((b & 3) * 105 + lw * 64 + lane) * 16, WS_KVP + (b >> 2) * 7168);
#pragma unroll
  for (int i = 0; i < 5; i++) asm volatile("" : "+v"(d.xs[i]));
  float s = 0;
#pragma unroll
  for (int i = 0; i < 5; i++) s += (blo(d.xs[i][0]) + bhi(d.xs[i][0])) + (blo(d.xs[i][1]) + bhi(d.xs[i][1]));
  s = sum32(s);
  const float mu = s * (1.f / 640.f);
  float q = 0;
#pragma unroll
  for (int i = 0; i < 5; i++) {
    const float a = blo(d.xs[i][0]) - mu, c = bhi(d.xs[i][0]) - mu, e = blo(d.xs[i][1]) - mu, g = bhi(d.xs[i][1]) - mu;
    q += a * a + c * c + e * e + g * g;
  }
  q = sum32(q);
  const float rs = rsqrtf(q * (1.f / 640.f) + 1e-5f);
  if ((lane & 31) == 0) bst64(u2{__float_as_uint(mu), __float_as_uint(rs)}, rws, (b * 8 + lw * 2 + (lane >> 5)) * 8, WS_STATS);
  if (hascs) {
    const int task = lw * 256 + b, kp = task / 78, cu = task - kp * 78;
#pragma unroll
    for (int i = 0; i < 10; i++) asm volatile("" : "+v"(d.cw[i]));
    float c[8];
#pragma unroll
    for (int j = 0; j < 8; j++) c[j] = 0;
#pragma unroll
    for (int i = 0; i < 10; i++)
#pragma unroll
      for (int j = 0; j < 4; j++) { c[2 * j] += blo(d.cw[i][j]); c[2 * j + 1] += bhi(d.cw[i][j]); }
    float e[4];
#pragma unroll
    for (int j = 0; j < 4; j++) {
      const u2 t = __builtin_amdgcn_permlane32_swap(__float_as_uint(c[j]), __float_as_uint(c[4 + j]), false, false);
      e[j] = dppadd<0x128>(swap16add(__uint_as_float(t[0]) + __uint_as_float(t[1])));
    }
    if ((lane & 24) == 0)
      bst128(u4{__float_as_uint(e[0]), __float_as_uint(e[1]), __float_as_uint(e[2]), __float_as_uint(e[3])}, rws,
             (kp * NCOL + cu * 64 + (lane & 7) * 8 + (lane >> 5) * 4) * 4, WS_CS);
  }
}

template <int TYPE>
DI void k1_main(LDSP char* sm, int w, int lane, f16v (&acc)[10]) {
  constexpr int NXF = TYPE == 0 ? 5 : 2, NWF = 7 - NXF;
  constexpr int XB = TYPE == 0 ? 160 * 128 : 256 * 128;
  const int r = lane & 31, hh = lane >> 5, blk = (lane >> 4) & 1, q = (lane & 15) >> 2, pp = lane & 3;
  unsigned xa[4];
#pragma unroll
  for (int k = 0; k < 4; k++) xa[k] = (unsigned)(((TYPE == 0 ? 0 : w * 64) + r) * 128 + (((2 * k + hh) ^ ((r >> 1) & 7)) << 4));
  unsigned wa[2];
  if (TYPE == 0) {
#pragma unroll
    for (int ft = 0; ft < 2; ft++)
      wa[ft] = (unsigned)(XB + blk * 16384 + (8 * hh + q) * 256 + (((w * 4 + ft * 2 + (pp >> 1)) ^ (q << 2) ^ (blk << 1)) << 4) + (pp & 1) * 8);
  } else {
    wa[0] = (unsigned)(XB + (8 * hh + q) * 320 + blk * 32 + pp * 8); wa[1] = 0;
  }
  u4 xf[2][NXF], wf[2][NWF];
  auto rd = [&](int buf, LDSP char* base, int k) {
#pragma unroll
    for (int i = 0; i < NWF; i++) {
      u2 lo, hi;
      if (TYPE == 0) { lo = lds_tr(base + wa[i] + k * 4096); hi = lds_tr(base + wa[i] + k * 4096 + 1024); }
      else { lo = lds_tr(base + wa[0] + k * 5120 + i * 64); hi = lds_tr(base + wa[0] + k * 5120 + i * 64 + 1280); }
      wf[buf][i] = u4{lo[0], lo[1], hi[0], hi[1]};
    }
#pragma unroll
    for (int i = 0; i < NXF; i++) xf[buf][i] = lds_r128(base + xa[k] + i * 4096);
  };
  auto mm = [&](int buf) {
#pragma unroll
    for (int i = 0; i < NXF; i++)
#pragma unroll
      for (int j = 0; j < NWF; j++) {
        if (TYPE == 2) acc[i * NWF + j] = mfma32(xf[buf][i], wf[buf][j], acc[i * NWF + j]);
        else acc[i * NWF + j] = mfma32(wf[buf][j], xf[buf][i], acc[i * NWF + j]);
      }
  };
  auto inter = [&]() {
    constexpr int NR = NXF + 2 * NWF;
#pragma unroll
    for (int i = 0; i < 6; i++) {
      __builtin_amdgcn_sched_group_barrier(0x8, 1, 0);
      __builtin_amdgcn_sched_group_barrier(0x100, (NR + 5) / 6, 0);
    }
    __builtin_amdgcn_sched_group_barrier(0x8, 4, 0);
  };
  BAR();
  rd(0, sm, 0);
  int slot = 0;
#pragma unroll 1
  for (int s = 0; s < 9; s++) {
    LDSP char* base = sm + slot * SLOT;
    slot = slot == 2 ? 0 : slot + 1;
    SCHED0();
    rd(1, base, 1); mm(0); inter(); SCHED0();
    rd(0, base, 2); mm(1); inter(); SCHED0();
    rd(1, base, 3); mm(0); inter(); SCHED0();
    WAITL0(); BAR(); SCHED0();
    rd(0, sm + slot * SLOT, 0); mm(1); inter(); SCHED0();
  }
  SCHED0();
  rd(1, sm, 1); mm(0); inter(); SCHED0();
  rd(0, sm, 2); mm(1); inter(); SCHED0();
  rd(1, sm, 3); mm(0); inter(); SCHED0();
  mm(1); SCHED0();
}

DI void kv_publish(LDSP char* sm, rsrc_t rws, int rt, int cg) {
  const int t = threadIdx.x;
  if (t < 420) {
    LDSP char* src = sm + t * 16;
    const f4 s = (*(LDSP f4*)src + *(LDSP f4*)(src + 8192)) + (*(LDSP f4*)(src + 16384) + *(LDSP f4*)(src + 24576));
    u4 v = __builtin_bit_cast(u4, s);
#pragma unroll
    for (int j = 0; j < 4; j++) v[j] = min(v[j], EMPTY - 1);
    bst128(v, rws, t * 16, WS_KVP + (rt * 8 + cg) * 7168);
  }
}

template <int TYPE>
DI void k1_mfma(const Params& p, LDSP char* sm, rsrc_t rws, int w, int lane, int rt, int cg, int row0) {
  const int hh = lane >> 5, r = lane & 31;
  f16v acc[10];
#pragma unroll
  for (int i = 0; i < 10; i++)
#pragma unroll
    for (int j = 0; j < 16; j++) acc[i][j] = 0.f;
  k1_main<TYPE>(sm, w, lane, acc);
  STAMP(3);
  if (TYPE == 0) {
    f4 cs[2][4];
#pragma unroll
    for (int ft = 0; ft < 2; ft++)
#pragma unroll
      for (int qd = 0; qd < 4; qd++)
        cs[ft][qd] = *(LDSP f4*)(sm + AUX + 2048 + ((qd >> 1) * 128 + w * 32 + ft * 16 + (qd & 1) * 8 + hh * 4) * 4);
    const unsigned dst = (unsigned)(((((row0 + r) >> 3) * 48 + cg * 4 + w) * 4 + hh) * 128 + (r & 7) * 16);
#pragma unroll
    for (int tt = 0; tt < 5; tt++) {
      const float mu = *(LDSP float*)(sm + AUX + (tt * 32 + r) * 4);
      if (tt == 0 && rt == 12) continue;
#pragma unroll
      for (int ft = 0; ft < 2; ft++) {
        const f16v a = acc[tt * 2 + ft];
        float h[8];
#pragma unroll
        for (int j = 0; j < 8; j++) h[j] = (a[j] - mu * cs[ft][j >> 2][j & 3]) * (a[8 + j] - mu * cs[ft][2 + (j >> 2)][j & 3]);
        bst128(u4{pk2(h[0], h[1]), pk2(h[2], h[3]), pk2(h[4], h[5]), pk2(h[6], h[7])}, rws, dst + ft * 256, WS_H + tt * 4 * 24576);
      }
    }
    STAMP(4);
    WAITV(0); BAR();
    if (threadIdx.x == 0) st_flag(p.ws, WS_FH + (rt * 16 + cg) * 8, lds_tag(sm));
  } else if (TYPE == 1) {
    u4 pq[2][10];
#pragma unroll
    for (int tt = 0; tt < 2; tt++) {
      const float mu = *(LDSP float*)(sm + AUX + (w * 64 + tt * 32 + r) * 4);
      const float rl = *(LDSP float*)(sm + AUX + 1024 + (w * 64 + tt * 32 + r) * 4) * LOG2E;
#pragma unroll
      for (int ft = 0; ft < 5; ft++) {
        const f16v a = acc[tt * 5 + ft];
#pragma unroll
        for (int qd = 0; qd < 4; qd++) {
          const f4 cs = *(LDSP f4*)(sm + AUX + 2048 + (ft * 32 + qd * 8 + hh * 4) * 4);
#pragma unroll
          for (int j = 0; j < 2; j++) {
            const f2 u = (f2{a[qd * 4 + 2 * j], a[qd * 4 + 2 * j + 1]} - f2{cs[2 * j], cs[2 * j + 1]} * mu) * rl;
            pq[tt][ft * 2 + (qd >> 1)][(qd & 1) * 2 + j] = phi2(u);
          }
        }
        asm volatile("" : "+v"(pq[tt][ft * 2]), "+v"(pq[tt][ft * 2 + 1]));
      }
    }
    STAMP(4);
    SCHED0(); BAR(); SCHED0();
    STAMP(5);
    const unsigned dst = (unsigned)((((row0 + w * 64 + r) >> 3) * 20 + cg * 5) * 512 + hh * 128 + (r & 7) * 16);
    f16v oa[2][2];
    u4 af[2][3];
    auto rdi = [&](int n) {
      const int hg = n / 5, T = n % 5, k0 = T < 2 ? 0 : T - 1, k1 = T == 0 ? 1 : (T == 4 ? 4 : T + 1);
#pragma unroll
      for (int k = k0; k <= k1; k++) af[n & 1][k - k0] = lds_r128(sm + IMG + (hg * 25 + T * 5 + k) * 1024 + lane * 16);
    };
    auto omm = [&](int n) {
      const int hg = n / 5, T = n % 5, k0 = T < 2 ? 0 : T - 1, k1 = T == 0 ? 1 : (T == 4 ? 4 : T + 1);
#pragma unroll
      for (int tt = 0; tt < 2; tt++)
#pragma unroll
        for (int j = 0; j < 16; j++) oa[n & 1][tt][j] = 0.f;
#pragma unroll
      for (int k = k0; k <= k1; k++)
#pragma unroll
        for (int tt = 0; tt < 2; tt++) oa[n & 1][tt] = mfma32(af[n & 1][k - k0], pq[tt][hg * 5 + k], oa[n & 1][tt]);
    };
    rdi(0); rdi(1); omm(0); SCHED0();
#pragma unroll
    for (int n = 0; n < 10; n++) {
      if (n + 1 < 10) omm(n + 1);
      if (n + 2 < 10) rdi(n + 2);
#pragma unroll
      for (int tt = 0; tt < 2; tt++) {
        const f16v a = oa[n & 1][tt];
        const float d0 = __builtin_amdgcn_rcpf(a[8] + 1e-6f), d1 = __builtin_amdgcn_rcpf(a[12] + 1e-6f);
        bst128(u4{pk2(a[0] * d0, a[1] * d0), pk2(a[2] * d0, a[3] * d0), pk2(a[4] * d1, a[5] * d1), pk2(a[6] * d1, a[7] * d1)},
               rws, dst + n * 256, WS_O + tt * 4 * 10240);
      }
#pragma unroll
      for (int i = 0; i < 6; i++) { __builtin_amdgcn_sched_group_barrier(0x8, 1, 0); __builtin_amdgcn_sched_group_barrier(0x2, 5, 0); }
      SCHED0();
    }
    STAMP(6);
    WAITV(0); BAR();
    if (threadIdx.x == 0) st_flag(p.ws, WS_FO + (rt * 16 + cg) * 8, lds_tag(sm));
  } else {
    float cs[5];
#pragma unroll
    for (int ct = 0; ct < 5; ct++) cs[ct] = *(LDSP float*)(sm + AUX + 2048 + (ct * 32 + r) * 4);
    const bool isk = r < 16;
    const float kmul = isk ? 1.f : 0.f, kadd = isk ? 1e-6f : 0.f, klo = isk ? 1e-6f : -__builtin_inff();
    u4 fr[4][5];
#pragma unroll
    for (int g = 0; g < 4; g++) {
      f2 mu[4], rs[4], rl[4];
#pragma unroll
      for (int hq = 0; hq < 2; hq++) {
        LDSP char* sp = sm + AUX + (w * 64 + g * 16 + hq * 8 + hh * 4) * 4;
        const f4 a = *(LDSP f4*)sp, b = *(LDSP f4*)(sp + 1024);
        mu[hq * 2] = f2{a[0], a[1]}; mu[hq * 2 + 1] = f2{a[2], a[3]};
        rs[hq * 2] = f2{b[0], b[1]}; rs[hq * 2 + 1] = f2{b[2], b[3]};
      }
#pragma unroll
      for (int j = 0; j < 4; j++) rl[j] = rs[j] * LOG2E;
#pragma unroll
      for (int ct = 0; ct < 5; ct++) {
#pragma unroll
        for (int j = 0; j < 4; j++) {
          const f16v& a = acc[(g >> 1) * 5 + ct];
          const f2 y = f2{a[(g & 1) * 8 + 2 * j], a[(g & 1) * 8 + 2 * j + 1]} - mu[j] * cs[ct];
          if (ct < 2) fr[g][ct][j] = phi2(y * rl[j]);
          else if (ct == 2) {
            const f2 u = y * rl[j], t = y * rs[j] + kadd, e = {exp2c(u[0]), exp2c(u[1])};
            const f2 v = e * kmul + f2{fmaxf(t[0], klo), fmaxf(t[1], klo)};
            fr[g][ct][j] = pk2(v[0], v[1]);
          } else { const f2 v = y * rs[j]; fr[g][ct][j] = pk2(v[0], v[1]); }
        }
      }
      SCHED0();
    }
    f16v z[8];
#pragma unroll
    for (int i = 0; i < 8; i++)
#pragma unroll
      for (int j = 0; j < 16; j++) z[i][j] = 0.f;
#pragma unroll
    for (int g = 0; g < 4; g++) {
      u4 mb;
#pragma unroll
      for (int j = 0; j < 4; j++) mb[j] = isk ? 0x3f803f80u : fr[g][2][j];
      z[0] = mfma32(fr[g][0], fr[g][3], z[0]);
      z[1] = mfma32(fr[g][0], fr[g][4], z[1]);
      z[2] = mfma32(fr[g][0], mb, z[2]);
      z[3] = mfma32(fr[g][1], fr[g][3], z[3]);
      z[4] = mfma32(fr[g][1], fr[g][4], z[4]);
      z[5] = mfma32(fr[g][1], mb, z[5]);
      z[6] = mfma32(fr[g][2], fr[g][4], z[6]);
      z[7] = mfma32(fr[g][2], mb, z[7]);
    }
    unsigned pa[3], pm[3];
#pragma unroll
    for (int vt = 0; vt < 3; vt++) {
      const int e = vt == 2 ? 48 + r : vt * 32 + r, he = e / 20;
      int ee = e - he * 20; unsigned m = 31u << (5 * he);
      if (vt == 2 && isk) { ee = 20; m = r == 0 ? 0xfffffu : 0u; }
      pa[vt] = (unsigned)(w * 8192 + ee * 16 + hh * 336); pm[vt] = m >> hh;
    }
    SCHED0();
#pragma unroll
    for (int i = 0; i < 8; i++)
#pragma unroll
      for (int qd = 0; qd < 4; qd++) {
        const int kt = i / 3, vt = i < 6 ? i % 3 : i - 5, c = kt * 8 + qd * 2;
        const bool need = (i == 1) ? qd >= 2 : (i == 3) ? qd == 0 : (i >= 6) ? qd < 2 : true;
        if (need && (pm[vt] & (1u << c)))
          *(LDSP f4*)(sm + pa[vt] + c * 336) = f4{z[i][qd * 4], z[i][qd * 4 + 1], z[i][qd * 4 + 2], z[i][qd * 4 + 3]};
      }
    STAMP(4);
    WAITL0(); BAR();
    kv_publish(sm, rws, rt, cg);
    WAITL0(); BAR();
  }
}

DI void p2_out(const Params& p, LDSP char* sm, int item, u4 xv, unsigned go) {
  const int row = item >> 3, c = item & 7;
  LDSP char* src = sm + (row >> 3) * 8192 + (row & 7) * 256;
  const unsigned c0 = ((2 * c) ^ (row & 7)) << 4, c1 = c0 ^ 16;
  f4 a = *(LDSP f4*)(src + c0), b = *(LDSP f4*)(src + c1);
#pragma unroll
  for (int i = 1; i < 4; i++) { a += *(LDSP f4*)(src + i * 2048 + c0); b += *(LDSP f4*)(src + i * 2048 + c1); }
  *(GLBP u4*)(p.out + go) = u4{pk2(blo(xv[0]) + a[0], bhi(xv[0]) + a[1]), pk2(blo(xv[1]) + a[2], bhi(xv[1]) + a[3]),
                               pk2(blo(xv[2]) + b[0], bhi(xv[2]) + b[1]), pk2(blo(xv[3]) + b[2], bhi(xv[3]) + b[3])};
}

constexpr int PD = 8;
DI void p2_mfma(const Params& p, LDSP char* sm, rsrc_t rws, int w, int lane, int row2, int cg2) {
  const int hh = lane >> 5, r = lane & 31, blk = (lane >> 4) & 1, q = (lane & 15) >> 2, pp = lane & 3;
  const u64 tag = lds_tag(sm);
  const unsigned fh = WS_FH + (((row2 + ((lane >> 2) & 1) * 95) / 160) * 16 + 3 * w + min(lane & 3, 2)) * 8;
  const unsigned fo = WS_FO + (((row2 + (lane & 1) * 95) >> 8) * 16 + w) * 8;
  float r2[3];
  wait_flag(p.ws, fh, tag, ld_flag(p.ws, fh));
  STAMP(8);
  const unsigned oct = (unsigned)(row2 + r) >> 3, in = hh * 128 + (r & 7) * 16;
  const unsigned ah = oct * 24576 + w * 6144 + in, ao = oct * 10240 + w * 2560 + in;
  u4 af[PD][3], bf[2][2];
  auto lda = [&](int i) {
#pragma unroll
    for (int tt = 0; tt < 3; tt++) {
      if (i < 24) af[i % PD][tt] = bld128<SC1>(rws, ah + (i & 15) * 256, WS_H + tt * 98304 + (i >> 4) * 4096);
      else af[i % PD][tt] = bld128<SC1>(rws, ao + (i - 24) * 256, WS_O + tt * 40960);
    }
  };
  LDSP char* ba[2][3];
#pragma unroll
  for (int ct = 0; ct < 2; ct++)
#pragma unroll
    for (int sg = 0; sg < 3; sg++) {
      ba[ct][sg] = sm + sg * 49152 + w * 2048 + (4 * hh + q) * 128 + (((ct * 4 + blk * 2 + (pp >> 1)) ^ ((q >> 1) << 2)) << 4) + (pp & 1) * 8;
      asm volatile("" : "+v"(ba[ct][sg]));
    }
  auto ldb = [&](int i) {
    const int sl = 17 - (i < 18 ? i : i - 18);
#pragma unroll
    for (int ct = 0; ct < 2; ct++) {
      LDSP char* base = ba[ct][sl / 6] + (sl % 6) * 8192;
      const u2 lo = lds_tr(base), hi = lds_tr(base + 1024);
      bf[i & 1][ct] = u4{lo[0], lo[1], hi[0], hi[1]};
    }
  };
  f16v acc[6];
#pragma unroll
  for (int i = 0; i < 6; i++)
#pragma unroll
    for (int j = 0; j < 16; j++) acc[i][j] = 0.f;
#pragma unroll
  for (int i = 0; i < PD; i++) lda(i);
  ldb(0);
  u64 ofl = 0;
  constexpr int OCHK = 20;
#pragma unroll
  for (int i = 0; i < 34; i++) {
    SCHED0();
    if (i == 7 || i == 15 || i == 23) { STAMP(12 + i / 4); WAITL0(); BAR(); STAMP(13 + i / 4); SCHED0(); }
    if (i == OCHK) {
      STAMP(19);
      wait_flag(p.ws, fo, tag, ofl);
      STAMP(9);
#pragma unroll
      for (int j = 24; j < OCHK + PD; j++) lda(j);
      SCHED0();
    }
    if (i == 24) {
#pragma unroll
      for (int tt = 0; tt < 3; tt++) { acc[tt * 2] *= r2[tt] * r2[tt]; acc[tt * 2 + 1] *= r2[tt] * r2[tt]; }
      SCHED0();
    }
    if (i + 1 < 34) ldb(i + 1);
#pragma unroll
    for (int tt = 0; tt < 3; tt++)
#pragma unroll
      for (int ct = 0; ct < 2; ct++) acc[tt * 2 + ct] = mfma32(bf[i & 1][ct], af[i % PD][tt], acc[tt * 2 + ct]);
    SCHED0();
    if (i + PD < 24 || (i >= OCHK && i + PD < 34)) lda(i + PD);
    if (i + PD == 24) {
#pragma unroll
      for (int tt = 0; tt < 3; tt++) r2[tt] = __uint_as_float(bld32<SC1>(rws, (row2 + tt * 32 + r) * 8 + 4, WS_STATS));
      ofl = ld_flag(p.ws, fo);
    }
  }
  STAMP(10);
  const int item = 512 + threadIdx.x;
  const unsigned go = (unsigned)((row2 + (item >> 3)) * 1280 + cg2 * 128 + (item & 7) * 16);
  const u4 xv = *(const GLBP u4*)(p.x + go);
#pragma unroll
  for (int tt = 0; tt < 3; tt++)
#pragma unroll
    for (int ct = 0; ct < 2; ct++)
#pragma unroll
      for (int qd = 0; qd < 4; qd++)
        *(LDSP f4*)(sm + (tt * 4 + (r >> 3)) * 8192 + w * 2048 + (r & 7) * 256 + (((ct * 8 + qd * 2 + hh) ^ (r & 7)) << 4)) =
            f4{acc[tt * 2 + ct][qd * 4], acc[tt * 2 + ct][qd * 4 + 1], acc[tt * 2 + ct][qd * 4 + 2], acc[tt * 2 + ct][qd * 4 + 3]};
  WAITL0(); BAR();
  p2_out(p, sm, item, xv, go);
}

DI void loader(const Params& p, LDSP char* sm, rsrc_t rws, int lw, int lane, int type, int rt, int cg, int row0, int row2, int cg2) {
  const int b = blockIdx.x, t = lw * 64 + lane;
  const bool gu = type == 0;
  __builtin_amdgcn_s_setprio(3);
  unsigned vo[13];
  {
    const int x0 = gu ? lw * 5 : lw * 8;
    const unsigned xr = (unsigned)((row0 + x0 * 8 + (lane >> 3)) * 1280), xs = (unsigned)(((lane & 7) ^ (lane >> 4) ^ ((x0 & 1) << 2)) << 4);
#pragma unroll
    for (int i = 0; i < 8; i++) vo[i] = xr + (xs ^ ((i & 1) << 6)) + i * 10240;
    if (gu) {
      const unsigned g0 = (unsigned)(((lw & 1) * 32 + (lane >> 4)) * 3072 + (((lane & 15) ^ ((lane >> 4) << 2) ^ (lw & 2)) << 4));
#pragma unroll
      for (int i = 5; i < 13; i++) vo[i] = g0 + (i - 5) * 12288;
    } else {
#pragma unroll
      for (int i = 0; i < 5; i++) {
        const int e = i * 64 + lane, rq = (e * 205) >> 12, pos = e - rq * 20;
        const int adj = type == 1 ? 0 : (pos < 10 ? 0 : (pos < 12 ? 1248 : 1088));
        vo[8 + i] = (unsigned)((lw * 16 + rq) * 3840 + pos * 16 + adj);
      }
    }
  }
  const unsigned wofs = gu ? cg * 256 : (type == 1 ? cg * 320 : 1280 + cg * 160);
  const char* wbase = (gu ? ((lw >> 1) ? p.wu : p.wg) : p.wqkv) + wofs;
  const unsigned wstep = gu ? 64 * 3072 : 64 * 3840, wlen = wstep * 10 - wofs, mstep = gu ? wstep : 128;
  const rsrc_t rx = mkrs(p.x, S * D * 2), rw = mkrs(wbase, wlen), rm = mkrs(gu ? wbase : p.x, gu ? wlen : S * D * 2);
  const int dx = gu ? lw * 5120 : lw * 8192, dm = gu ? 20480 + lw * 8192 : dx + 5120, dw = gu ? dm + 3072 : 32768 + lw * 5120;
  auto issue = [&](int s) {
    LDSP char* d = sm + (s % 3) * SLOT;
#pragma unroll
    for (int i = 0; i < 5; i++) blds16(rx, vo[i], s * 128, d + dx + i * 1024);
#pragma unroll
    for (int i = 0; i < 3; i++) blds16(rm, vo[5 + i], s * mstep, d + dm + i * 1024);
#pragma unroll
    for (int i = 0; i < 5; i++) blds16(rw, vo[8 + i], s * wstep, d + dw + i * 1024);
    FENCE();
  };
  issue(0); issue(1);
  STAMP(12);
  WAITV(13); BAR(); STAMP(13);
  issue(2);
  const bool hascs = lw * 256 + b < 624;
  P0 d0;
  p0_load(p, d0, lw, lane, hascs);
  const rsrc_t rd_ = mkrs(p.wd, type == 1 ? 0 : F * D * 2), ro_ = mkrs(p.wo, type == 1 ? 0 : D * D * 2);
  const unsigned bo = (unsigned)((lane >> 3) * 1280 + cg2 * 128 + (((lane & 7) ^ (((lane >> 4) & 1) << 2)) << 4));
  auto bdma = [&](int i) {
#pragma unroll
    for (int h = 0; h < 2; h++) blds16(rd_, bo, (lw * 384 + i * 16 + h * 8) * 1280, sm + (17 - i) * 8192 + lw * 2048 + h * 1024);
  };
  u4 br[46];
  auto breg = [&](int i0, int i1) {
#pragma unroll
    for (int i = i0; i < i1; i++) {
      const int st = 11 + (i >> 1);
      br[i] = st < 24 ? bld128<0>(rd_, bo, (lw * 384 + st * 16 + (i & 1) * 8) * 1280) : bld128<0>(ro_, bo, (lw * 160 + (st - 24) * 16 + (i & 1) * 8) * 1280);
    }
    FENCE();
  };
  auto bput = [&](int i0, int i1) {
#pragma unroll
    for (int i = i0; i < i1; i++) {
      const int st = 11 + (i >> 1), sl = st < 18 ? st : st - 18;
      *(LDSP u4*)(sm + (17 - sl) * 8192 + lw * 2048 + (i & 1) * 1024 + lane * 16) = br[i];
    }
  };
  FENCE();
  if (hascs) WAITV(28); else WAITV(18);
  BAR(); STAMP(14);
  issue(3);
  if (hascs) WAITV(28); else WAITV(18);
  BAR();
  issue(4); p0_finish(p, d0, rws, lw, lane, hascs);
  STAMP(15); WAITV(13); BAR();
  issue(5); breg(0, 8); WAITV(21); BAR();
  STAMP(16);
  const u64 tag = lds_tag(sm);
  if (t == 0) st_flag(p.ws, WS_F0 + b * 8, tag);
  FENCE();
  issue(6); breg(8, 16); WAITV(29); BAR();
  issue(7); breg(16, 24); WAITV(29); BAR();
  issue(8);
  u64 f0[4];
#pragma unroll
  for (int i = 0; i < 4; i++) f0[i] = ld_flag(p.ws, WS_F0 + (i * 64 + lane) * 8);
  FENCE();
  breg(24, 31);
  WAITV(32); BAR(); STAMP(17);
  issue(9);
  if (!__all(f0[0] == tag && f0[1] == tag && f0[2] == tag && f0[3] == tag)) {
#pragma unroll 1
    for (int i = 0; i < 4; i++) wait_flag(p.ws, WS_F0 + (i * 64 + lane) * 8, tag, tag - 1);
  }
  FENCE();
  const u2 stv = bld64<SC1>(rws, min(row0 + t, S - 1) * 8, WS_STATS);
  float csv[8];
  {
    const int tc = gu ? t : min(t, 159);
    const int col = gu ? 1920 + (tc >> 7) * 1536 + cg * 128 + (tc & 127)
                       : (type == 1 ? cg * 160 + tc : cg * 80 + tc + (tc < 80 ? 640 : (tc < 96 ? 1264 : 1184)));
#pragma unroll
    for (int i = 0; i < 8; i++) csv[i] = __uint_as_float(bld32<SC1>(rws, col * 4, WS_CS + i * NCOL * 4));
  }
  FENCE();
  breg(31, 38);
  BAR(); STAMP(18);
#pragma unroll
  for (int i = 5; i < 11; i++) bdma(i);
  FENCE();
  breg(38, 46);
  *(LDSP u2*)(sm + IMG + t * 8) = u2{0, 0};
  *(LDSP unsigned*)(sm + AUX + t * 4) = stv[0];
  *(LDSP unsigned*)(sm + AUX + 1024 + t * 4) = stv[1];
  *(LDSP float*)(sm + AUX + 2048 + t * 4) = ((csv[0] + csv[1]) + (csv[2] + csv[3])) + ((csv[4] + csv[5]) + (csv[6] + csv[7]));
  if (type == 1) WAITV(0);
  WAITV(27); WAITL0(); BAR();
  STAMP(3);
  if (type == 1) {
    {
      const unsigned ho = WS_KVP + ((lane & 7) * 8 + cg * 2 + ((lane >> 3) & 1)) * 7168 + 419 * 16;
#pragma unroll 1
      for (int it = 0; it < (1 << 18); it++) {
        if (__any(bld32<SC1>(rws, ho, 0) != EMPTY)) break;
        FENCE();
      }
    }
    STAMP(4);
    u4 pv[4][8];
    unsigned off[4];
#pragma unroll
    for (int m = 0; m < 4; m++) { const int g = min(t + m * 256, 839), hg = g >= 420; off[m] = hg * 7168 + (g - hg * 420) * 16; }
    unsigned need = 255;
#pragma unroll 1
    for (int it = 0; it < (1 << 18) && need; it++) {
#pragma unroll
      for (int i = 0; i < 8; i++)
        if (need >> i & 1) {
#pragma unroll
          for (int m = 0; m < 3; m++) pv[m][i] = bld128<SC1>(rws, off[m], WS_KVP + (i * 8 + cg * 2) * 7168);
          if (lw < 2) pv[3][i] = bld128<SC1>(rws, off[3], WS_KVP + (i * 8 + cg * 2) * 7168);
          else pv[3][i] = u4{0, 0, 0, 0};
        }
      FENCE();
#pragma unroll
      for (int i = 0; i < 8; i++)
        if (need >> i & 1) {
          unsigned mx = 0;
#pragma unroll
          for (int m = 0; m < 4; m++) mx = max(max(mx, max(pv[m][i][0], pv[m][i][1])), max(pv[m][i][2], pv[m][i][3]));
          if (!__any(mx == EMPTY)) need &= ~(1u << i);
        }
      FENCE();
    }
    STAMP(5);
#pragma unroll
    for (int m = 0; m < 4; m++) {
      if (m < 3 || lw < 2) {
        const int g = min(t + m * 256, 839), hg = g >= 420, idx = g - hg * 420, dq = idx / 21, ee = idx - dq * 21, hl = dq / 5;
        f4 s = __builtin_bit_cast(f4, pv[m][0]);
#pragma unroll
        for (int i = 1; i < 8; i++) s += __builtin_bit_cast(f4, pv[m][i]);
        const u2 v = u2{pk2(s[0], s[1]), pk2(s[2], s[3])};
        LDSP char* dst = sm + IMG + hg * 25600 + (dq >> 2) * 1024 + (dq & 1) * 512 + ((dq >> 1) & 1) * 8;
        if (ee < 20) {
          const int eg = hl * 20 + ee;
          *(LDSP u2*)(dst + (eg >> 4) * 5120 + (eg & 15) * 16) = v;
        } else {
#pragma unroll
          for (int k = 0; k < 5; k++) {
            const int eg = hl * 20 + k * 4;
            *(LDSP u2*)(dst + (eg >> 4) * 5120 + (eg & 15) * 16 + 256) = v;
          }
        }
      }
    }
    WAITL0(); BAR();
    BAR();
    return;
  }
#pragma unroll
  for (int i = 0; i < 5; i++) bdma(i);
  WAITV(0);
  __builtin_amdgcn_s_setprio(0);
  STAMP(4);
  if (type == 2) { BAR(); kv_publish(sm, rws, rt, cg); WAITL0(); }
  BAR();
  bput(0, 14);
  WAITL0(); BAR();
  bput(14, 30);
  WAITL0(); BAR();
  bput(30, 46);
  WAITL0(); BAR();
  unsigned go[2]; u4 xv[2];
#pragma unroll
  for (int i = 0; i < 2; i++) {
    const int item = i * 256 + t;
    go[i] = (unsigned)((row2 + (item >> 3)) * 1280 + cg2 * 128 + (item & 7) * 16);
    xv[i] = *(const GLBP u4*)(p.x + go[i]);
  }
  BAR();
#pragma unroll
  for (int i = 0; i < 2; i++) p2_out(p, sm, i * 256 + t, xv[i], go[i]);
}

__global__ __launch_bounds__(512, 2) void fused_kernel(Params p) {
  extern __shared__ __attribute__((aligned(16))) char smem[];
  LDSP char* sm = (LDSP char*)smem;
  const int lane = threadIdx.x & 63, w = __builtin_amdgcn_readfirstlane(threadIdx.x >> 6), b = blockIdx.x;
  STAMPR(0); STAMP(1);
  const u64 pc0 = __builtin_amdgcn_s_getpc();
  int type, rt, cg, n2;
  {
    const int c = b & 7, j = b >> 3, a = c & 1, q = c >> 1;
    if (j < 12) {
      rt = 4 * a + j / 3; const int m = j % 3;
      if (m == 0) { type = 1; cg = q; } else { type = 2; cg = 2 * q + m - 1; }
      n2 = (j / 3) * 2 + m - 1;
    } else {
      const int n = j - 12 + (a ? 19 : 0);
      type = (!a && j == 31) ? 3 : 0; rt = n / 3; cg = 3 * q + n % 3;
      n2 = j - 4;
    }
    n2 = max(n2 + q * 55 + a * 27, 0);
  }
  const int row0 = type == 0 ? min(rt * 160, S - 160) : rt * 256;
  const int rt2 = n2 / 10, cg2 = n2 - rt2 * 10, row2 = min(rt2 * 96, S - 96);
  const rsrc_t rws = mkrs(p.ws, WS_END);
  if (type == 3) {
    if (w < 4) return;
    const u64 tag = ld_flag(p.ws, WS_GEN) + 1;
    const bool hascs = (w - 4) * 256 + b < 624;
    P0 d0;
    p0_load(p, d0, w - 4, lane, hascs);
    p0_finish(p, d0, rws, w - 4, lane, hascs);
    WAITV(0); BAR();
    if (w != 4) return;
    if (lane == 0) st_flag(p.ws, WS_F0 + b * 8, tag);
    if (b != 248) return;
#pragma unroll 1
    for (int i = 0; i < 4; i++) wait_flag(p.ws, WS_F0 + (i * 64 + lane) * 8, tag, tag - 1);
    if (lane == 0) st_flag(p.ws, WS_GEN, tag);
    return;
  }
  if (w >= 4) {
    loader(p, sm, rws, w - 4, lane, type, rt, cg, row0, row2, cg2);
  } else {
    const u64 pc = pc0 + threadIdx.x * 128;
    unsigned t0, t1;
    asm volatile("global_load_dword %0, %2, off\n\tglobal_load_dword %1, %3, off"
                 : "=&v"(t0), "=&v"(t1) : "v"(pc), "v"(pc + min(32768u, CODE_BYTES - 128u * threadIdx.x)) : "memory");
    if (threadIdx.x == 0) {
      const u64 tag = ld_flag(p.ws, WS_GEN) + 1;
      *(LDSP u2*)(sm + AUX + 3072) = u2{(unsigned)tag, (unsigned)(tag >> 32)};
    }
    asm volatile("s_waitcnt vmcnt(0) lgkmcnt(0)" : "+v"(t0), "+v"(t1) :: "memory");
    if (type == 0) k1_mfma<0>(p, sm, rws, w, lane, rt, cg, row0);
    else if (type == 1) k1_mfma<1>(p, sm, rws, w, lane, rt, cg, row0);
    else k1_mfma<2>(p, sm, rws, w, lane, rt, cg, row0);
    STAMP(7);
    if (type != 1) p2_mfma(p, sm, rws, w, lane, row2, cg2);
  }
  STAMP(11); STAMPR(2);
}

extern "C" void kernel_launch(void* const* d_in, const int* in_sizes, int n_in, void* d_out, int out_size,
                              void* d_ws, size_t ws_size, hipStream_t stream) {
  Params p;
  p.x = (const char*)d_in[0]; p.wqkv = (const char*)d_in[1]; p.wo = (const char*)d_in[2];
  p.wg = (const char*)d_in[4]; p.wu = (const char*)d_in[5]; p.wd = (const char*)d_in[6];
  p.out = (char*)d_out; p.ws = (char*)d_ws;
  (void)hipFuncSetAttribute((const void*)fused_kernel, hipFuncAttributeMaxDynamicSharedMemorySize, LDS_BYTES);
  fused_kernel<<<256, 512, LDS_BYTES, stream>>>(p);
}
```

```cpp
#include <hip/hip_runtime.h>
#include <hip/hip_bf16.h>
#include <stdint.h>

typedef __attribute__((ext_vector_type(8))) __bf16 bf8;
typedef __attribute__((ext_vector_type(2))) __bf16 bf2;
typedef __attribute__((ext_vector_type(4))) short s4;
typedef __attribute__((ext_vector_type(2))) float f2;
typedef __attribute__((ext_vector_type(4))) float f4;
typedef __attribute__((ext_vector_type(16))) float f16v;
typedef __attribute__((ext_vector_type(2))) unsigned u2;
typedef __attribute__((ext_vector_type(4))) unsigned u4;
typedef unsigned long long u64;
typedef __amdgpu_buffer_rsrc_t rsrc_t;
#define LDSP __attribute__((address_space(3)))
#define GLBP __attribute__((address_space(1)))
#define DI __device__ __forceinline__

constexpr int S = 2048, D = 640, F = 1536;
constexpr int NCOL = 4992;
constexpr unsigned WS_GEN = 0;
constexpr unsigned WS_F0 = 4096;
constexpr unsigned WS_FH = 8192;
constexpr unsigned WS_FKV = 12288;
constexpr unsigned WS_FO = 16384;
constexpr unsigned WS_STATS = 32768;
constexpr unsigned WS_CS = 65536;
constexpr unsigned WS_KVP = 262144;
constexpr unsigned WS_H = 1 << 20;
constexpr unsigned WS_O = WS_H + S * F * 2;
constexpr unsigned WS_END = WS_O + S * D * 2;

constexpr int SLOT = 52 * 1024;
constexpr int LDS_BYTES = 160 * 1024;
constexpr int AUX = 3 * SLOT;
constexpr int IMG = 54 * 1024;
constexpr int SC1 = 16;
constexpr unsigned CODE_BYTES = 53 * 1024;

DI rsrc_t mkrs(const void* b, unsigned n) { return __builtin_amdgcn_make_buffer_rsrc((void*)b, (short)0, (int)n, 0x00020000); }
DI void blds16(rsrc_t rs, unsigned voff, unsigned soff, LDSP char* l) {
  __builtin_amdgcn_raw_ptr_buffer_load_lds(rs, (LDSP void*)l, 16, voff, soff, 0, 0);
}
template <int AUXB> DI u4 bld128(rsrc_t rs, unsigned voff, unsigned soff) { return __builtin_amdgcn_raw_buffer_load_b128(rs, voff, soff, AUXB); }
template <int AUXB> DI u2 bld64(rsrc_t rs, unsigned voff, unsigned soff) { return __builtin_amdgcn_raw_buffer_load_b64(rs, voff, soff, AUXB); }
template <int AUXB> DI unsigned bld32(rsrc_t rs, unsigned voff, unsigned soff) { return __builtin_amdgcn_raw_buffer_load_b32(rs, voff, soff, AUXB); }
DI void bst128(u4 v, rsrc_t rs, unsigned voff, unsigned soff) { __builtin_amdgcn_raw_buffer_store_b128(v, rs, voff, soff, SC1); }
DI void bst64(u2 v, rsrc_t rs, unsigned voff, unsigned soff) { __builtin_amdgcn_raw_buffer_store_b64(v, rs, voff, soff, SC1); }
DI u4 lds_r128(LDSP char* p) { return *(LDSP u4*)p; }
DI u2 lds_tr(LDSP char* p) { return __builtin_bit_cast(u2, __builtin_amdgcn_ds_read_tr16_b64_v4i16((LDSP s4*)p)); }
DI f16v mfma32(u4 a, u4 b, f16v c) { return __builtin_amdgcn_mfma_f32_32x32x16_bf16(__builtin_bit_cast(bf8, a), __builtin_bit_cast(bf8, b), c, 0, 0, 0); }
DI unsigned pk2(float lo, float hi) { f2 v = {lo, hi}; return __builtin_bit_cast(unsigned, __builtin_convertvector(v, bf2)); }
DI float blo(unsigned v) { return __uint_as_float(v << 16); }
DI float bhi(unsigned v) { return __uint_as_float(v & 0xffff0000u); }
DI float exp2c(float u) { return __builtin_amdgcn_fmed3f(__builtin_amdgcn_exp2f(u), 0.f, 1.f); }
constexpr float LOG2E = 1.44269504088896f, LN2 = 0.693147180559945f;
DI unsigned phi2(f2 u) {
  const f2 t = u * LN2 + 1e-6f, e = {exp2c(u[0]), exp2c(u[1])};
  const f2 v = f2{fmaxf(t[0], 1e-6f), fmaxf(t[1], 1e-6f)} + e;
  return pk2(v[0], v[1]);
}
DI u64 ld_flag(const char* ws, unsigned off) { return __hip_atomic_load((const GLBP u64*)(ws + off), __ATOMIC_RELAXED, __HIP_MEMORY_SCOPE_AGENT); }
DI void st_flag(char* ws, unsigned off, u64 v) { __hip_atomic_store((GLBP u64*)(ws + off), v, __ATOMIC_RELAXED, __HIP_MEMORY_SCOPE_AGENT); }
#define BAR() __builtin_amdgcn_s_barrier()
#define WAITV(n) asm volatile("s_waitcnt vmcnt(" #n ")" ::: "memory")
#define WAITL0() asm volatile("s_waitcnt lgkmcnt(0)" ::: "memory")
#define SCHED0() __builtin_amdgcn_sched_barrier(0)
#define FENCE() do { asm volatile("" ::: "memory"); __builtin_amdgcn_sched_barrier(0); } while (0)

#define STAMP(i)
#define STAMPR(i)

struct Params {
  const char* x; const char* wqkv; const char* wo; const char* wg; const char* wu; const char* wd;
  char* out; char* ws;
};

DI void wait_flag(const char* ws, unsigned off, u64 tag, u64 first) {
  u64 v = first;
#pragma unroll 1
  for (int it = 0; it < (1 << 18); it++) {
    if (__all(v == tag)) break;
    __builtin_amdgcn_s_sleep(1);
    v = ld_flag(ws, off);
  }
  FENCE();
}
DI u64 lds_tag(LDSP char* sm) {
  const u2 t = *(LDSP u2*)(sm + AUX + 3072);
  return (u64)(unsigned)__builtin_amdgcn_readfirstlane(t[0]) | ((u64)(unsigned)__builtin_amdgcn_readfirstlane(t[1]) << 32);
}

template <int CTL> DI float dppadd(float v) { return v + __int_as_float(__builtin_amdgcn_update_dpp(0, __float_as_int(v), CTL, 0xf, 0xf, true)); }
DI float swap16add(float v) { const u2 t = __builtin_amdgcn_permlane16_swap(__float_as_uint(v), __float_as_uint(v), false, false); return __uint_as_float(t[0]) + __uint_as_float(t[1]); }
DI float sum32(float v) { return swap16add(dppadd<0x121>(dppadd<0x122>(dppadd<0x124>(dppadd<0x128>(v))))); }
constexpr unsigned EMPTY = 0xffffffffu;

struct P0 { u2 xs[5]; u4 cw[10]; };
DI void p0_load(const Params& p, P0& d, int lw, int lane, bool hascs) {
  const int b = blockIdx.x;
  const rsrc_t rx = mkrs(p.x, S * D * 2);
  const unsigned xo = (unsigned)((b * 8 + lw * 2 + (lane >> 5)) * 1280 + (lane & 31) * 8);
#pragma unroll
  for (int i = 0; i < 5; i++) d.xs[i] = bld64<0>(rx, xo + i * 256, 0);
  if (hascs) {
    const int task = lw * 256 + b, kp = task / 78, cu = task - kp * 78;
    const char* base = cu < 30 ? p.wqkv + cu * 128 : (cu < 54 ? p.wg + (cu - 30) * 128 : p.wu + (cu - 54) * 128);
    const unsigned stride = cu < 30 ? 3840 : 3072;
    const rsrc_t rw = mkrs(base, 640 * stride - 127);
    const unsigned wo = (unsigned)(kp * 80 + (lane >> 3)) * stride + (lane & 7) * 16;
#pragma unroll
    for (int i = 0; i < 10; i++) d.cw[i] = bld128<0>(rw, wo, i * 8 * stride);
  }
}
DI void p0_finish(const Params& p, P0& d, rsrc_t rws, int lw, int lane, bool hascs) {
  const int b = blockIdx.x;
  if (lw * 64 + lane < 105) bst128(u4{EMPTY, EMPTY, EMPTY, EMPTY}, rws, ((b & 3) * 105 + lw * 64 + lane) * 16, WS_KVP + (b >> 2) * 7168);
#pragma unroll
  for (int i = 0; i < 5; i++) asm volatile("" : "+v"(d.xs[i]));
  float s = 0;
#pragma unroll
  for (int i = 0; i < 5; i++) s += (blo(d.xs[i][0]) + bhi(d.xs[i][0])) + (blo(d.xs[i][1]) + bhi(d.xs[i][1]));
  s = sum32(s);
  const float mu = s * (1.f / 640.f);
  float q = 0;
#pragma unroll
  for (int i = 0; i < 5; i++) {
    const float a = blo(d.xs[i][0]) - mu, c = bhi(d.xs[i][0]) - mu, e = blo(d.xs[i][1]) - mu, g = bhi(d.xs[i][1]) - mu;
    q += a * a + c * c + e * e + g * g;
  }
  q = sum32(q);
  const float rs = rsqrtf(q * (1.f / 640.f) + 1e-5f);
  if ((lane & 31) == 0) bst64(u2{__float_as_uint(mu), __float_as_uint(rs)}, rws, (b * 8 + lw * 2 + (lane >> 5)) * 8, WS_STATS);
  if (hascs) {
    const int task = lw * 256 + b, kp = task / 78, cu = task - kp * 78;
#pragma unroll
    for (int i = 0; i < 10; i++) asm volatile("" : "+v"(d.cw[i]));
    float c[8];
#pragma unroll
    for (int j = 0; j < 8; j++) c[j] = 0;
#pragma unroll
    for (int i = 0; i < 10; i++)
#pragma unroll
      for (int j = 0; j < 4; j++) { c[2 * j] += blo(d.cw[i][j]); c[2 * j + 1] += bhi(d.cw[i][j]); }
    float e[4];
#pragma unroll
    for (int j = 0; j < 4; j++) {
      const u2 t = __builtin_amdgcn_permlane32_swap(__float_as_uint(c[j]), __float_as_uint(c[4 + j]), false, false);
      e[j] = dppadd<0x128>(swap16add(__uint_as_float(t[0]) + __uint_as_float(t[1])));
    }
    if ((lane & 24) == 0)
      bst128(u4{__float_as_uint(e[0]), __float_as_uint(e[1]), __float_as_uint(e[2]), __float_as_uint(e[3])}, rws,
             (kp * NCOL + cu * 64 + (lane & 7) * 8 + (lane >> 5) * 4) * 4, WS_CS);
  }
}

template <int TYPE>
DI void k1_main(LDSP char* sm, int w, int lane, f16v (&acc)[10]) {
  constexpr int NXF = TYPE == 0 ? 5 : 2, NWF = 7 - NXF;
  constexpr int XB = TYPE == 0 ? 160 * 128 : 256 * 128;
  const int r = lane & 31, hh = lane >> 5, blk = (lane >> 4) & 1, q = (lane & 15) >> 2, pp = lane & 3;
  unsigned xa[4];
#pragma unroll
  for (int k = 0; k < 4; k++) xa[k] = (unsigned)(((TYPE == 0 ? 0 : w * 64) + r) * 128 + (((2 * k + hh) ^ ((r >> 1) & 7)) << 4));
  unsigned wa[2];
  if (TYPE == 0) {
#pragma unroll
    for (int ft = 0; ft < 2; ft++)
      wa[ft] = (unsigned)(XB + blk * 16384 + (8 * hh + q) * 256 + (((w * 4 + ft * 2 + (pp >> 1)) ^ (q << 2) ^ (blk << 1)) << 4) + (pp & 1) * 8);
  } else {
    wa[0] = (unsigned)(XB + (8 * hh + q) * 320 + blk * 32 + pp * 8); wa[1] = 0;
  }
  u4 xf[2][NXF], wf[2][NWF];
  auto rd = [&](int buf, LDSP char* base, int k) {
#pragma unroll
    for (int i = 0; i < NWF; i++) {
      u2 lo, hi;
      if (TYPE == 0) { lo = lds_tr(base + wa[i] + k * 4096); hi = lds_tr(base + wa[i] + k * 4096 + 1024); }
      else { lo = lds_tr(base + wa[0] + k * 5120 + i * 64); hi = lds_tr(base + wa[0] + k * 5120 + i * 64 + 1280); }
      wf[buf][i] = u4{lo[0], lo[1], hi[0], hi[1]};
    }
#pragma unroll
    for (int i = 0; i < NXF; i++) xf[buf][i] = lds_r128(base + xa[k] + i * 4096);
  };
  auto mm = [&](int buf) {
#pragma unroll
    for (int i = 0; i < NXF; i++)
#pragma unroll
      for (int j = 0; j < NWF; j++) {
        if (TYPE == 2) acc[i * NWF + j] = mfma32(xf[buf][i], wf[buf][j], acc[i * NWF + j]);
        else acc[i * NWF + j] = mfma32(wf[buf][j], xf[buf][i], acc[i * NWF + j]);
      }
  };
  auto inter = [&]() {
    constexpr int NR = NXF + 2 * NWF;
#pragma unroll
    for (int i = 0; i < 6; i++) {
      __builtin_amdgcn_sched_group_barrier(0x8, 1, 0);
      __builtin_amdgcn_sched_group_barrier(0x100, (NR + 5) / 6, 0);
    }
    __builtin_amdgcn_sched_group_barrier(0x8, 4, 0);
  };
  BAR();
  rd(0, sm, 0);
  int slot = 0;
#pragma unroll 1
  for (int s = 0; s < 9; s++) {
    LDSP char* base = sm + slot * SLOT;
    slot = slot == 2 ? 0 : slot + 1;
    SCHED0();
    rd(1, base, 1); mm(0); inter(); SCHED0();
    rd(0, base, 2); mm(1); inter(); SCHED0();
    rd(1, base, 3); mm(0); inter(); SCHED0();
    WAITL0(); BAR(); SCHED0();
    rd(0, sm + slot * SLOT, 0); mm(1); inter(); SCHED0();
  }
  SCHED0();
  rd(1, sm, 1); mm(0); inter(); SCHED0();
  rd(0, sm, 2); mm(1); inter(); SCHED0();
  rd(1, sm, 3); mm(0); inter(); SCHED0();
  mm(1); SCHED0();
}

DI void kv_publish(LDSP char* sm, rsrc_t rws, int rt, int cg) {
  const int t = threadIdx.x;
  if (t < 420) {
    LDSP char* src = sm + t * 16;
    const f4 s = (*(LDSP f4*)src + *(LDSP f4*)(src + 8192)) + (*(LDSP f4*)(src + 16384) + *(LDSP f4*)(src + 24576));
    u4 v = __builtin_bit_cast(u4, s);
#pragma unroll
    for (int j = 0; j < 4; j++) v[j] = min(v[j], EMPTY - 1);
    bst128(v, rws, t * 16, WS_KVP + (rt * 8 + cg) * 7168);
  }
}

template <int TYPE>
DI void k1_mfma(const Params& p, LDSP char* sm, rsrc_t rws, int w, int lane, int rt, int cg, int row0) {
  const int hh = lane >> 5, r = lane & 31;
  f16v acc[10];
#pragma unroll
  for (int i = 0; i < 10; i++)
#pragma unroll
    for (int j = 0; j < 16; j++) acc[i][j] = 0.f;
  k1_main<TYPE>(sm, w, lane, acc);
  STAMP(3);
  if (TYPE == 0) {
    f4 cs[2][4];
#pragma unroll
    for (int ft = 0; ft < 2; ft++)
#pragma unroll
      for (int qd = 0; qd < 4; qd++)
        cs[ft][qd] = *(LDSP f4*)(sm + AUX + 2048 + ((qd >> 1) * 128 + w * 32 + ft * 16 + (qd & 1) * 8 + hh * 4) * 4);
    const unsigned dst = (unsigned)(((((row0 + r) >> 3) * 48 + cg * 4 + w) * 4 + hh) * 128 + (r & 7) * 16);
#pragma unroll
    for (int tt = 0; tt < 5; tt++) {
      const float mu = *(LDSP float*)(sm + AUX + (tt * 32 + r) * 4);
      if (tt == 0 && rt == 12) continue;
#pragma unroll
      for (int ft = 0; ft < 2; ft++) {
        const f16v a = acc[tt * 2 + ft];
        float h[8];
#pragma unroll
        for (int j = 0; j < 8; j++) h[j] = (a[j] - mu * cs[ft][j >> 2][j & 3]) * (a[8 + j] - mu * cs[ft][2 + (j >> 2)][j & 3]);
        bst128(u4{pk2(h[0], h[1]), pk2(h[2], h[3]), pk2(h[4], h[5]), pk2(h[6], h[7])}, rws, dst + ft * 256, WS_H + tt * 4 * 24576);
      }
    }
    STAMP(4);
    FENCE(); BAR();
    WAITV(0); BAR();
    if (threadIdx.x == 0) st_flag(p.ws, WS_FH + (rt * 16 + cg) * 8, lds_tag(sm));
  } else if (TYPE == 1) {
    u4 pq[2][10];
#pragma unroll
    for (int tt = 0; tt < 2; tt++) {
      const float mu = *(LDSP float*)(sm + AUX + (w * 64 + tt * 32 + r) * 4);
      const float rl = *(LDSP float*)(sm + AUX + 1024 + (w * 64 + tt * 32 + r) * 4) * LOG2E;
#pragma unroll
      for (int ft = 0; ft < 5; ft++) {
        const f16v a = acc[tt * 5 + ft];
#pragma unroll
        for (int qd = 0; qd < 4; qd++) {
          const f4 cs = *(LDSP f4*)(sm + AUX + 2048 + (ft * 32 + qd * 8 + hh * 4) * 4);
#pragma unroll
          for (int j = 0; j < 2; j++) {
            const f2 u = (f2{a[qd * 4 + 2 * j], a[qd * 4 + 2 * j + 1]} - f2{cs[2 * j], cs[2 * j + 1]} * mu) * rl;
            pq[tt][ft * 2 + (qd >> 1)][(qd & 1) * 2 + j] = phi2(u);
          }
        }
        asm volatile("" : "+v"(pq[tt][ft * 2]), "+v"(pq[tt][ft * 2 + 1]));
      }
    }
    STAMP(4);
    SCHED0(); BAR(); SCHED0();
    STAMP(5);
    const unsigned dst = (unsigned)((((row0 + w * 64 + r) >> 3) * 20 + cg * 5) * 512 + hh * 128 + (r & 7) * 16);
    f16v oa[2][2];
    u4 af[2][3];
    auto rdi = [&](int n) {
      const int hg = n / 5, T = n % 5, k0 = T < 2 ? 0 : T - 1, k1 = T == 0 ? 1 : (T == 4 ? 4 : T + 1);
#pragma unroll
      for (int k = k0; k <= k1; k++) af[n & 1][k - k0] = lds_r128(sm + IMG + (hg * 25 + T * 5 + k) * 1024 + lane * 16);
    };
    auto omm = [&](int n) {
      const int hg = n / 5, T = n % 5, k0 = T < 2 ? 0 : T - 1, k1 = T == 0 ? 1 : (T == 4 ? 4 : T + 1);
#pragma unroll
      for (int tt = 0; tt < 2; tt++)
#pragma unroll
        for (int j = 0; j < 16; j++) oa[n & 1][tt][j] = 0.f;
#pragma unroll
      for (int k = k0; k <= k1; k++)
#pragma unroll
        for (int tt = 0; tt < 2; tt++) oa[n & 1][tt] = mfma32(af[n & 1][k - k0], pq[tt][hg * 5 + k], oa[n & 1][tt]);
    };
    rdi(0); rdi(1); omm(0); SCHED0();
#pragma unroll
    for (int n = 0; n < 10; n++) {
      if (n + 1 < 10) omm(n + 1);
      if (n + 2 < 10) rdi(n + 2);
#pragma unroll
      for (int tt = 0; tt < 2; tt++) {
        const f16v a = oa[n & 1][tt];
        const float d0 = __builtin_amdgcn_rcpf(a[8] + 1e-6f), d1 = __builtin_amdgcn_rcpf(a[12] + 1e-6f);
        bst128(u4{pk2(a[0] * d0, a[1] * d0), pk2(a[2] * d0, a[3] * d0), pk2(a[4] * d1, a[5] * d1), pk2(a[6] * d1, a[7] * d1)},
               rws, dst + n * 256, WS_O + tt * 4 * 10240);
      }
#pragma unroll
      for (int i = 0; i < 6; i++) { __builtin_amdgcn_sched_group_barrier(0x8, 1, 0); __builtin_amdgcn_sched_group_barrier(0x2, 5, 0); }
      SCHED0();
    }
    STAMP(6);
    WAITV(0); BAR();
    if (threadIdx.x == 0) st_flag(p.ws, WS_FO + (rt * 16 + cg) * 8, lds_tag(sm));
  } else {
    float cs[5];
#pragma unroll
    for (int ct = 0; ct < 5; ct++) cs[ct] = *(LDSP float*)(sm + AUX + 2048 + (ct * 32 + r) * 4);
    const bool isk = r < 16;
    const float kmul = isk ? 1.f : 0.f, kadd = isk ? 1e-6f : 0.f, klo = isk ? 1e-6f : -__builtin_inff();
    u4 fr[4][5];
#pragma unroll
    for (int g = 0; g < 4; g++) {
      f2 mu[4], rs[4], rl[4];
#pragma unroll
      for (int hq = 0; hq < 2; hq++) {
        LDSP char* sp = sm + AUX + (w * 64 + g * 16 + hq * 8 + hh * 4) * 4;
        const f4 a = *(LDSP f4*)sp, b = *(LDSP f4*)(sp + 1024);
        mu[hq * 2] = f2{a[0], a[1]}; mu[hq * 2 + 1] = f2{a[2], a[3]};
        rs[hq * 2] = f2{b[0], b[1]}; rs[hq * 2 + 1] = f2{b[2], b[3]};
      }
#pragma unroll
      for (int j = 0; j < 4; j++) rl[j] = rs[j] * LOG2E;
#pragma unroll
      for (int ct = 0; ct < 5; ct++) {
#pragma unroll
        for (int j = 0; j < 4; j++) {
          const f16v& a = acc[(g >> 1) * 5 + ct];
          const f2 y = f2{a[(g & 1) * 8 + 2 * j], a[(g & 1) * 8 + 2 * j + 1]} - mu[j] * cs[ct];
          if (ct < 2) fr[g][ct][j] = phi2(y * rl[j]);
          else if (ct == 2) {
            const f2 u = y * rl[j], t = y * rs[j] + kadd, e = {exp2c(u[0]), exp2c(u[1])};
            const f2 v = e * kmul + f2{fmaxf(t[0], klo), fmaxf(t[1], klo)};
            fr[g][ct][j] = pk2(v[0], v[1]);
          } else { const f2 v = y * rs[j]; fr[g][ct][j] = pk2(v[0], v[1]); }
        }
      }
      SCHED0();
    }
    f16v z[8];
#pragma unroll
    for (int i = 0; i < 8; i++)
#pragma unroll
      for (int j = 0; j < 16; j++) z[i][j] = 0.f;
#pragma unroll
    for (int g = 0; g < 4; g++) {
      u4 mb;
#pragma unroll
      for (int j = 0; j < 4; j++) mb[j] = isk ? 0x3f803f80u : fr[g][2][j];
      z[0] = mfma32(fr[g][0], fr[g][3], z[0]);
      z[1] = mfma32(fr[g][0], fr[g][4], z[1]);
      z[2] = mfma32(fr[g][0], mb, z[2]);
      z[3] = mfma32(fr[g][1], fr[g][3], z[3]);
      z[4] = mfma32(fr[g][1], fr[g][4], z[4]);
      z[5] = mfma32(fr[g][1], mb, z[5]);
      z[6] = mfma32(fr[g][2], fr[g][4], z[6]);
      z[7] = mfma32(fr[g][2], mb, z[7]);
    }
    unsigned pa[3], pm[3];
#pragma unroll
    for (int vt = 0; vt < 3; vt++) {
      const int e = vt == 2 ? 48 + r : vt * 32 + r, he = e / 20;
      int ee = e - he * 20; unsigned m = 31u << (5 * he);
      if (vt == 2 && isk) { ee = 20; m = r == 0 ? 0xfffffu : 0u; }
      pa[vt] = (unsigned)(w * 8192 + ee * 16 + hh * 336); pm[vt] = m >> hh;
    }
    SCHED0();
#pragma unroll
    for (int i = 0; i < 8; i++)
#pragma unroll
      for (int qd = 0; qd < 4; qd++) {
        const int kt = i / 3, vt = i < 6 ? i % 3 : i - 5, c = kt * 8 + qd * 2;
        const bool need = (i == 1) ? qd >= 2 : (i == 3) ? qd == 0 : (i >= 6) ? qd < 2 : true;
        if (need && (pm[vt] & (1u << c)))
          *(LDSP f4*)(sm + pa[vt] + c * 336) = f4{z[i][qd * 4], z[i][qd * 4 + 1], z[i][qd * 4 + 2], z[i][qd * 4 + 3]};
      }
    STAMP(4);
    WAITL0(); BAR();
    kv_publish(sm, rws, rt, cg);
    WAITL0(); BAR();
  }
}

DI void p2_out(const Params& p, LDSP char* sm, int item, u4 xv, unsigned go) {
  const int row = item >> 3, c = item & 7;
  LDSP char* src = sm + (row >> 3) * 8192 + (row & 7) * 256;
  const unsigned c0 = ((2 * c) ^ (row & 7)) << 4, c1 = c0 ^ 16;
  f4 a = *(LDSP f4*)(src + c0), b = *(LDSP f4*)(src + c1);
#pragma unroll
  for (int i = 1; i < 4; i++) { a += *(LDSP f4*)(src + i * 2048 + c0); b += *(LDSP f4*)(src + i * 2048 + c1); }
  *(GLBP u4*)(p.out + go) = u4{pk2(blo(xv[0]) + a[0], bhi(xv[0]) + a[1]), pk2(blo(xv[1]) + a[2], bhi(xv[1]) + a[3]),
                               pk2(blo(xv[2]) + b[0], bhi(xv[2]) + b[1]), pk2(blo(xv[3]) + b[2], bhi(xv[3]) + b[3])};
}

constexpr int PD = 8;
DI void p2_mfma(const Params& p, LDSP char* sm, rsrc_t rws, int w, int lane, int row2, int cg2) {
  const int hh = lane >> 5, r = lane & 31, blk = (lane >> 4) & 1, q = (lane & 15) >> 2, pp = lane & 3;
  const u64 tag = lds_tag(sm);
  const unsigned fh = WS_FH + (((row2 + ((lane >> 2) & 1) * 95) / 160) * 16 + 3 * w + min(lane & 3, 2)) * 8;
  const unsigned fo = WS_FO + (((row2 + (lane & 1) * 95) >> 8) * 16 + w) * 8;
  float r2[3];
  wait_flag(p.ws, fh, tag, ld_flag(p.ws, fh));
  STAMP(8);
  const unsigned oct = (unsigned)(row2 + r) >> 3, in = hh * 128 + (r & 7) * 16;
  const unsigned ah = oct * 24576 + w * 6144 + in, ao = oct * 10240 + w * 2560 + in;
  u4 af[PD][3], bf[2][2];
  auto lda = [&](int i) {
#pragma unroll
    for (int tt = 0; tt < 3; tt++) {
      if (i < 24) af[i % PD][tt] = bld128<SC1>(rws, ah + (i & 15) * 256, WS_H + tt * 98304 + (i >> 4) * 4096);
      else af[i % PD][tt] = bld128<SC1>(rws, ao + (i - 24) * 256, WS_O + tt * 40960);
    }
  };
  LDSP char* ba[2][3];
#pragma unroll
  for (int ct = 0; ct < 2; ct++)
#pragma unroll
    for (int sg = 0; sg < 3; sg++) {
      ba[ct][sg] = sm + sg * 49152 + w * 2048 + (4 * hh + q) * 128 + (((ct * 4 + blk * 2 + (pp >> 1)) ^ ((q >> 1) << 2)) << 4) + (pp & 1) * 8;
      asm volatile("" : "+v"(ba[ct][sg]));
    }
  auto ldb = [&](int i) {
    const int sl = 17 - (i < 18 ? i : i - 18);
#pragma unroll
    for (int ct = 0; ct < 2; ct++) {
      LDSP char* base = ba[ct][sl / 6] + (sl % 6) * 8192;
      const u2 lo = lds_tr(base), hi = lds_tr(base + 1024);
      bf[i & 1][ct] = u4{lo[0], lo[1], hi[0], hi[1]};
    }
  };
  f16v acc[6];
#pragma unroll
  for (int i = 0; i < 6; i++)
#pragma unroll
    for (int j = 0; j < 16; j++) acc[i][j] = 0.f;
#pragma unroll
  for (int i = 0; i < PD; i++) lda(i);
  ldb(0);
  u64 ofl = 0;
  constexpr int OCHK = 20;
#pragma unroll
  for (int i = 0; i < 34; i++) {
    SCHED0();
    if (i == 7 || i == 15 || i == 23) { STAMP(12 + i / 4); WAITL0(); BAR(); STAMP(13 + i / 4); SCHED0(); }
    if (i == OCHK) {
      STAMP(19);
      wait_flag(p.ws, fo, tag, ofl);
      STAMP(9);
#pragma unroll
      for (int j = 24; j < OCHK + PD; j++) lda(j);
      SCHED0();
    }
    if (i == 24) {
#pragma unroll
      for (int tt = 0; tt < 3; tt++) { acc[tt * 2] *= r2[tt] * r2[tt]; acc[tt * 2 + 1] *= r2[tt] * r2[tt]; }
      SCHED0();
    }
    if (i + 1 < 34) ldb(i + 1);
#pragma unroll
    for (int tt = 0; tt < 3; tt++)
#pragma unroll
      for (int ct = 0; ct < 2; ct++) acc[tt * 2 + ct] = mfma32(bf[i & 1][ct], af[i % PD][tt], acc[tt * 2 + ct]);
    SCHED0();
    if (i + PD < 24 || (i >= OCHK && i + PD < 34)) lda(i + PD);
    if (i + PD == 24) {
#pragma unroll
      for (int tt = 0; tt < 3; tt++) r2[tt] = __uint_as_float(bld32<SC1>(rws, (row2 + tt * 32 + r) * 8 + 4, WS_STATS));
      ofl = ld_flag(p.ws, fo);
    }
  }
  STAMP(10);
  const int item = 512 + threadIdx.x;
  const unsigned go = (unsigned)((row2 + (item >> 3)) * 1280 + cg2 * 128 + (item & 7) * 16);
  const u4 xv = *(const GLBP u4*)(p.x + go);
#pragma unroll
  for (int tt = 0; tt < 3; tt++)
#pragma unroll
    for (int ct = 0; ct < 2; ct++)
#pragma unroll
      for (int qd = 0; qd < 4; qd++)
        *(LDSP f4*)(sm + (tt * 4 + (r >> 3)) * 8192 + w * 2048 + (r & 7) * 256 + (((ct * 8 + qd * 2 + hh) ^ (r & 7)) << 4)) =
            f4{acc[tt * 2 + ct][qd * 4], acc[tt * 2 + ct][qd * 4 + 1], acc[tt * 2 + ct][qd * 4 + 2], acc[tt * 2 + ct][qd * 4 + 3]};
  WAITL0(); BAR();
  p2_out(p, sm, item, xv, go);
}

template <bool gu>
DI void loader(const Params& p, LDSP char* sm, rsrc_t rws, int lw, int lane, int type, int rt, int cg, int row0, int row2, int cg2) {
  const int b = blockIdx.x, t = lw * 64 + lane;
  __builtin_amdgcn_s_setprio(3);
  unsigned vo[13];
  {
    const int x0 = gu ? lw * 5 : lw * 8;
    const unsigned xr = (unsigned)((row0 + x0 * 8 + (lane >> 3)) * 1280), xs = (unsigned)(((lane & 7) ^ (lane >> 4) ^ ((x0 & 1) << 2)) << 4);
#pragma unroll
    for (int i = 0; i < 8; i++) vo[i] = xr + (xs ^ ((i & 1) << 6)) + i * 10240;
    if (gu) {
      const unsigned g0 = (unsigned)(((lw & 1) * 32 + (lane >> 4)) * 3072 + (((lane & 15) ^ ((lane >> 4) << 2) ^ (lw & 2)) << 4));
#pragma unroll
      for (int i = 5; i < 13; i++) vo[i] = g0 + (i - 5) * 12288;
    } else {
#pragma unroll
      for (int i = 0; i < 5; i++) {
        const int e = i * 64 + lane, rq = (e * 205) >> 12, pos = e - rq * 20;
        const int adj = type == 1 ? 0 : (pos < 10 ? 0 : (pos < 12 ? 1248 : 1088));
        vo[8 + i] = (unsigned)((lw * 16 + rq) * 3840 + pos * 16 + adj);
      }
    }
  }
  const unsigned wofs = gu ? cg * 256 : (type == 1 ? cg * 320 : 1280 + cg * 160);
  const char* wbase = (gu ? ((lw >> 1) ? p.wu : p.wg) : p.wqkv) + wofs;
  const unsigned wstep = gu ? 64 * 3072 : 64 * 3840, wlen = wstep * 10 - wofs, mstep = gu ? wstep : 128;
  const rsrc_t rx = mkrs(p.x, S * D * 2), rw = mkrs(wbase, wlen), rm = mkrs(gu ? wbase : p.x, gu ? wlen : S * D * 2);
  const int dx = gu ? lw * 5120 : lw * 8192, dm = gu ? 20480 + lw * 8192 : dx + 5120, dw = gu ? dm + 3072 : 32768 + lw * 5120;
  auto issue = [&](int s) {
    LDSP char* d = sm + (s % 3) * SLOT;
#pragma unroll
    for (int i = 0; i < 5; i++) blds16(rx, vo[i], s * 128, d + dx + i * 1024);
#pragma unroll
    for (int i = 0; i < 3; i++) blds16(rm, vo[5 + i], s * mstep, d + dm + i * 1024);
#pragma unroll
    for (int i = 0; i < 5; i++) blds16(rw, vo[8 + i], s * wstep, d + dw + i * 1024);
    FENCE();
  };
  issue(0); issue(1);
  STAMP(12);
  WAITV(13); BAR(); STAMP(13);
  issue(2);
  const bool hascs = lw * 256 + b < 624;
  P0 d0;
  p0_load(p, d0, lw, lane, hascs);
  const rsrc_t rd_ = mkrs(p.wd, type == 1 ? 0 : F * D * 2), ro_ = mkrs(p.wo, type == 1 ? 0 : D * D * 2);
  const unsigned bo = (unsigned)((lane >> 3) * 1280 + cg2 * 128 + (((lane & 7) ^ (((lane >> 4) & 1) << 2)) << 4));
  auto bdma = [&](int i) {
#pragma unroll
    for (int h = 0; h < 2; h++) blds16(rd_, bo, (lw * 384 + i * 16 + h * 8) * 1280, sm + (17 - i) * 8192 + lw * 2048 + h * 1024);
  };
  u4 br[46];
  auto breg = [&](int i0, int i1) {
#pragma unroll
    for (int i = i0; i < i1; i++) {
      const int st = 11 + (i >> 1);
      br[i] = st < 24 ? bld128<0>(rd_, bo, (lw * 384 + st * 16 + (i & 1) * 8) * 1280) : bld128<0>(ro_, bo, (lw * 160 + (st - 24) * 16 + (i & 1) * 8) * 1280);
    }
    FENCE();
  };
  auto bput = [&](int i0, int i1) {
#pragma unroll
    for (int i = i0; i < i1; i++) {
      const int st = 11 + (i >> 1), sl = st < 18 ? st : st - 18;
      *(LDSP u4*)(sm + (17 - sl) * 8192 + lw * 2048 + (i & 1) * 1024 + lane * 16) = br[i];
    }
  };
  auto bgu = [&](int i0, int i1) { if (gu) breg(i0, i1); };
#define WAITG(a, b) do { if (gu) WAITV(a); else WAITV(b); } while (0)
  FENCE();
  if (hascs) WAITV(28); else WAITV(18);
  BAR(); STAMP(14);
  issue(3); bgu(0, 4);
  if (hascs) WAITG(32, 28); else WAITG(22, 18);
  BAR(); STAMP(19);
  issue(4); p0_finish(p, d0, rws, lw, lane, hascs);
  STAMP(15); WAITG(17, 13); BAR();
  issue(5); bgu(4, 8);
  unsigned sto = min(row0 + t, S - 1) * 8, cso;
  {
    const int tc = gu ? t : min(t, 159);
    cso = 4 * (gu ? 1920 + (tc >> 7) * 1536 + cg * 128 + (tc & 127)
                  : (type == 1 ? cg * 160 + tc : cg * 80 + tc + (tc < 80 ? 640 : (tc < 96 ? 1264 : 1184))));
  }
  asm volatile("" : "+v"(sto), "+v"(cso));
  WAITG(17, 13); BAR();
  STAMP(16);
  const u64 tag = lds_tag(sm);
  if (t == 0) st_flag(p.ws, WS_F0 + b * 8, tag);
  FENCE();
  issue(6); bgu(8, 12); WAITG(21, 13); BAR(); STAMP(6);
  issue(7); bgu(12, 16); WAITG(21, 13); BAR(); STAMP(7);
  issue(8);
  u64 f0[4];
#pragma unroll
  for (int i = 0; i < 4; i++) f0[i] = ld_flag(p.ws, WS_F0 + (i * 64 + lane) * 8);
  FENCE();
  bgu(16, 19);
  WAITG(24, 17); BAR(); STAMP(17);
  issue(9);
  if (!__all(f0[0] == tag && f0[1] == tag && f0[2] == tag && f0[3] == tag)) {
#pragma unroll 1
    for (int i = 0; i < 4; i++) wait_flag(p.ws, WS_F0 + (i * 64 + lane) * 8, tag, tag - 1);
  }
  FENCE();
  const u2 stv = bld64<SC1>(rws, sto, WS_STATS);
  float csv[8];
#pragma unroll
  for (int i = 0; i < 8; i++) csv[i] = __uint_as_float(bld32<SC1>(rws, cso, WS_CS + i * NCOL * 4));
  FENCE();
  bgu(19, 22);
  BAR(); STAMP(18);
#pragma unroll
  for (int i = 5; i < 11; i++) bdma(i);
  FENCE();
  bgu(22, 28);
  if (!gu) *(LDSP u2*)(sm + IMG + t * 8) = u2{0, 0};
  *(LDSP unsigned*)(sm + AUX + t * 4) = stv[0];
  *(LDSP unsigned*)(sm + AUX + 1024 + t * 4) = stv[1];
  *(LDSP float*)(sm + AUX + 2048 + t * 4) = ((csv[0] + csv[1]) + (csv[2] + csv[3])) + ((csv[4] + csv[5]) + (csv[6] + csv[7]));
  if (type == 1) WAITV(0);
  WAITG(21, 12); WAITL0(); BAR();
  STAMP(3);
  if (type == 1) {
    {
      const unsigned ho = WS_KVP + ((lane & 7) * 8 + cg * 2 + ((lane >> 3) & 1)) * 7168 + 419 * 16;
#pragma unroll 1
      for (int it = 0; it < (1 << 18); it++) {
        if (__any(bld32<SC1>(rws, ho, 0) != EMPTY)) break;
        FENCE();
      }
    }
    STAMP(4);
    u4 pv[4][8];
    unsigned off[4];
#pragma unroll
    for (int m = 0; m < 4; m++) { const int g = min(t + m * 256, 839), hg = g >= 420; off[m] = hg * 7168 + (g - hg * 420) * 16; }
    unsigned need = 255;
#pragma unroll 1
    for (int it = 0; it < (1 << 18) && need; it++) {
#pragma unroll
      for (int i = 0; i < 8; i++)
        if (need >> i & 1) {
#pragma unroll
          for (int m = 0; m < 3; m++) pv[m][i] = bld128<SC1>(rws, off[m], WS_KVP + (i * 8 + cg * 2) * 7168);
          if (lw < 2) pv[3][i] = bld128<SC1>(rws, off[3], WS_KVP + (i * 8 + cg * 2) * 7168);
          else pv[3][i] = u4{0, 0, 0, 0};
        }
      FENCE();
#pragma unroll
      for (int i = 0; i < 8; i++)
        if (need >> i & 1) {
          unsigned mx = 0;
#pragma unroll
          for (int m = 0; m < 4; m++) mx = max(max(mx, max(pv[m][i][0], pv[m][i][1])), max(pv[m][i][2], pv[m][i][3]));
          if (!__any(mx == EMPTY)) need &= ~(1u << i);
        }
      FENCE();
    }
    STAMP(5);
#pragma unroll
    for (int m = 0; m < 4; m++) {
      if (m < 3 || lw < 2) {
        const int g = min(t + m * 256, 839), hg = g >= 420, idx = g - hg * 420, dq = idx / 21, ee = idx - dq * 21, hl = dq / 5;
        f4 s = __builtin_bit_cast(f4, pv[m][0]);
#pragma unroll
        for (int i = 1; i < 8; i++) s += __builtin_bit_cast(f4, pv[m][i]);
        const u2 v = u2{pk2(s[0], s[1]), pk2(s[2], s[3])};
        LDSP char* dst = sm + IMG + hg * 25600 + (dq >> 2) * 1024 + (dq & 1) * 512 + ((dq >> 1) & 1) * 8;
        if (ee < 20) {
          const int eg = hl * 20 + ee;
          *(LDSP u2*)(dst + (eg >> 4) * 5120 + (eg & 15) * 16) = v;
        } else {
#pragma unroll
          for (int k = 0; k < 5; k++) {
            const int eg = hl * 20 + k * 4;
            *(LDSP u2*)(dst + (eg >> 4) * 5120 + (eg & 15) * 16 + 256) = v;
          }
        }
      }
    }
    WAITL0(); BAR();
    BAR();
    return;
  }
#pragma unroll
  for (int i = 0; i < 5; i++) bdma(i);
  FENCE();
  __builtin_amdgcn_s_setprio(0);
  if (gu) {
    breg(28, 34);
    BAR();
    breg(34, 46);
    WAITV(18);
    STAMP(4);
  } else {
    breg(0, 46);
    WAITV(46);
    STAMP(4);
    BAR(); kv_publish(sm, rws, rt, cg); WAITL0();
  }
  BAR();
  bput(0, 14);
  WAITL0(); BAR();
  bput(14, 30);
  WAITL0(); BAR();
  bput(30, 46);
  WAITL0(); BAR();
  unsigned go[2]; u4 xv[2];
#pragma unroll
  for (int i = 0; i < 2; i++) {
    const int item = i * 256 + t;
    go[i] = (unsigned)((row2 + (item >> 3)) * 1280 + cg2 * 128 + (item & 7) * 16);
    xv[i] = *(const GLBP u4*)(p.x + go[i]);
  }
  BAR();
#pragma unroll
  for (int i = 0; i < 2; i++) p2_out(p, sm, i * 256 + t, xv[i], go[i]);
}

__global__ __launch_bounds__(512, 2) void fused_kernel(Params p) {
  extern __shared__ __attribute__((aligned(16))) char smem[];
  LDSP char* sm = (LDSP char*)smem;
  const int lane = threadIdx.x & 63, w = __builtin_amdgcn_readfirstlane(threadIdx.x >> 6), b = blockIdx.x;
  STAMPR(0); STAMP(1);
  const u64 pc0 = __builtin_amdgcn_s_getpc();
  int type, rt, cg, n2;
  {
    const int c = b & 7, j = b >> 3, a = c & 1, q = c >> 1;
    if (j < 12) {
      rt = 4 * a + j / 3; const int m = j % 3;
      if (m == 0) { type = 1; cg = q; } else { type = 2; cg = 2 * q + m - 1; }
      n2 = (j / 3) * 2 + m - 1;
    } else {
      const int n = j - 12 + (a ? 19 : 0);
      type = (!a && j == 31) ? 3 : 0; rt = n / 3; cg = 3 * q + n % 3;
      n2 = j - 4;
    }
    n2 = max(n2 + q * 55 + a * 27, 0);
  }
  const int row0 = type == 0 ? min(rt * 160, S - 160) : rt * 256;
  const int rt2 = n2 / 10, cg2 = n2 - rt2 * 10, row2 = min(rt2 * 96, S - 96);
  const rsrc_t rws = mkrs(p.ws, WS_END);
  if (type == 3) {
    if (w < 4) return;
    const u64 tag = ld_flag(p.ws, WS_GEN) + 1;
    const bool hascs = (w - 4) * 256 + b < 624;
    P0 d0;
    p0_load(p, d0, w - 4, lane, hascs);
    p0_finish(p, d0, rws, w - 4, lane, hascs);
    WAITV(0); BAR();
    if (w != 4) return;
    if (lane == 0) st_flag(p.ws, WS_F0 + b * 8, tag);
    if (b != 248) return;
#pragma unroll 1
    for (int i = 0; i < 4; i++) wait_flag(p.ws, WS_F0 + (i * 64 + lane) * 8, tag, tag - 1);
    if (lane == 0) st_flag(p.ws, WS_GEN, tag);
    return;
  }
  if (w >= 4) {
    if (type == 0) loader<true>(p, sm, rws, w - 4, lane, 0, rt, cg, row0, row2, cg2);
    else loader<false>(p, sm, rws, w - 4, lane, type, rt, cg, row0, row2, cg2);
  } else {
    const u64 pc = pc0 + threadIdx.x * 128;
    unsigned t0, t1;
    asm volatile("global_load_dword %0, %2, off\n\tglobal_load_dword %1, %3, off"
                 : "=&v"(t0), "=&v"(t1) : "v"(pc), "v"(pc + min(32768u, CODE_BYTES - 128u * threadIdx.x)) : "memory");
    if (threadIdx.x == 0) {
      const u64 tag = ld_flag(p.ws, WS_GEN) + 1;
      *(LDSP u2*)(sm + AUX + 3072) = u2{(unsigned)tag, (unsigned)(tag >> 32)};
    }
    asm volatile("s_waitcnt vmcnt(0) lgkmcnt(0)" : "+v"(t0), "+v"(t1) :: "memory");
    if (type == 0) k1_mfma<0>(p, sm, rws, w, lane, rt, cg, row0);
    else if (type == 1) k1_mfma<1>(p, sm, rws, w, lane, rt, cg, row0);
    else k1_mfma<2>(p, sm, rws, w, lane, rt, cg, row0);
    STAMP(7);
    if (type != 1) p2_mfma(p, sm, rws, w, lane, row2, cg2);
  }
  STAMP(11); STAMPR(2);
}

extern "C" void kernel_launch(void* const* d_in, const int* in_sizes, int n_in, void* d_out, int out_size,
                              void* d_ws, size_t ws_size, hipStream_t stream) {
  Params p;
  p.x = (const char*)d_in[0]; p.wqkv = (const char*)d_in[1]; p.wo = (const char*)d_in[2];
  p.wg = (const char*)d_in[4]; p.wu = (const char*)d_in[5]; p.wd = (const char*)d_in[6];
  p.out = (char*)d_out; p.ws = (char*)d_ws;
  (void)hipFuncSetAttribute((const void*)fused_kernel, hipFuncAttributeMaxDynamicSharedMemorySize, LDS_BYTES);
  fused_kernel<<<256, 512, LDS_BYTES, stream>>>(p);
}
```

```cpp
#include <hip/hip_runtime.h>
#include <hip/hip_bf16.h>
#include <stdint.h>

typedef __attribute__((ext_vector_type(8))) __bf16 bf8;
typedef __attribute__((ext_vector_type(2))) __bf16 bf2;
typedef __attribute__((ext_vector_type(4))) short s4;
typedef __attribute__((ext_vector_type(2))) float f2;
typedef __attribute__((ext_vector_type(4))) float f4;
typedef __attribute__((ext_vector_type(16))) float f16v;
typedef __attribute__((ext_vector_type(2))) unsigned u2;
typedef __attribute__((ext_vector_type(4))) unsigned u4;
typedef unsigned long long u64;
typedef __amdgpu_buffer_rsrc_t rsrc_t;
#define LDSP __attribute__((address_space(3)))
#define GLBP __attribute__((address_space(1)))
#define DI __device__ __forceinline__

constexpr int S = 2048, D = 640, F = 1536;
constexpr int NCOL = 4992;
constexpr unsigned WS_GEN = 0;
constexpr unsigned WS_F0 = 4096;
constexpr unsigned WS_FH = 8192;
constexpr unsigned WS_FKV = 12288;
constexpr unsigned WS_FO = 16384;
constexpr unsigned WS_STATS = 32768;
constexpr unsigned WS_CS = 65536;
constexpr unsigned WS_KVP = 327680;
constexpr unsigned WS_H = 1 << 20;
constexpr unsigned WS_O = WS_H + S * F * 2;
constexpr unsigned WS_END = WS_O + S * D * 2;

constexpr int SLOT = 52 * 1024;
constexpr int LDS_BYTES = 160 * 1024;
constexpr int AUX = 3 * SLOT;
constexpr int IMG = 54 * 1024;
constexpr int SC1 = 16;
constexpr unsigned CODE_BYTES = 56 * 1024;

DI rsrc_t mkrs(const void* b, unsigned n) { return __builtin_amdgcn_make_buffer_rsrc((void*)b, (short)0, (int)n, 0x00020000); }
DI void blds16(rsrc_t rs, unsigned voff, unsigned soff, LDSP char* l) {
  __builtin_amdgcn_raw_ptr_buffer_load_lds(rs, (LDSP void*)l, 16, voff, soff, 0, 0);
}
template <int AUXB> DI u4 bld128(rsrc_t rs, unsigned voff, unsigned soff) { return __builtin_amdgcn_raw_buffer_load_b128(rs, voff, soff, AUXB); }
template <int AUXB> DI u2 bld64(rsrc_t rs, unsigned voff, unsigned soff) { return __builtin_amdgcn_raw_buffer_load_b64(rs, voff, soff, AUXB); }
template <int AUXB> DI unsigned bld32(rsrc_t rs, unsigned voff, unsigned soff) { return __builtin_amdgcn_raw_buffer_load_b32(rs, voff, soff, AUXB); }
DI void bst128(u4 v, rsrc_t rs, unsigned voff, unsigned soff) { __builtin_amdgcn_raw_buffer_store_b128(v, rs, voff, soff, SC1); }
DI void bst64(u2 v, rsrc_t rs, unsigned voff, unsigned soff) { __builtin_amdgcn_raw_buffer_store_b64(v, rs, voff, soff, SC1); }
DI u4 lds_r128(LDSP char* p) { return *(LDSP u4*)p; }
DI u2 lds_tr(LDSP char* p) { return __builtin_bit_cast(u2, __builtin_amdgcn_ds_read_tr16_b64_v4i16((LDSP s4*)p)); }
DI f16v mfma32(u4 a, u4 b, f16v c) { return __builtin_amdgcn_mfma_f32_32x32x16_bf16(__builtin_bit_cast(bf8, a), __builtin_bit_cast(bf8, b), c, 0, 0, 0); }
DI unsigned pk2(float lo, float hi) { f2 v = {lo, hi}; return __builtin_bit_cast(unsigned, __builtin_convertvector(v, bf2)); }
DI float blo(unsigned v) { return __uint_as_float(v << 16); }
DI float bhi(unsigned v) { return __uint_as_float(v & 0xffff0000u); }
DI float exp2c(float u) { return __builtin_amdgcn_fmed3f(__builtin_amdgcn_exp2f(u), 0.f, 1.f); }
constexpr float LOG2E = 1.44269504088896f, LN2 = 0.693147180559945f;
DI unsigned phi2(f2 u) {
  const f2 t = u * LN2 + 1e-6f, e = {exp2c(u[0]), exp2c(u[1])};
  const f2 v = f2{fmaxf(t[0], 1e-6f), fmaxf(t[1], 1e-6f)} + e;
  return pk2(v[0], v[1]);
}
DI u64 ld_flag(const char* ws, unsigned off) { return __hip_atomic_load((const GLBP u64*)(ws + off), __ATOMIC_RELAXED, __HIP_MEMORY_SCOPE_AGENT); }
DI void st_flag(char* ws, unsigned off, u64 v) { __hip_atomic_store((GLBP u64*)(ws + off), v, __ATOMIC_RELAXED, __HIP_MEMORY_SCOPE_AGENT); }
#define BAR() __builtin_amdgcn_s_barrier()
#define WAITV(n) asm volatile("s_waitcnt vmcnt(" #n ")" ::: "memory")
#define WAITL0() asm volatile("s_waitcnt lgkmcnt(0)" ::: "memory")
#define SCHED0() __builtin_amdgcn_sched_barrier(0)
#define FENCE() do { asm volatile("" ::: "memory"); __builtin_amdgcn_sched_barrier(0); } while (0)

#define STAMP(i)
#define STAMPR(i)

struct Params {
  const char* x; const char* wqkv; const char* wo; const char* wg; const char* wu; const char* wd;
  char* out; char* ws;
};

DI void wait_flag(const char* ws, unsigned off, u64 tag, u64 first) {
  u64 v = first;
#pragma unroll 1
  for (int it = 0; it < (1 << 18); it++) {
    if (__all(v == tag)) break;
    __builtin_amdgcn_s_sleep(1);
    v = ld_flag(ws, off);
  }
  FENCE();
}
DI u64 lds_tag(LDSP char* sm) {
  const u2 t = *(LDSP u2*)(sm + AUX + 3072);
  return (u64)(unsigned)__builtin_amdgcn_readfirstlane(t[0]) | ((u64)(unsigned)__builtin_amdgcn_readfirstlane(t[1]) << 32);
}

template <int CTL> DI float dppadd(float v) { return v + __int_as_float(__builtin_amdgcn_update_dpp(0, __float_as_int(v), CTL, 0xf, 0xf, true)); }
DI float swap16add(float v) { const u2 t = __builtin_amdgcn_permlane16_swap(__float_as_uint(v), __float_as_uint(v), false, false); return __uint_as_float(t[0]) + __uint_as_float(t[1]); }
DI float sum32(float v) { return swap16add(dppadd<0x121>(dppadd<0x122>(dppadd<0x124>(dppadd<0x128>(v))))); }
constexpr unsigned EMPTY = 0xffffffffu;

DI u64 row_load(const Params& p, u2 (&xs)[5], int w, int lane) {
  const u64 gen = ld_flag(p.ws, WS_GEN);
  const rsrc_t rx = mkrs(p.x, S * D * 2);
  const unsigned xo = (unsigned)((blockIdx.x * 8 + w * 2 + (lane >> 5)) * 1280 + (lane & 31) * 8);
#pragma unroll
  for (int i = 0; i < 5; i++) xs[i] = bld64<0>(rx, xo + i * 256, 0);
  FENCE();
  return gen;
}
DI void row_stats(const u2 (&xs)[5], rsrc_t rws, int w, int lane, unsigned tag) {
  const int b = blockIdx.x;
  float s = 0;
#pragma unroll
  for (int i = 0; i < 5; i++) s += (blo(xs[i][0]) + bhi(xs[i][0])) + (blo(xs[i][1]) + bhi(xs[i][1]));
  s = sum32(s);
  const float mu = s * (1.f / 640.f);
  float q = 0;
#pragma unroll
  for (int i = 0; i < 5; i++) {
    const float a = blo(xs[i][0]) - mu, c = bhi(xs[i][0]) - mu, e = blo(xs[i][1]) - mu, g = bhi(xs[i][1]) - mu;
    q += a * a + c * c + e * e + g * g;
  }
  q = sum32(q);
  const float rs = rsqrtf(q * (1.f / 640.f) + 1e-5f);
  if ((lane & 31) == 0) bst128(u4{__float_as_uint(mu), tag, __float_as_uint(rs), tag}, rws, (b * 8 + w * 2 + (lane >> 5)) * 16, WS_STATS);
}
struct CS { rsrc_t rs; unsigned vo, step, dst; u4 d[32]; float c[8]; };
DI void cs_init(const Params& p, CS& k, int task, int lane) {
  const int kp = task / 78, cu = task - kp * 78;
  const char* base = cu < 30 ? p.wqkv + cu * 128 : (cu < 54 ? p.wg + (cu - 30) * 128 : p.wu + (cu - 54) * 128);
  const unsigned stride = cu < 30 ? 3840 : 3072;
  k.rs = mkrs(base, 640 * stride - stride + 128);
  k.vo = (unsigned)(kp * 256 + (lane >> 3)) * stride + (lane & 7) * 16;
  k.step = 8 * stride;
  k.dst = (kp * NCOL + cu * 64 + (lane & 7) * 8 + (lane >> 5) * 4 + ((lane >> 3) & 1) * 2) * 8;
#pragma unroll
  for (int j = 0; j < 8; j++) k.c[j] = 0;
}
DI void cs_load(CS& k, int i) { k.d[i] = bld128<0>(k.rs, k.vo, i * k.step); }
DI void cs_add(CS& k, int i) {
#pragma unroll
  for (int j = 0; j < 4; j++) { k.c[2 * j] += blo(k.d[i][j]); k.c[2 * j + 1] += bhi(k.d[i][j]); }
}
DI void cs_store(CS& k, rsrc_t rws, int lane, unsigned tag) {
  float e[4];
#pragma unroll
  for (int j = 0; j < 4; j++) {
    const u2 t = __builtin_amdgcn_permlane32_swap(__float_as_uint(k.c[j]), __float_as_uint(k.c[4 + j]), false, false);
    e[j] = dppadd<0x128>(swap16add(__uint_as_float(t[0]) + __uint_as_float(t[1])));
  }
  if ((lane & 16) == 0) bst128(u4{__float_as_uint(lane & 8 ? e[2] : e[0]), tag, __float_as_uint(lane & 8 ? e[3] : e[1]), tag}, rws, k.dst, WS_CS);
}

template <int TYPE>
DI void k1_main(LDSP char* sm, int w, int lane, f16v (&acc)[10]) {
  constexpr int NXF = TYPE == 0 ? 5 : 2, NWF = 7 - NXF;
  constexpr int XB = TYPE == 0 ? 160 * 128 : 256 * 128;
  const int r = lane & 31, hh = lane >> 5, blk = (lane >> 4) & 1, q = (lane & 15) >> 2, pp = lane & 3;
  unsigned xa[4];
#pragma unroll
  for (int k = 0; k < 4; k++) xa[k] = (unsigned)(((TYPE == 0 ? 0 : w * 64) + r) * 128 + (((2 * k + hh) ^ ((r >> 1) & 7)) << 4));
  unsigned wa[2];
  if (TYPE == 0) {
#pragma unroll
    for (int ft = 0; ft < 2; ft++)
      wa[ft] = (unsigned)(XB + blk * 16384 + (8 * hh + q) * 256 + (((w * 4 + ft * 2 + (pp >> 1)) ^ (q << 2) ^ (blk << 1)) << 4) + (pp & 1) * 8);
  } else {
    wa[0] = (unsigned)(XB + (8 * hh + q) * 320 + blk * 32 + pp * 8); wa[1] = 0;
  }
  u4 xf[2][NXF], wf[2][NWF];
  auto rd = [&](int buf, LDSP char* base, int k) {
#pragma unroll
    for (int i = 0; i < NWF; i++) {
      u2 lo, hi;
      if (TYPE == 0) { lo = lds_tr(base + wa[i] + k * 4096); hi = lds_tr(base + wa[i] + k * 4096 + 1024); }
      else { lo = lds_tr(base + wa[0] + k * 5120 + i * 64); hi = lds_tr(base + wa[0] + k * 5120 + i * 64 + 1280); }
      wf[buf][i] = u4{lo[0], lo[1], hi[0], hi[1]};
    }
#pragma unroll
    for (int i = 0; i < NXF; i++) xf[buf][i] = lds_r128(base + xa[k] + i * 4096);
  };
  auto mm = [&](int buf) {
#pragma unroll
    for (int i = 0; i < NXF; i++)
#pragma unroll
      for (int j = 0; j < NWF; j++) {
        if (TYPE == 2) acc[i * NWF + j] = mfma32(xf[buf][i], wf[buf][j], acc[i * NWF + j]);
        else acc[i * NWF + j] = mfma32(wf[buf][j], xf[buf][i], acc[i * NWF + j]);
      }
  };
  auto inter = [&]() {
    constexpr int NR = NXF + 2 * NWF;
#pragma unroll
    for (int i = 0; i < 6; i++) {
      __builtin_amdgcn_sched_group_barrier(0x8, 1, 0);
      __builtin_amdgcn_sched_group_barrier(0x100, (NR + 5) / 6, 0);
    }
    __builtin_amdgcn_sched_group_barrier(0x8, 4, 0);
  };
  BAR();
  rd(0, sm, 0);
  int slot = 0;
#pragma unroll 1
  for (int s = 0; s < 9; s++) {
    LDSP char* base = sm + slot * SLOT;
    slot = slot == 2 ? 0 : slot + 1;
    SCHED0();
    rd(1, base, 1); mm(0); inter(); SCHED0();
    rd(0, base, 2); mm(1); inter(); SCHED0();
    rd(1, base, 3); mm(0); inter(); SCHED0();
    WAITL0(); BAR(); SCHED0();
    rd(0, sm + slot * SLOT, 0); mm(1); inter(); SCHED0();
  }
  SCHED0();
  rd(1, sm, 1); mm(0); inter(); SCHED0();
  rd(0, sm, 2); mm(1); inter(); SCHED0();
  rd(1, sm, 3); mm(0); inter(); SCHED0();
  mm(1); SCHED0();
}

DI void kv_publish(LDSP char* sm, rsrc_t rws, int rt, int cg) {
  const int t = threadIdx.x;
  if (t < 420) {
    LDSP char* src = sm + t * 16;
    const f4 s = (*(LDSP f4*)src + *(LDSP f4*)(src + 8192)) + (*(LDSP f4*)(src + 16384) + *(LDSP f4*)(src + 24576));
    u4 v = __builtin_bit_cast(u4, s);
#pragma unroll
    for (int j = 0; j < 4; j++) v[j] = min(v[j], EMPTY - 1);
    bst128(v, rws, t * 16, WS_KVP + (rt * 8 + cg) * 7168);
  }
}

template <int TYPE>
DI void k1_mfma(const Params& p, LDSP char* sm, rsrc_t rws, int w, int lane, int rt, int cg, int row0, u64 pc0) {
  u2 xs[5];
  const u64 gen = row_load(p, xs, w, lane);
  const int hh = lane >> 5, r = lane & 31;
  f16v acc[10];
#pragma unroll
  for (int i = 0; i < 10; i++)
#pragma unroll
    for (int j = 0; j < 16; j++) acc[i][j] = 0.f;
#pragma unroll
  for (int i = 0; i < 10; i++) asm volatile("" : "+v"(acc[i]));
  FENCE();
  {
    const u64 tag = gen + 1;
    row_stats(xs, rws, w, lane, (unsigned)tag);
    if (threadIdx.x == 0) *(LDSP u2*)(sm + AUX + 3072) = u2{(unsigned)tag, (unsigned)(tag >> 32)};
    const u4 rc = {(unsigned)pc0, (unsigned)(pc0 >> 32) & 0xffffu, CODE_BYTES, 0x00020000u};
    asm volatile("s_mov_b32 m0, %0\n\ts_nop 0\n\tbuffer_load_dword %1, %2, 0 offen lds\n\ts_waitcnt lgkmcnt(0)"
                 :: "s"((unsigned)(size_t)(sm + AUX + 3328)), "v"(min(threadIdx.x * 128 + ((blockIdx.x >> 3) & 1) * 32768, CODE_BYTES - 128)), "s"(rc) : "memory", "m0");
    STAMP(12);
  }
  k1_main<TYPE>(sm, w, lane, acc);
  STAMP(3);
  BAR(); FENCE();
  STAMP(19);
  if (TYPE == 0) {
    f4 cs[2][4];
#pragma unroll
    for (int ft = 0; ft < 2; ft++)
#pragma unroll
      for (int qd = 0; qd < 4; qd++)
        cs[ft][qd] = *(LDSP f4*)(sm + AUX + 2048 + ((qd >> 1) * 128 + w * 32 + ft * 16 + (qd & 1) * 8 + hh * 4) * 4);
    const unsigned dst = (unsigned)(((row0 >> 5) * 96 + cg * 8 + w * 2) * 1024 + lane * 16);
#pragma unroll
    for (int tt = 0; tt < 5; tt++) {
      const float mu = *(LDSP float*)(sm + AUX + (tt * 32 + r) * 4);
      if (tt == 0 && rt == 12) continue;
#pragma unroll
      for (int ft = 0; ft < 2; ft++) {
        const f16v a = acc[tt * 2 + ft];
        float h[8];
#pragma unroll
        for (int j = 0; j < 8; j++) h[j] = (a[j] - mu * cs[ft][j >> 2][j & 3]) * (a[8 + j] - mu * cs[ft][2 + (j >> 2)][j & 3]);
        bst128(u4{pk2(h[0], h[1]), pk2(h[2], h[3]), pk2(h[4], h[5]), pk2(h[6], h[7])}, rws, dst + ft * 1024, WS_H + tt * 98304);
      }
    }
    STAMP(4);
    FENCE(); BAR();
    WAITV(0); BAR();
    if (threadIdx.x == 0) st_flag(p.ws, WS_FH + (rt * 16 + cg) * 8, lds_tag(sm));
  } else if (TYPE == 1) {
    u4 pq[2][10];
#pragma unroll
    for (int tt = 0; tt < 2; tt++) {
      const float mu = *(LDSP float*)(sm + AUX + (w * 64 + tt * 32 + r) * 4);
      const float rl = *(LDSP float*)(sm + AUX + 1024 + (w * 64 + tt * 32 + r) * 4) * LOG2E;
#pragma unroll
      for (int ft = 0; ft < 5; ft++) {
        const f16v a = acc[tt * 5 + ft];
#pragma unroll
        for (int qd = 0; qd < 4; qd++) {
          const f4 cs = *(LDSP f4*)(sm + AUX + 2048 + (ft * 32 + qd * 8 + hh * 4) * 4);
#pragma unroll
          for (int j = 0; j < 2; j++) {
            const f2 u = (f2{a[qd * 4 + 2 * j], a[qd * 4 + 2 * j + 1]} - f2{cs[2 * j], cs[2 * j + 1]} * mu) * rl;
            pq[tt][ft * 2 + (qd >> 1)][(qd & 1) * 2 + j] = phi2(u);
          }
        }
        asm volatile("" : "+v"(pq[tt][ft * 2]), "+v"(pq[tt][ft * 2 + 1]));
      }
    }
    STAMP(4);
    SCHED0(); BAR(); SCHED0();
    STAMP(5);
    const unsigned dst = (unsigned)((((row0 >> 5) + w * 2) * 40 + cg * 10) * 1024 + lane * 16);
    f16v oa[2][2];
    u4 af[2][3];
    auto rdi = [&](int n) {
      const int hg = n / 5, T = n % 5, k0 = T < 2 ? 0 : T - 1, k1 = T == 0 ? 1 : (T == 4 ? 4 : T + 1);
#pragma unroll
      for (int k = k0; k <= k1; k++) af[n & 1][k - k0] = lds_r128(sm + IMG + (hg * 25 + T * 5 + k) * 1024 + lane * 16);
    };
    auto omm = [&](int n) {
      const int hg = n / 5, T = n % 5, k0 = T < 2 ? 0 : T - 1, k1 = T == 0 ? 1 : (T == 4 ? 4 : T + 1);
#pragma unroll
      for (int tt = 0; tt < 2; tt++)
#pragma unroll
        for (int j = 0; j < 16; j++) oa[n & 1][tt][j] = 0.f;
#pragma unroll
      for (int k = k0; k <= k1; k++)
#pragma unroll
        for (int tt = 0; tt < 2; tt++) oa[n & 1][tt] = mfma32(af[n & 1][k - k0], pq[tt][hg * 5 + k], oa[n & 1][tt]);
    };
    rdi(0); rdi(1); omm(0); SCHED0();
#pragma unroll
    for (int n = 0; n < 10; n++) {
      if (n + 1 < 10) omm(n + 1);
      if (n + 2 < 10) rdi(n + 2);
#pragma unroll
      for (int tt = 0; tt < 2; tt++) {
        const f16v a = oa[n & 1][tt];
        const float d0 = __builtin_amdgcn_rcpf(a[8] + 1e-6f), d1 = __builtin_amdgcn_rcpf(a[12] + 1e-6f);
        bst128(u4{pk2(a[0] * d0, a[1] * d0), pk2(a[2] * d0, a[3] * d0), pk2(a[4] * d1, a[5] * d1), pk2(a[6] * d1, a[7] * d1)},
               rws, dst + (n & 3) * 1024, WS_O + tt * 40960 + (n >> 2) * 4096);
      }
#pragma unroll
      for (int i = 0; i < 6; i++) { __builtin_amdgcn_sched_group_barrier(0x8, 1, 0); __builtin_amdgcn_sched_group_barrier(0x2, 5, 0); }
      SCHED0();
    }
    STAMP(6);
    WAITV(0); BAR();
    if (threadIdx.x == 0) st_flag(p.ws, WS_FO + (rt * 16 + cg) * 8, lds_tag(sm));
  } else {
    float cs[5];
#pragma unroll
    for (int ct = 0; ct < 5; ct++) cs[ct] = *(LDSP float*)(sm + AUX + 2048 + (ct * 32 + r) * 4);
    const bool isk = r < 16;
    const float kmul = isk ? 1.f : 0.f, kadd = isk ? 1e-6f : 0.f, klo = isk ? 1e-6f : -__builtin_inff();
    u4 fr[4][5];
#pragma unroll
    for (int g = 0; g < 4; g++) {
      f2 mu[4], rs[4], rl[4];
#pragma unroll
      for (int hq = 0; hq < 2; hq++) {
        LDSP char* sp = sm + AUX + (w * 64 + g * 16 + hq * 8 + hh * 4) * 4;
        const f4 a = *(LDSP f4*)sp, b = *(LDSP f4*)(sp + 1024);
        mu[hq * 2] = f2{a[0], a[1]}; mu[hq * 2 + 1] = f2{a[2], a[3]};
        rs[hq * 2] = f2{b[0], b[1]}; rs[hq * 2 + 1] = f2{b[2], b[3]};
      }
#pragma unroll
      for (int j = 0; j < 4; j++) rl[j] = rs[j] * LOG2E;
#pragma unroll
      for (int ct = 0; ct < 5; ct++) {
#pragma unroll
        for (int j = 0; j < 4; j++) {
          const f16v& a = acc[(g >> 1) * 5 + ct];
          const f2 y = f2{a[(g & 1) * 8 + 2 * j], a[(g & 1) * 8 + 2 * j + 1]} - mu[j] * cs[ct];
          if (ct < 2) fr[g][ct][j] = phi2(y * rl[j]);
          else if (ct == 2) {
            const f2 u = y * rl[j], t = y * rs[j] + kadd, e = {exp2c(u[0]), exp2c(u[1])};
            const f2 v = e * kmul + f2{fmaxf(t[0], klo), fmaxf(t[1], klo)};
            fr[g][ct][j] = pk2(v[0], v[1]);
          } else { const f2 v = y * rs[j]; fr[g][ct][j] = pk2(v[0], v[1]); }
        }
      }
      SCHED0();
    }
    f16v z[8];
#pragma unroll
    for (int i = 0; i < 8; i++)
#pragma unroll
      for (int j = 0; j < 16; j++) z[i][j] = 0.f;
#pragma unroll
    for (int g = 0; g < 4; g++) {
      u4 mb;
#pragma unroll
      for (int j = 0; j < 4; j++) mb[j] = isk ? 0x3f803f80u : fr[g][2][j];
      z[0] = mfma32(fr[g][0], fr[g][3], z[0]);
      z[1] = mfma32(fr[g][0], fr[g][4], z[1]);
      z[2] = mfma32(fr[g][0], mb, z[2]);
      z[3] = mfma32(fr[g][1], fr[g][3], z[3]);
      z[4] = mfma32(fr[g][1], fr[g][4], z[4]);
      z[5] = mfma32(fr[g][1], mb, z[5]);
      z[6] = mfma32(fr[g][2], fr[g][4], z[6]);
      z[7] = mfma32(fr[g][2], mb, z[7]);
    }
    unsigned pa[3], pm[3];
#pragma unroll
    for (int vt = 0; vt < 3; vt++) {
      const int e = vt == 2 ? 48 + r : vt * 32 + r, he = e / 20;
      int ee = e - he * 20; unsigned m = 31u << (5 * he);
      if (vt == 2 && isk) { ee = 20; m = r == 0 ? 0xfffffu : 0u; }
      pa[vt] = (unsigned)(w * 8192 + ee * 16 + hh * 336); pm[vt] = m >> hh;
    }
    SCHED0();
#pragma unroll
    for (int i = 0; i < 8; i++)
#pragma unroll
      for (int qd = 0; qd < 4; qd++) {
        const int kt = i / 3, vt = i < 6 ? i % 3 : i - 5, c = kt * 8 + qd * 2;
        const bool need = (i == 1) ? qd >= 2 : (i == 3) ? qd == 0 : (i >= 6) ? qd < 2 : true;
        if (need && (pm[vt] & (1u << c)))
          *(LDSP f4*)(sm + pa[vt] + c * 336) = f4{z[i][qd * 4], z[i][qd * 4 + 1], z[i][qd * 4 + 2], z[i][qd * 4 + 3]};
      }
    STAMP(4);
    WAITL0(); BAR();
    kv_publish(sm, rws, rt, cg);
    WAITL0(); BAR();
  }
}

DI void p2_out(const Params& p, LDSP char* sm, int item, u4 xv, unsigned go) {
  const int row = item >> 3, c = item & 7;
  LDSP char* src = sm + (row >> 3) * 8192 + (row & 7) * 256;
  const unsigned c0 = ((2 * c) ^ (row & 7)) << 4, c1 = c0 ^ 16;
  f4 a = *(LDSP f4*)(src + c0), b = *(LDSP f4*)(src + c1);
#pragma unroll
  for (int i = 1; i < 4; i++) { a += *(LDSP f4*)(src + i * 2048 + c0); b += *(LDSP f4*)(src + i * 2048 + c1); }
  *(GLBP u4*)(p.out + go) = u4{pk2(blo(xv[0]) + a[0], bhi(xv[0]) + a[1]), pk2(blo(xv[1]) + a[2], bhi(xv[1]) + a[3]),
                               pk2(blo(xv[2]) + b[0], bhi(xv[2]) + b[1]), pk2(blo(xv[3]) + b[2], bhi(xv[3]) + b[3])};
}

constexpr int PD = 8;
DI void p2_mfma(const Params& p, LDSP char* sm, rsrc_t rws, int w, int lane, int row2, int cg2) {
  const int hh = lane >> 5, r = lane & 31, blk = (lane >> 4) & 1, q = (lane & 15) >> 2, pp = lane & 3;
  const u64 tag = lds_tag(sm);
  const unsigned fh = WS_FH + (((row2 + ((lane >> 2) & 1) * 95) / 160) * 16 + 3 * w + min(lane & 3, 2)) * 8;
  const unsigned fo = WS_FO + (((row2 + (lane & 1) * 95) >> 8) * 16 + w) * 8;
  float r2[3];
  wait_flag(p.ws, fh, tag, ld_flag(p.ws, fh));
  STAMP(8);
  const unsigned ah = (unsigned)(((row2 >> 5) * 96 + w * 24) * 1024 + lane * 16), ao = (unsigned)(((row2 >> 5) * 40 + w * 10) * 1024 + lane * 16);
  u4 af[PD][3], bf[2][2];
  auto lda = [&](int i) {
#pragma unroll
    for (int tt = 0; tt < 3; tt++) {
      if (i < 24) af[i % PD][tt] = bld128<SC1>(rws, ah + (i & 3) * 1024, WS_H + tt * 98304 + (i >> 2) * 4096);
      else af[i % PD][tt] = bld128<SC1>(rws, ao + (i & 3) * 1024, WS_O + tt * 40960 + ((i - 24) >> 2) * 4096);
    }
  };
  LDSP char* ba[2][3];
#pragma unroll
  for (int ct = 0; ct < 2; ct++)
#pragma unroll
    for (int sg = 0; sg < 3; sg++) {
      ba[ct][sg] = sm + sg * 49152 + w * 2048 + (4 * hh + q) * 128 + (((ct * 4 + blk * 2 + (pp >> 1)) ^ ((q >> 1) << 2)) << 4) + (pp & 1) * 8;
      asm volatile("" : "+v"(ba[ct][sg]));
    }
  auto ldb = [&](int i) {
    const int sl = 17 - (i < 18 ? i : i - 18);
#pragma unroll
    for (int ct = 0; ct < 2; ct++) {
      LDSP char* base = ba[ct][sl / 6] + (sl % 6) * 8192;
      const u2 lo = lds_tr(base), hi = lds_tr(base + 1024);
      bf[i & 1][ct] = u4{lo[0], lo[1], hi[0], hi[1]};
    }
  };
  f16v acc[6];
#pragma unroll
  for (int i = 0; i < 6; i++)
#pragma unroll
    for (int j = 0; j < 16; j++) acc[i][j] = 0.f;
#pragma unroll
  for (int i = 0; i < PD; i++) lda(i);
  ldb(0);
  u64 ofl = 0;
  constexpr int OCHK = 20;
#pragma unroll
  for (int i = 0; i < 34; i++) {
    SCHED0();
    if (i == 7 || i == 15 || i == 23) { STAMP(12 + i / 4); WAITL0(); BAR(); STAMP(13 + i / 4); SCHED0(); }
    if (i == OCHK) {
      STAMP(5);
      wait_flag(p.ws, fo, tag, ofl);
      STAMP(9);
#pragma unroll
      for (int j = 24; j < OCHK + PD; j++) lda(j);
      SCHED0();
    }
    if (i == 24) {
#pragma unroll
      for (int tt = 0; tt < 3; tt++) { acc[tt * 2] *= r2[tt] * r2[tt]; acc[tt * 2 + 1] *= r2[tt] * r2[tt]; }
      SCHED0();
    }
    if (i + 1 < 34) ldb(i + 1);
#pragma unroll
    for (int tt = 0; tt < 3; tt++)
#pragma unroll
      for (int ct = 0; ct < 2; ct++) acc[tt * 2 + ct] = mfma32(bf[i & 1][ct], af[i % PD][tt], acc[tt * 2 + ct]);
    SCHED0();
    if (i + PD < 24 || (i >= OCHK && i + PD < 34)) lda(i + PD);
    if (i + PD == 24) {
#pragma unroll
      for (int tt = 0; tt < 3; tt++) r2[tt] = __uint_as_float(bld32<SC1>(rws, (row2 + tt * 32 + r) * 16 + 8, WS_STATS));
      ofl = ld_flag(p.ws, fo);
    }
  }
  STAMP(10);
  const int item = 512 + threadIdx.x;
  const unsigned go = (unsigned)((row2 + (item >> 3)) * 1280 + cg2 * 128 + (item & 7) * 16);
  const u4 xv = *(const GLBP u4*)(p.x + go);
#pragma unroll
  for (int tt = 0; tt < 3; tt++)
#pragma unroll
    for (int ct = 0; ct < 2; ct++)
#pragma unroll
      for (int qd = 0; qd < 4; qd++)
        *(LDSP f4*)(sm + (tt * 4 + (r >> 3)) * 8192 + w * 2048 + (r & 7) * 256 + (((ct * 8 + qd * 2 + hh) ^ (r & 7)) << 4)) =
            f4{acc[tt * 2 + ct][qd * 4], acc[tt * 2 + ct][qd * 4 + 1], acc[tt * 2 + ct][qd * 4 + 2], acc[tt * 2 + ct][qd * 4 + 3]};
  WAITL0(); BAR();
  p2_out(p, sm, item, xv, go);
}

template <int type>
DI void loader(const Params& p, LDSP char* sm, rsrc_t rws, int lw, int lane, int rt, int cg, int row0, int row2, int cg2) {
  const int b = blockIdx.x, t = lw * 64 + lane;
  constexpr bool gu = type == 0;
  unsigned vo[13];
  {
    const int x0 = gu ? lw * 5 : lw * 8;
    const unsigned xr = (unsigned)((row0 + x0 * 8 + (lane >> 3)) * 1280), xs = (unsigned)(((lane & 7) ^ (lane >> 4) ^ ((x0 & 1) << 2)) << 4);
#pragma unroll
    for (int i = 0; i < 8; i++) vo[i] = xr + (xs ^ ((i & 1) << 6)) + i * 10240;
    if (gu) {
      const unsigned g0 = (unsigned)(((lw & 1) * 32 + (lane >> 4)) * 3072 + (((lane & 15) ^ ((lane >> 4) << 2) ^ (lw & 2)) << 4));
#pragma unroll
      for (int i = 5; i < 13; i++) vo[i] = g0 + (i - 5) * 12288;
    } else {
#pragma unroll
      for (int i = 0; i < 5; i++) {
        const int e = i * 64 + lane, rq = (e * 205) >> 12, pos = e - rq * 20;
        const int adj = type == 1 ? 0 : (pos < 10 ? 0 : (pos < 12 ? 1248 : 1088));
        vo[8 + i] = (unsigned)((lw * 16 + rq) * 3840 + pos * 16 + adj);
      }
    }
  }
  const unsigned wofs = gu ? cg * 256 : (type == 1 ? cg * 320 : 1280 + cg * 160);
  const char* wbase = (gu ? ((lw >> 1) ? p.wu : p.wg) : p.wqkv) + wofs;
  const unsigned wstep = gu ? 64 * 3072 : 64 * 3840, wlen = wstep * 10 - wofs, mstep = gu ? wstep : 128;
  const rsrc_t rx = mkrs(p.x, S * D * 2), rw = mkrs(wbase, wlen), rm = mkrs(gu ? wbase : p.x, gu ? wlen : S * D * 2);
  const int dx = gu ? lw * 5120 : lw * 8192, dm = gu ? 20480 + lw * 8192 : dx + 5120, dw = gu ? dm + 3072 : 32768 + lw * 5120;
  CS ck;
  auto issue = [&](int s, int a0 = 0, int a1 = 0) {
    LDSP char* d = sm + (s % 3) * SLOT;
#pragma unroll
    for (int i = 0; i < 13; i++) {
      if (i < 5) blds16(rx, vo[i], s * 128, d + dx + i * 1024);
      else if (i < 8) blds16(rm, vo[i], s * mstep, d + dm + (i - 5) * 1024);
      else blds16(rw, vo[i], s * wstep, d + dw + (i - 8) * 1024);
      if (type == 1 && i >= 1 && a0 + i - 1 < a1) { FENCE(); cs_add(ck, a0 + i - 1); FENCE(); }
    }
    FENCE();
  };
  auto csl = [&](int i0, int i1) {
    if (type == 1) {
#pragma unroll
      for (int i = i0; i < i1; i++) cs_load(ck, i);
      FENCE();
    }
  };
  issue(0); issue(1);
  if (type == 1) cs_init(p, ck, (rt * 4 + cg) * 4 + lw, lane);
  STAMP(12);
  WAITV(13); BAR(); STAMP(13);
  if (type == 2) {
    bst128(u4{EMPTY, EMPTY, EMPTY, EMPTY}, rws, t * 16, WS_KVP + (rt * 8 + cg) * 7168);
    bst128(u4{EMPTY, EMPTY, EMPTY, EMPTY}, rws, min(t + 256, 419) * 16, WS_KVP + (rt * 8 + cg) * 7168);
    FENCE();
  }
  issue(2);
  const rsrc_t rd_ = mkrs(p.wd, type == 1 ? 0 : F * D * 2), ro_ = mkrs(p.wo, type == 1 ? 0 : D * D * 2);
  const unsigned bo = (unsigned)((lane >> 3) * 1280 + cg2 * 128 + (((lane & 7) ^ (((lane >> 4) & 1) << 2)) << 4));
  auto bdma = [&](int i) {
#pragma unroll
    for (int h = 0; h < 2; h++) blds16(rd_, bo, (lw * 384 + i * 16 + h * 8) * 1280, sm + (17 - i) * 8192 + lw * 2048 + h * 1024);
  };
  u4 br[46];
  auto breg = [&](int i0, int i1) {
#pragma unroll
    for (int i = i0; i < i1; i++) {
      const int st = 11 + (i >> 1);
      br[i] = st < 24 ? bld128<0>(rd_, bo, (lw * 384 + st * 16 + (i & 1) * 8) * 1280) : bld128<0>(ro_, bo, (lw * 160 + (st - 24) * 16 + (i & 1) * 8) * 1280);
    }
    FENCE();
  };
  auto bput = [&](int i0, int i1) {
#pragma unroll
    for (int i = i0; i < i1; i++) {
      const int st = 11 + (i >> 1), sl = st < 18 ? st : st - 18;
      *(LDSP u4*)(sm + (17 - sl) * 8192 + lw * 2048 + (i & 1) * 1024 + lane * 16) = br[i];
    }
  };
  auto bgu = [&](int i0, int i1) { if (type != 1) breg(i0, i1); };
#define WAITG(a, b, c) do { if (gu) WAITV(a); else if (type == 1) WAITV(b); else WAITV(c); } while (0)
  bgu(0, 4); csl(0, 11); WAITG(17, 24, 19); BAR(); STAMP(14);
  issue(3); bgu(4, 8); csl(11, 22); WAITG(21, 35, 21); BAR(); STAMP(19);
  const u64 tag = lds_tag(sm);
  if (t < (type == 2 ? 2 : 1)) st_flag(p.ws, t ? WS_FKV + (cg * 8 + rt) * 8 : WS_F0 + b * 8, tag);
  FENCE();
  issue(4, 0, 11); bgu(8, 12); csl(22, 32); WAITG(21, 34, 21); BAR(); STAMP(15);
  issue(5, 11, 22); bgu(12, 16); WAITG(21, 23, 21); BAR(); STAMP(16);
  issue(6, 22, 32); bgu(16, 20);
  if (type == 1) cs_store(ck, rws, lane, (unsigned)tag);
  WAITG(21, 14, 21); BAR(); STAMP(6);
  issue(7); bgu(20, 24);
  unsigned sto = min(row0 + t, S - 1) * 16, cso;
  {
    const int tc = gu ? t : min(t, 159);
    cso = 8 * (gu ? 1920 + (tc >> 7) * 1536 + cg * 128 + (tc & 127)
                  : (type == 1 ? cg * 160 + tc : cg * 80 + tc + (tc < 80 ? 640 : (tc < 96 ? 1264 : 1184))));
  }
  asm volatile("" : "+v"(sto), "+v"(cso));
  WAITG(21, 14, 21); BAR(); STAMP(7);
  issue(8); bgu(24, 28); WAITG(21, 13, 21); BAR(); STAMP(17);
  issue(9); bgu(28, 32); WAITG(21, 13, 21); BAR(); STAMP(18);
  u4 stv; u2 csv[3];
  auto aux = [&]() {
    stv = bld128<SC1>(rws, sto, WS_STATS);
#pragma unroll
    for (int i = 0; i < 3; i++) csv[i] = bld64<SC1>(rws, cso, WS_CS + i * NCOL * 8);
    FENCE();
  };
  aux();
#pragma unroll
  for (int i = 5; i < 11; i++) bdma(i);
  FENCE();
  if (type == 1) *(LDSP u2*)(sm + IMG + t * 8) = u2{0, 0};
  WAITG(20, 16, 20); BAR();
  if (type != 1) {
#pragma unroll
    for (int i = 0; i < 5; i++) bdma(i);
    FENCE();
  }
  auto fresh = [&]() {
    const unsigned tg = (unsigned)tag;
    return __all(stv[1] == tg && stv[3] == tg && csv[0][1] == tg && csv[1][1] == tg && csv[2][1] == tg);
  };
  if (!fresh()) {
#pragma unroll 1
    for (int it = 0; it < (1 << 18); it++) { aux(); if (fresh()) break; }
  }
  *(LDSP unsigned*)(sm + AUX + t * 4) = stv[0];
  *(LDSP unsigned*)(sm + AUX + 1024 + t * 4) = stv[2];
  *(LDSP float*)(sm + AUX + 2048 + t * 4) = (__uint_as_float(csv[0][0]) + __uint_as_float(csv[1][0])) + __uint_as_float(csv[2][0]);
  if (type == 1) WAITV(0);
  FENCE();
  bgu(32, 36);
  WAITL0(); BAR();
  STAMP(3);
  if (type == 1) {
    const unsigned fk = WS_FKV + (cg * 16 + (lane & 15)) * 8;
    wait_flag(p.ws, fk, tag, ld_flag(p.ws, fk));
    u4 pv[4][8];
    unsigned off[4], da[4];
#pragma unroll
    for (int m = 0; m < 4; m++) {
      const int g = min(t + m * 256, 999), dd = g / 25, e5 = g - dd * 25, hg = dd >= 20, dq = dd - hg * 20;
      const int eg = (dq / 5) * 20 + (e5 < 20 ? e5 : (e5 - 20) * 4);
      off[m] = WS_KVP + (cg * 2 + hg) * 7168 + (dq * 21 + min(e5, 20)) * 16;
      da[m] = IMG + hg * 25600 + (dq >> 2) * 1024 + (dq & 1) * 512 + ((dq >> 1) & 1) * 8 + (eg >> 4) * 5120 + (eg & 15) * 16 + (e5 < 20 ? 0 : 256);
      asm volatile("" : "+v"(off[m]), "+v"(da[m]));
#pragma unroll
      for (int i = 0; i < 8; i++) asm volatile("" : "=v"(pv[m][i]));
    }
    const u4 rk = {(unsigned)(size_t)p.ws, (unsigned)((size_t)p.ws >> 32) & 0xffffu, WS_END, 0x00020000u};
    auto fetch = [&](unsigned set) {
#pragma unroll
      for (int i = 0; i < 8; i++)
        if (set >> i & 1)
          asm volatile("buffer_load_dwordx4 %0, %4, %8, %9 offen sc1\n\tbuffer_load_dwordx4 %1, %5, %8, %9 offen sc1\n\t"
                       "buffer_load_dwordx4 %2, %6, %8, %9 offen sc1\n\tbuffer_load_dwordx4 %3, %7, %8, %9 offen sc1"
                       : "+v"(pv[0][i]), "+v"(pv[1][i]), "+v"(pv[2][i]), "+v"(pv[3][i])
                       : "v"(off[0]), "v"(off[1]), "v"(off[2]), "v"(off[3]), "s"(rk), "s"(i * 8 * 7168) : "memory");
    };
    auto landed = [&]() {
      WAITV(0);
#pragma unroll
      for (int i = 0; i < 8; i++) asm volatile("" : "+v"(pv[0][i]), "+v"(pv[1][i]), "+v"(pv[2][i]), "+v"(pv[3][i]));
    };
    {
      const unsigned ho = WS_KVP + ((lane & 7) * 8 + cg * 2 + ((lane >> 3) & 1)) * 7168 + 419 * 16;
      unsigned got = 0;
#pragma unroll 1
      for (int it = 0; it < (1 << 18) && got != 255; it++) {
        const unsigned mk = (unsigned)__ballot(bld32<SC1>(rws, ho, 0) != EMPTY), now = mk & (mk >> 8) & 255 & ~got;
        fetch(now);
        got |= now;
      }
    }
    STAMP(4);
    f4 sum[4];
#pragma unroll 1
    for (int it = 0; it < (1 << 18); it++) {
      landed();
      bool nan = false;
#pragma unroll
      for (int m = 0; m < 4; m++) {
        sum[m] = __builtin_bit_cast(f4, pv[m][0]);
#pragma unroll
        for (int i = 1; i < 8; i++) sum[m] += __builtin_bit_cast(f4, pv[m][i]);
        nan = nan || __builtin_isunordered(sum[m][0], sum[m][1]) || __builtin_isunordered(sum[m][2], sum[m][3]);
      }
      if (!__any(nan)) break;
      unsigned mx = 0;
#pragma unroll
      for (int i = 0; i < 8; i++)
#pragma unroll
        for (int m = 0; m < 4; m++) mx = max(max(mx, max(pv[m][i][0], pv[m][i][1])), max(pv[m][i][2], pv[m][i][3]));
      if (!__any(mx == EMPTY)) break;
      fetch(255);
    }
    STAMP(5);
#pragma unroll
    for (int m = 0; m < 4; m++) *(LDSP u2*)(sm + da[m]) = u2{pk2(sum[m][0], sum[m][1]), pk2(sum[m][2], sum[m][3])};
    WAITL0(); BAR();
    BAR();
    return;
  }
  __builtin_amdgcn_s_setprio(0);
  if (gu) {
    BAR();
    breg(36, 46);
    WAITV(14);
    STAMP(4);
  } else {
    breg(36, 46);
    WAITV(14);
    STAMP(4);
    BAR(); kv_publish(sm, rws, rt, cg); WAITL0();
  }
  BAR();
  bput(0, 14);
  WAITL0(); BAR();
  bput(14, 30);
  WAITL0(); BAR();
  bput(30, 46);
  WAITL0(); BAR();
  unsigned go[2]; u4 xv[2];
#pragma unroll
  for (int i = 0; i < 2; i++) {
    const int item = i * 256 + t;
    go[i] = (unsigned)((row2 + (item >> 3)) * 1280 + cg2 * 128 + (item & 7) * 16);
    xv[i] = *(const GLBP u4*)(p.x + go[i]);
  }
  BAR();
#pragma unroll
  for (int i = 0; i < 2; i++) p2_out(p, sm, i * 256 + t, xv[i], go[i]);
}

__global__ __launch_bounds__(512, 2) void fused_kernel(Params p) {
  extern __shared__ __attribute__((aligned(16))) char smem[];
  LDSP char* sm = (LDSP char*)smem;
  const int lane = threadIdx.x & 63, w = __builtin_amdgcn_readfirstlane(threadIdx.x >> 6), b = blockIdx.x;
  STAMPR(0); STAMP(1);
  u64 pc0 = __builtin_amdgcn_s_getpc();
  asm volatile("" : "+s"(pc0));
  const rsrc_t rws = mkrs(p.ws, WS_END);
  int type, rt, cg, n2;
  {
    const int c = b & 7, j = b >> 3, a = c & 1, q = c >> 1;
    if (j < 12) {
      rt = 4 * a + j / 3; const int m = j % 3;
      if (m == 0) { type = 1; cg = q; } else { type = 2; cg = 2 * q + m - 1; }
      n2 = (j / 3) * 2 + m - 1;
    } else {
      const int n = j - 12 + (a ? 19 : 0);
      type = (!a && j == 31) ? 3 : 0; rt = n / 3; cg = 3 * q + n % 3;
      n2 = j - 4;
    }
    n2 = max(n2 + q * 55 + a * 27, 0);
  }
  const int row0 = type == 0 ? min(rt * 160, S - 160) : rt * 256;
  const int rt2 = n2 / 10, cg2 = n2 - rt2 * 10, row2 = min(rt2 * 96, S - 96);
  if (type == 3) {
    u2 xs[5];
    const u64 tag = row_load(p, xs, w & 3, lane) + 1;
    if (w < 4) row_stats(xs, rws, w, lane, (unsigned)tag);
#pragma unroll 1
    for (int task = 128 + ((b >> 1) & 3) + w * 4; task < 234; task += 32) {
      CS ck;
      cs_init(p, ck, task, lane);
#pragma unroll 1
      for (int h = task < 156 ? 0 : 1; h < 2; h++) {
#pragma unroll
        for (int i = 0; i < 16; i++) cs_load(ck, i);
#pragma unroll
        for (int i = 0; i < 16; i++) cs_add(ck, i);
        ck.vo += 16 * ck.step;
      }
      cs_store(ck, rws, lane, (unsigned)tag);
    }
    BAR();
    if (w != 0) return;
    if (lane == 0) st_flag(p.ws, WS_F0 + b * 8, tag);
    if (b != 248) return;
#pragma unroll 1
    for (int i = 0; i < 4; i++) wait_flag(p.ws, WS_F0 + (i * 64 + lane) * 8, tag, tag - 1);
    if (lane == 0) st_flag(p.ws, WS_GEN, tag);
    return;
  }
  if (w >= 4) {
    __builtin_amdgcn_s_setprio(3);
    if (type == 2) loader<2>(p, sm, rws, w - 4, lane, rt, cg, row0, row2, cg2);
    else if (type == 0) loader<0>(p, sm, rws, w - 4, lane, rt, cg, row0, row2, cg2);
    else loader<1>(p, sm, rws, w - 4, lane, rt, cg, row0, row2, cg2);
  } else {
    if (type == 0) k1_mfma<0>(p, sm, rws, w, lane, rt, cg, row0, pc0);
    else if (type == 1) k1_mfma<1>(p, sm, rws, w, lane, rt, cg, row0, pc0);
    else k1_mfma<2>(p, sm, rws, w, lane, rt, cg, row0, pc0);
    STAMP(7);
    if (type != 1) p2_mfma(p, sm, rws, w, lane, row2, cg2);
  }
  STAMP(11); STAMPR(2);
}

extern "C" void kernel_launch(void* const* d_in, const int* in_sizes, int n_in, void* d_out, int out_size,
                              void* d_ws, size_t ws_size, hipStream_t stream) {
  Params p;
  p.x = (const char*)d_in[0]; p.wqkv = (const char*)d_in[1]; p.wo = (const char*)d_in[2];
  p.wg = (const char*)d_in[4]; p.wu = (const char*)d_in[5]; p.wd = (const char*)d_in[6];
  p.out = (char*)d_out; p.ws = (char*)d_ws;
  (void)hipFuncSetAttribute((const void*)fused_kernel, hipFuncAttributeMaxDynamicSharedMemorySize, LDS_BYTES);
  fused_kernel<<<256, 512, LDS_BYTES, stream>>>(p);
}
```

```cpp
#include <hip/hip_runtime.h>
#include <hip/hip_bf16.h>
#include <stdint.h>

typedef __attribute__((ext_vector_type(8))) __bf16 bf8;
typedef __attribute__((ext_vector_type(2))) __bf16 bf2;
typedef __attribute__((ext_vector_type(4))) short s4;
typedef __attribute__((ext_vector_type(2))) float f2;
typedef __attribute__((ext_vector_type(4))) float f4;
typedef __attribute__((ext_vector_type(16))) float f16v;
typedef __attribute__((ext_vector_type(2))) unsigned u2;
typedef __attribute__((ext_vector_type(4))) unsigned u4;
typedef unsigned long long u64;
typedef __amdgpu_buffer_rsrc_t rsrc_t;
#define LDSP __attribute__((address_space(3)))
#define GLBP __attribute__((address_space(1)))
#define DI __device__ __forceinline__

constexpr int S = 2048, D = 640, F = 1536;
constexpr int NCOL = 4992;
constexpr unsigned WS_GEN = 0;
constexpr unsigned WS_F0 = 4096;
constexpr unsigned WS_FH = 8192;
constexpr unsigned WS_FKV = 12288;
constexpr unsigned WS_FO = 16384;
constexpr unsigned WS_STATS = 32768;
constexpr unsigned WS_CS = 65536;
constexpr unsigned WS_KVP = 327680;
constexpr unsigned WS_H = 1 << 20;
constexpr unsigned WS_O = WS_H + S * F * 2;
constexpr unsigned WS_END = WS_O + S * D * 2;

constexpr int SLOT = 52 * 1024;
constexpr int LDS_BYTES = 160 * 1024;
constexpr int AUX = 3 * SLOT;
constexpr int IMG = 54 * 1024;
constexpr int SC1 = 16;
constexpr unsigned CODE_BYTES = 62 * 1024;

DI rsrc_t mkrs(const void* b, unsigned n) { return __builtin_amdgcn_make_buffer_rsrc((void*)b, (short)0, (int)n, 0x00020000); }
DI void blds16(rsrc_t rs, unsigned voff, unsigned soff, LDSP char* l) {
  __builtin_amdgcn_raw_ptr_buffer_load_lds(rs, (LDSP void*)l, 16, voff, soff, 0, 0);
}
template <int AUXB> DI u4 bld128(rsrc_t rs, unsigned voff, unsigned soff) { return __builtin_amdgcn_raw_buffer_load_b128(rs, voff, soff, AUXB); }
template <int AUXB> DI u2 bld64(rsrc_t rs, unsigned voff, unsigned soff) { return __builtin_amdgcn_raw_buffer_load_b64(rs, voff, soff, AUXB); }
template <int AUXB> DI unsigned bld32(rsrc_t rs, unsigned voff, unsigned soff) { return __builtin_amdgcn_raw_buffer_load_b32(rs, voff, soff, AUXB); }
DI void bst128(u4 v, rsrc_t rs, unsigned voff, unsigned soff) { __builtin_amdgcn_raw_buffer_store_b128(v, rs, voff, soff, SC1); }
DI void bst64(u2 v, rsrc_t rs, unsigned voff, unsigned soff) { __builtin_amdgcn_raw_buffer_store_b64(v, rs, voff, soff, SC1); }
DI u4 lds_r128(LDSP char* p) { return *(LDSP u4*)p; }
DI u2 lds_tr(LDSP char* p) { return __builtin_bit_cast(u2, __builtin_amdgcn_ds_read_tr16_b64_v4i16((LDSP s4*)p)); }
DI f16v mfma32(u4 a, u4 b, f16v c) { return __builtin_amdgcn_mfma_f32_32x32x16_bf16(__builtin_bit_cast(bf8, a), __builtin_bit_cast(bf8, b), c, 0, 0, 0); }
DI unsigned pk2(float lo, float hi) { f2 v = {lo, hi}; return __builtin_bit_cast(unsigned, __builtin_convertvector(v, bf2)); }
DI float blo(unsigned v) { return __uint_as_float(v << 16); }
DI float bhi(unsigned v) { return __uint_as_float(v & 0xffff0000u); }
DI float exp2c(float u) { return __builtin_amdgcn_fmed3f(__builtin_amdgcn_exp2f(u), 0.f, 1.f); }
constexpr float LOG2E = 1.44269504088896f, LN2 = 0.693147180559945f;
DI unsigned phi2(f2 u) {
  const f2 t = u * LN2 + 1e-6f, e = {exp2c(u[0]), exp2c(u[1])};
  const f2 v = f2{fmaxf(t[0], 1e-6f), fmaxf(t[1], 1e-6f)} + e;
  return pk2(v[0], v[1]);
}
DI u64 ld_flag(const char* ws, unsigned off) { return __hip_atomic_load((const GLBP u64*)(ws + off), __ATOMIC_RELAXED, __HIP_MEMORY_SCOPE_AGENT); }
DI void st_flag(char* ws, unsigned off, u64 v) { __hip_atomic_store((GLBP u64*)(ws + off), v, __ATOMIC_RELAXED, __HIP_MEMORY_SCOPE_AGENT); }
#define BAR() __builtin_amdgcn_s_barrier()
#define WAITV(n) asm volatile("s_waitcnt vmcnt(" #n ")" ::: "memory")
#define WAITL0() asm volatile("s_waitcnt lgkmcnt(0)" ::: "memory")
#define SCHED0() __builtin_amdgcn_sched_barrier(0)
#define FENCE() do { asm volatile("" ::: "memory"); __builtin_amdgcn_sched_barrier(0); } while (0)

#define STAMP(i)
#define STAMPR(i)

struct Params {
  const char* x; const char* wqkv; const char* wo; const char* wg; const char* wu; const char* wd;
  char* out; char* ws;
};

DI void wait_flag(const char* ws, unsigned off, u64 tag, u64 first) {
  u64 v = first;
#pragma unroll 1
  for (int it = 0; it < (1 << 18); it++) {
    if (__all(v == tag)) break;
    __builtin_amdgcn_s_sleep(1);
    v = ld_flag(ws, off);
  }
  FENCE();
}
DI u64 lds_tag(LDSP char* sm) {
  const u2 t = *(LDSP u2*)(sm + AUX + 3072);
  return (u64)(unsigned)__builtin_amdgcn_readfirstlane(t[0]) | ((u64)(unsigned)__builtin_amdgcn_readfirstlane(t[1]) << 32);
}

template <int CTL> DI float dppadd(float v) { return v + __int_as_float(__builtin_amdgcn_update_dpp(0, __float_as_int(v), CTL, 0xf, 0xf, true)); }
DI float swap16add(float v) { const u2 t = __builtin_amdgcn_permlane16_swap(__float_as_uint(v), __float_as_uint(v), false, false); return __uint_as_float(t[0]) + __uint_as_float(t[1]); }
DI float sum32(float v) { return swap16add(dppadd<0x121>(dppadd<0x122>(dppadd<0x124>(dppadd<0x128>(v))))); }
constexpr unsigned EMPTY = 0xffffffffu;

DI u64 row_load(const Params& p, u2 (&xs)[5], int w, int lane) {
  const u64 gen = ld_flag(p.ws, WS_GEN);
  const rsrc_t rx = mkrs(p.x, S * D * 2);
  const unsigned xo = (unsigned)((blockIdx.x * 8 + w * 2 + (lane >> 5)) * 1280 + (lane & 31) * 8);
#pragma unroll
  for (int i = 0; i < 5; i++) xs[i] = bld64<0>(rx, xo + i * 256, 0);
  FENCE();
  return gen;
}
DI void row_stats(const u2 (&xs)[5], rsrc_t rws, int w, int lane, unsigned tag) {
  const int b = blockIdx.x;
  float s = 0;
#pragma unroll
  for (int i = 0; i < 5; i++) s += (blo(xs[i][0]) + bhi(xs[i][0])) + (blo(xs[i][1]) + bhi(xs[i][1]));
  s = sum32(s);
  const float mu = s * (1.f / 640.f);
  float q = 0;
#pragma unroll
  for (int i = 0; i < 5; i++) {
    const float a = blo(xs[i][0]) - mu, c = bhi(xs[i][0]) - mu, e = blo(xs[i][1]) - mu, g = bhi(xs[i][1]) - mu;
    q += a * a + c * c + e * e + g * g;
  }
  q = sum32(q);
  const float rs = rsqrtf(q * (1.f / 640.f) + 1e-5f);
  if ((lane & 31) == 0) bst128(u4{__float_as_uint(mu), tag, __float_as_uint(rs), tag}, rws, (b * 8 + w * 2 + (lane >> 5)) * 16, WS_STATS);
}
struct CS { rsrc_t rs; unsigned vo, step, dst; u4 d[32]; float c[8]; };
DI void cs_init(const Params& p, CS& k, int task, int lane) {
  const int kp = task / 78, cu = task - kp * 78;
  const char* base = cu < 30 ? p.wqkv + cu * 128 : (cu < 54 ? p.wg + (cu - 30) * 128 : p.wu + (cu - 54) * 128);
  const unsigned stride = cu < 30 ? 3840 : 3072;
  k.rs = mkrs(base, task < 234 ? 640 * stride - stride + 128 : 0);
  k.vo = (unsigned)(kp * 256 + (lane >> 3)) * stride + (lane & 7) * 16;
  k.step = 8 * stride;
  k.dst = (kp * NCOL + cu * 64 + (lane & 7) * 8 + (lane >> 5) * 4 + ((lane >> 3) & 1) * 2) * 8;
#pragma unroll
  for (int j = 0; j < 8; j++) k.c[j] = 0;
}
DI void cs_load(CS& k, int i) { k.d[i] = bld128<0>(k.rs, k.vo, i * k.step); }
DI void cs_add(CS& k, int i) {
#pragma unroll
  for (int j = 0; j < 4; j++) { k.c[2 * j] += blo(k.d[i][j]); k.c[2 * j + 1] += bhi(k.d[i][j]); }
}
DI void cs_store(CS& k, rsrc_t rws, int lane, unsigned tag) {
  float e[4];
#pragma unroll
  for (int j = 0; j < 4; j++) {
    const u2 t = __builtin_amdgcn_permlane32_swap(__float_as_uint(k.c[j]), __float_as_uint(k.c[4 + j]), false, false);
    e[j] = dppadd<0x128>(swap16add(__uint_as_float(t[0]) + __uint_as_float(t[1])));
  }
  if ((lane & 16) == 0) bst128(u4{__float_as_uint(lane & 8 ? e[2] : e[0]), tag, __float_as_uint(lane & 8 ? e[3] : e[1]), tag}, rws, k.dst, WS_CS);
}

template <int TYPE>
DI void k1_main(LDSP char* sm, int w, int lane, f16v (&acc)[10]) {
  constexpr int NXF = TYPE == 0 ? 5 : 2, NWF = 7 - NXF;
  constexpr int XB = TYPE == 0 ? 160 * 128 : 256 * 128;
  const int r = lane & 31, hh = lane >> 5, blk = (lane >> 4) & 1, q = (lane & 15) >> 2, pp = lane & 3;
  unsigned xa[4];
#pragma unroll
  for (int k = 0; k < 4; k++) xa[k] = (unsigned)(((TYPE == 0 ? 0 : w * 64) + r) * 128 + (((2 * k + hh) ^ ((r >> 1) & 7)) << 4));
  unsigned wa[2];
  if (TYPE == 0) {
#pragma unroll
    for (int ft = 0; ft < 2; ft++)
      wa[ft] = (unsigned)(XB + blk * 16384 + (8 * hh + q) * 256 + (((w * 4 + ft * 2 + (pp >> 1)) ^ (q << 2) ^ (blk << 1)) << 4) + (pp & 1) * 8);
  } else {
    wa[0] = (unsigned)(XB + (8 * hh + q) * 320 + blk * 32 + pp * 8); wa[1] = 0;
  }
  u4 xf[2][NXF], wf[2][NWF];
  auto rd = [&](int buf, LDSP char* base, int k) {
#pragma unroll
    for (int i = 0; i < NWF; i++) {
      u2 lo, hi;
      if (TYPE == 0) { lo = lds_tr(base + wa[i] + k * 4096); hi = lds_tr(base + wa[i] + k * 4096 + 1024); }
      else { lo = lds_tr(base + wa[0] + k * 5120 + i * 64); hi = lds_tr(base + wa[0] + k * 5120 + i * 64 + 1280); }
      wf[buf][i] = u4{lo[0], lo[1], hi[0], hi[1]};
    }
#pragma unroll
    for (int i = 0; i < NXF; i++) xf[buf][i] = lds_r128(base + xa[k] + i * 4096);
  };
  auto mm = [&](int buf) {
#pragma unroll
    for (int i = 0; i < NXF; i++)
#pragma unroll
      for (int j = 0; j < NWF; j++) {
        if (TYPE == 2) acc[i * NWF + j] = mfma32(xf[buf][i], wf[buf][j], acc[i * NWF + j]);
        else acc[i * NWF + j] = mfma32(wf[buf][j], xf[buf][i], acc[i * NWF + j]);
      }
  };
  auto inter = [&]() {
    constexpr int NR = NXF + 2 * NWF;
#pragma unroll
    for (int i = 0; i < 6; i++) {
      __builtin_amdgcn_sched_group_barrier(0x8, 1, 0);
      __builtin_amdgcn_sched_group_barrier(0x100, (NR + 5) / 6, 0);
    }
    __builtin_amdgcn_sched_group_barrier(0x8, 4, 0);
  };
  BAR();
  rd(0, sm, 0);
  int slot = 0;
#pragma unroll 1
  for (int s = 0; s < 9; s++) {
    LDSP char* base = sm + slot * SLOT;
    slot = slot == 2 ? 0 : slot + 1;
    SCHED0();
    rd(1, base, 1); mm(0); inter(); SCHED0();
    rd(0, base, 2); mm(1); inter(); SCHED0();
    rd(1, base, 3); mm(0); inter(); SCHED0();
    WAITL0(); BAR(); SCHED0();
    rd(0, sm + slot * SLOT, 0); mm(1); inter(); SCHED0();
  }
  SCHED0();
  rd(1, sm, 1); mm(0); inter(); SCHED0();
  rd(0, sm, 2); mm(1); inter(); SCHED0();
  rd(1, sm, 3); mm(0); inter(); SCHED0();
  mm(1); SCHED0();
}

DI void kv_publish(LDSP char* sm, rsrc_t rws, int rt, int cg) {
  const int t = threadIdx.x;
  if (t < 420) {
    LDSP char* src = sm + t * 16;
    const f4 s = (*(LDSP f4*)src + *(LDSP f4*)(src + 8192)) + (*(LDSP f4*)(src + 16384) + *(LDSP f4*)(src + 24576));
    u4 v = __builtin_bit_cast(u4, s);
#pragma unroll
    for (int j = 0; j < 4; j++) v[j] = min(v[j], EMPTY - 1);
    bst128(v, rws, t * 16, WS_KVP + (rt * 8 + cg) * 7168);
  }
}

template <int TYPE>
DI void k1_mfma(const Params& p, LDSP char* sm, rsrc_t rws, int w, int lane, int rt, int cg, int row0, u64 pc0) {
  u2 xs[5];
  const u64 gen = row_load(p, xs, w, lane);
  const int hh = lane >> 5, r = lane & 31;
  f16v acc[10];
#pragma unroll
  for (int i = 0; i < 10; i++)
#pragma unroll
    for (int j = 0; j < 16; j++) acc[i][j] = 0.f;
#pragma unroll
  for (int i = 0; i < 10; i++) asm volatile("" : "+v"(acc[i]));
  FENCE();
  {
    const u64 tag = gen + 1;
    row_stats(xs, rws, w, lane, (unsigned)tag);
    if (threadIdx.x == 0) *(LDSP u2*)(sm + AUX + 3072) = u2{(unsigned)tag, (unsigned)(tag >> 32)};
    const u4 rc = {(unsigned)pc0, (unsigned)(pc0 >> 32) & 0xffffu, CODE_BYTES, 0x00020000u};
    asm volatile("s_mov_b32 m0, %0\n\ts_nop 0\n\tbuffer_load_dword %1, %2, 0 offen lds\n\ts_waitcnt lgkmcnt(0)"
                 :: "s"((unsigned)(size_t)(sm + AUX + 3328)), "v"(min(threadIdx.x * 128 + ((blockIdx.x >> 3) & 1) * 32768, CODE_BYTES - 128)), "s"(rc) : "memory", "m0");
    STAMP(12);
  }
  k1_main<TYPE>(sm, w, lane, acc);
  STAMP(3);
  BAR(); FENCE();
  STAMP(19);
  if (TYPE == 0) {
    f4 cs[2][4];
#pragma unroll
    for (int ft = 0; ft < 2; ft++)
#pragma unroll
      for (int qd = 0; qd < 4; qd++)
        cs[ft][qd] = *(LDSP f4*)(sm + AUX + 2048 + ((qd >> 1) * 128 + w * 32 + ft * 16 + (qd & 1) * 8 + hh * 4) * 4);
    const unsigned dst = (unsigned)(((row0 >> 5) * 96 + cg * 8 + w * 2) * 1024 + lane * 16);
#pragma unroll
    for (int tt = 0; tt < 5; tt++) {
      const float mu = *(LDSP float*)(sm + AUX + (tt * 32 + r) * 4);
      if (tt == 0 && rt == 12) continue;
#pragma unroll
      for (int ft = 0; ft < 2; ft++) {
        const f16v a = acc[tt * 2 + ft];
        float h[8];
#pragma unroll
        for (int j = 0; j < 8; j++) h[j] = (a[j] - mu * cs[ft][j >> 2][j & 3]) * (a[8 + j] - mu * cs[ft][2 + (j >> 2)][j & 3]);
        bst128(u4{pk2(h[0], h[1]), pk2(h[2], h[3]), pk2(h[4], h[5]), pk2(h[6], h[7])}, rws, dst + ft * 1024, WS_H + tt * 98304);
      }
    }
    STAMP(4);
    FENCE(); BAR();
    WAITV(0); BAR();
    if (threadIdx.x == 0) st_flag(p.ws, WS_FH + (rt * 16 + cg) * 8, lds_tag(sm));
  } else if (TYPE == 1) {
    u4 pq[2][10];
#pragma unroll
    for (int tt = 0; tt < 2; tt++) {
      const float mu = *(LDSP float*)(sm + AUX + (w * 64 + tt * 32 + r) * 4);
      const float rl = *(LDSP float*)(sm + AUX + 1024 + (w * 64 + tt * 32 + r) * 4) * LOG2E;
#pragma unroll
      for (int ft = 0; ft < 5; ft++) {
        const f16v a = acc[tt * 5 + ft];
#pragma unroll
        for (int qd = 0; qd < 4; qd++) {
          const f4 cs = *(LDSP f4*)(sm + AUX + 2048 + (ft * 32 + qd * 8 + hh * 4) * 4);
#pragma unroll
          for (int j = 0; j < 2; j++) {
            const f2 u = (f2{a[qd * 4 + 2 * j], a[qd * 4 + 2 * j + 1]} - f2{cs[2 * j], cs[2 * j + 1]} * mu) * rl;
            pq[tt][ft * 2 + (qd >> 1)][(qd & 1) * 2 + j] = phi2(u);
          }
        }
        asm volatile("" : "+v"(pq[tt][ft * 2]), "+v"(pq[tt][ft * 2 + 1]));
      }
    }
    STAMP(4);
    SCHED0(); BAR(); SCHED0();
    STAMP(5);
    const unsigned dst = (unsigned)((((row0 >> 5) + w * 2) * 40 + cg * 10) * 1024 + lane * 16);
    f16v oa[2][2];
    u4 af[2][3];
    auto rdi = [&](int n) {
      const int hg = n / 5, T = n % 5, k0 = T < 2 ? 0 : T - 1, k1 = T == 0 ? 1 : (T == 4 ? 4 : T + 1);
#pragma unroll
      for (int k = k0; k <= k1; k++) af[n & 1][k - k0] = lds_r128(sm + IMG + (hg * 25 + T * 5 + k) * 1024 + lane * 16);
    };
    auto omm = [&](int n) {
      const int hg = n / 5, T = n % 5, k0 = T < 2 ? 0 : T - 1, k1 = T == 0 ? 1 : (T == 4 ? 4 : T + 1);
#pragma unroll
      for (int tt = 0; tt < 2; tt++)
#pragma unroll
        for (int j = 0; j < 16; j++) oa[n & 1][tt][j] = 0.f;
#pragma unroll
      for (int k = k0; k <= k1; k++)
#pragma unroll
        for (int tt = 0; tt < 2; tt++) oa[n & 1][tt] = mfma32(af[n & 1][k - k0], pq[tt][hg * 5 + k], oa[n & 1][tt]);
    };
    rdi(0); rdi(1); omm(0); SCHED0();
#pragma unroll
    for (int n = 0; n < 10; n++) {
      if (n + 1 < 10) omm(n + 1);
      if (n + 2 < 10) rdi(n + 2);
#pragma unroll
      for (int tt = 0; tt < 2; tt++) {
        const f16v a = oa[n & 1][tt];
        const float d0 = __builtin_amdgcn_rcpf(a[8] + 1e-6f), d1 = __builtin_amdgcn_rcpf(a[12] + 1e-6f);
        bst128(u4{pk2(a[0] * d0, a[1] * d0), pk2(a[2] * d0, a[3] * d0), pk2(a[4] * d1, a[5] * d1), pk2(a[6] * d1, a[7] * d1)},
               rws, dst + (n & 3) * 1024, WS_O + tt * 40960 + (n >> 2) * 4096);
      }
#pragma unroll
      for (int i = 0; i < 6; i++) { __builtin_amdgcn_sched_group_barrier(0x8, 1, 0); __builtin_amdgcn_sched_group_barrier(0x2, 5, 0); }
      SCHED0();
    }
    STAMP(6);
    WAITV(0); BAR();
    if (threadIdx.x == 0) st_flag(p.ws, WS_FO + (rt * 16 + cg) * 8, lds_tag(sm));
  } else {
    float cs[5];
#pragma unroll
    for (int ct = 0; ct < 5; ct++) cs[ct] = *(LDSP float*)(sm + AUX + 2048 + (ct * 32 + r) * 4);
    const bool isk = r < 16;
    const float kmul = isk ? 1.f : 0.f, kadd = isk ? 1e-6f : 0.f, klo = isk ? 1e-6f : -__builtin_inff();
    u4 fr[4][5];
#pragma unroll
    for (int g = 0; g < 4; g++) {
      f2 mu[4], rs[4], rl[4];
#pragma unroll
      for (int hq = 0; hq < 2; hq++) {
        LDSP char* sp = sm + AUX + (w * 64 + g * 16 + hq * 8 + hh * 4) * 4;
        const f4 a = *(LDSP f4*)sp, b = *(LDSP f4*)(sp + 1024);
        mu[hq * 2] = f2{a[0], a[1]}; mu[hq * 2 + 1] = f2{a[2], a[3]};
        rs[hq * 2] = f2{b[0], b[1]}; rs[hq * 2 + 1] = f2{b[2], b[3]};
      }
#pragma unroll
      for (int j = 0; j < 4; j++) rl[j] = rs[j] * LOG2E;
#pragma unroll
      for (int ct = 0; ct < 5; ct++) {
#pragma unroll
        for (int j = 0; j < 4; j++) {
          const f16v& a = acc[(g >> 1) * 5 + ct];
          const f2 y = f2{a[(g & 1) * 8 + 2 * j], a[(g & 1) * 8 + 2 * j + 1]} - mu[j] * cs[ct];
          if (ct < 2) fr[g][ct][j] = phi2(y * rl[j]);
          else if (ct == 2) {
            const f2 u = y * rl[j], t = y * rs[j] + kadd, e = {exp2c(u[0]), exp2c(u[1])};
            const f2 v = e * kmul + f2{fmaxf(t[0], klo), fmaxf(t[1], klo)};
            fr[g][ct][j] = pk2(v[0], v[1]);
          } else { const f2 v = y * rs[j]; fr[g][ct][j] = pk2(v[0], v[1]); }
        }
      }
      SCHED0();
    }
    f16v z[8];
#pragma unroll
    for (int i = 0; i < 8; i++)
#pragma unroll
      for (int j = 0; j < 16; j++) z[i][j] = 0.f;
#pragma unroll
    for (int g = 0; g < 4; g++) {
      u4 mb;
#pragma unroll
      for (int j = 0; j < 4; j++) mb[j] = isk ? 0x3f803f80u : fr[g][2][j];
      z[0] = mfma32(fr[g][0], fr[g][3], z[0]);
      z[1] = mfma32(fr[g][0], fr[g][4], z[1]);
      z[2] = mfma32(fr[g][0], mb, z[2]);
      z[3] = mfma32(fr[g][1], fr[g][3], z[3]);
      z[4] = mfma32(fr[g][1], fr[g][4], z[4]);
      z[5] = mfma32(fr[g][1], mb, z[5]);
      z[6] = mfma32(fr[g][2], fr[g][4], z[6]);
      z[7] = mfma32(fr[g][2], mb, z[7]);
    }
    unsigned pa[3], pm[3];
#pragma unroll
    for (int vt = 0; vt < 3; vt++) {
      const int e = vt == 2 ? 48 + r : vt * 32 + r, he = e / 20;
      int ee = e - he * 20; unsigned m = 31u << (5 * he);
      if (vt == 2 && isk) { ee = 20; m = r == 0 ? 0xfffffu : 0u; }
      pa[vt] = (unsigned)(w * 8192 + ee * 16 + hh * 336); pm[vt] = m >> hh;
    }
    SCHED0();
#pragma unroll
    for (int i = 0; i < 8; i++)
#pragma unroll
      for (int qd = 0; qd < 4; qd++) {
        const int kt = i / 3, vt = i < 6 ? i % 3 : i - 5, c = kt * 8 + qd * 2;
        const bool need = (i == 1) ? qd >= 2 : (i == 3) ? qd == 0 : (i >= 6) ? qd < 2 : true;
        if (need && (pm[vt] & (1u << c)))
          *(LDSP f4*)(sm + pa[vt] + c * 336) = f4{z[i][qd * 4], z[i][qd * 4 + 1], z[i][qd * 4 + 2], z[i][qd * 4 + 3]};
      }
    STAMP(4);
    WAITL0(); BAR();
    kv_publish(sm, rws, rt, cg);
    WAITL0(); BAR();
  }
}

DI void p2_out(const Params& p, LDSP char* sm, int item, u4 xv, unsigned go) {
  const int row = item >> 3, c = item & 7;
  LDSP char* src = sm + (row >> 3) * 8192 + (row & 7) * 256;
  const unsigned c0 = ((2 * c) ^ (row & 7)) << 4, c1 = c0 ^ 16;
  f4 a = *(LDSP f4*)(src + c0), b = *(LDSP f4*)(src + c1);
#pragma unroll
  for (int i = 1; i < 4; i++) { a += *(LDSP f4*)(src + i * 2048 + c0); b += *(LDSP f4*)(src + i * 2048 + c1); }
  *(GLBP u4*)(p.out + go) = u4{pk2(blo(xv[0]) + a[0], bhi(xv[0]) + a[1]), pk2(blo(xv[1]) + a[2], bhi(xv[1]) + a[3]),
                               pk2(blo(xv[2]) + b[0], bhi(xv[2]) + b[1]), pk2(blo(xv[3]) + b[2], bhi(xv[3]) + b[3])};
}

constexpr int PD = 8;
DI void p2_mfma(const Params& p, LDSP char* sm, rsrc_t rws, int w, int lane, int row2, int cg2) {
  const int hh = lane >> 5, r = lane & 31, blk = (lane >> 4) & 1, q = (lane & 15) >> 2, pp = lane & 3;
  const u64 tag = lds_tag(sm);
  const unsigned fh = WS_FH + (((row2 + ((lane >> 2) & 1) * 95) / 160) * 16 + 3 * w + min(lane & 3, 2)) * 8;
  const unsigned fo = WS_FO + (((row2 + (lane & 1) * 95) >> 8) * 16 + w) * 8;
  float r2[3];
  wait_flag(p.ws, fh, tag, ld_flag(p.ws, fh));
  STAMP(8);
  const unsigned ah = (unsigned)(((row2 >> 5) * 96 + w * 24) * 1024 + lane * 16), ao = (unsigned)(((row2 >> 5) * 40 + w * 10) * 1024 + lane * 16);
  u4 af[PD][3], bf[2][2];
  auto lda = [&](int i) {
#pragma unroll
    for (int tt = 0; tt < 3; tt++) {
      if (i < 24) af[i % PD][tt] = bld128<SC1>(rws, ah + (i & 3) * 1024, WS_H + tt * 98304 + (i >> 2) * 4096);
      else af[i % PD][tt] = bld128<SC1>(rws, ao + (i & 3) * 1024, WS_O + tt * 40960 + ((i - 24) >> 2) * 4096);
    }
  };
  LDSP char* ba[2][3];
#pragma unroll
  for (int ct = 0; ct < 2; ct++)
#pragma unroll
    for (int sg = 0; sg < 3; sg++) {
      ba[ct][sg] = sm + sg * 49152 + w * 2048 + (4 * hh + q) * 128 + (((ct * 4 + blk * 2 + (pp >> 1)) ^ ((q >> 1) << 2)) << 4) + (pp & 1) * 8;
      asm volatile("" : "+v"(ba[ct][sg]));
    }
  auto ldb = [&](int i) {
    const int sl = 17 - (i < 18 ? i : i - 18);
#pragma unroll
    for (int ct = 0; ct < 2; ct++) {
      LDSP char* base = ba[ct][sl / 6] + (sl % 6) * 8192;
      const u2 lo = lds_tr(base), hi = lds_tr(base + 1024);
      bf[i & 1][ct] = u4{lo[0], lo[1], hi[0], hi[1]};
    }
  };
  f16v acc[6];
#pragma unroll
  for (int i = 0; i < 6; i++)
#pragma unroll
    for (int j = 0; j < 16; j++) acc[i][j] = 0.f;
#pragma unroll
  for (int i = 0; i < PD; i++) lda(i);
  ldb(0);
  u64 ofl = 0;
  constexpr int OCHK = 20;
#pragma unroll
  for (int i = 0; i < 34; i++) {
    SCHED0();
    if (i == 7 || i == 15 || i == 23) { STAMP(12 + i / 4); WAITL0(); BAR(); STAMP(13 + i / 4); SCHED0(); }
    if (i == OCHK) {
      STAMP(5);
      wait_flag(p.ws, fo, tag, ofl);
      STAMP(9);
#pragma unroll
      for (int j = 24; j < OCHK + PD; j++) lda(j);
      SCHED0();
    }
    if (i == 24) {
#pragma unroll
      for (int tt = 0; tt < 3; tt++) { acc[tt * 2] *= r2[tt] * r2[tt]; acc[tt * 2 + 1] *= r2[tt] * r2[tt]; }
      SCHED0();
    }
    if (i + 1 < 34) ldb(i + 1);
#pragma unroll
    for (int tt = 0; tt < 3; tt++)
#pragma unroll
      for (int ct = 0; ct < 2; ct++) acc[tt * 2 + ct] = mfma32(bf[i & 1][ct], af[i % PD][tt], acc[tt * 2 + ct]);
    SCHED0();
    if (i + PD < 24 || (i >= OCHK && i + PD < 34)) lda(i + PD);
    if (i + PD == 24) {
#pragma unroll
      for (int tt = 0; tt < 3; tt++) r2[tt] = __uint_as_float(bld32<SC1>(rws, (row2 + tt * 32 + r) * 16 + 8, WS_STATS));
      ofl = ld_flag(p.ws, fo);
    }
  }
  STAMP(10);
  const int item = 512 + threadIdx.x;
  const unsigned go = (unsigned)((row2 + (item >> 3)) * 1280 + cg2 * 128 + (item & 7) * 16);
  const u4 xv = *(const GLBP u4*)(p.x + go);
#pragma unroll
  for (int tt = 0; tt < 3; tt++)
#pragma unroll
    for (int ct = 0; ct < 2; ct++)
#pragma unroll
      for (int qd = 0; qd < 4; qd++)
        *(LDSP f4*)(sm + (tt * 4 + (r >> 3)) * 8192 + w * 2048 + (r & 7) * 256 + (((ct * 8 + qd * 2 + hh) ^ (r & 7)) << 4)) =
            f4{acc[tt * 2 + ct][qd * 4], acc[tt * 2 + ct][qd * 4 + 1], acc[tt * 2 + ct][qd * 4 + 2], acc[tt * 2 + ct][qd * 4 + 3]};
  WAITL0(); BAR();
  p2_out(p, sm, item, xv, go);
}

template <int type>
DI void loader(const Params& p, LDSP char* sm, rsrc_t rws, int lw, int lane, int rt, int cg, int row0, int row2, int cg2) {
  const int b = blockIdx.x, t = lw * 64 + lane;
  constexpr bool gu = type == 0;
  unsigned vo[13];
  {
    const int x0 = gu ? lw * 5 : lw * 8;
    const unsigned xr = (unsigned)((row0 + x0 * 8 + (lane >> 3)) * 1280), xs = (unsigned)(((lane & 7) ^ (lane >> 4) ^ ((x0 & 1) << 2)) << 4);
#pragma unroll
    for (int i = 0; i < 8; i++) vo[i] = xr + (xs ^ ((i & 1) << 6)) + i * 10240;
    if (gu) {
      const unsigned g0 = (unsigned)(((lw & 1) * 32 + (lane >> 4)) * 3072 + (((lane & 15) ^ ((lane >> 4) << 2) ^ (lw & 2)) << 4));
#pragma unroll
      for (int i = 5; i < 13; i++) vo[i] = g0 + (i - 5) * 12288;
    } else {
#pragma unroll
      for (int i = 0; i < 5; i++) {
        const int e = i * 64 + lane, rq = (e * 205) >> 12, pos = e - rq * 20;
        const int adj = type == 1 ? 0 : (pos < 10 ? 0 : (pos < 12 ? 1248 : 1088));
        vo[8 + i] = (unsigned)((lw * 16 + rq) * 3840 + pos * 16 + adj);
      }
    }
  }
  const unsigned wofs = gu ? cg * 256 : (type == 1 ? cg * 320 : 1280 + cg * 160);
  const char* wbase = (gu ? ((lw >> 1) ? p.wu : p.wg) : p.wqkv) + wofs;
  const unsigned wstep = gu ? 64 * 3072 : 64 * 3840, wlen = wstep * 10 - wofs, mstep = gu ? wstep : 128;
  const rsrc_t rx = mkrs(p.x, S * D * 2), rw = mkrs(wbase, wlen), rm = mkrs(gu ? wbase : p.x, gu ? wlen : S * D * 2);
  const int dx = gu ? lw * 5120 : lw * 8192, dm = gu ? 20480 + lw * 8192 : dx + 5120, dw = gu ? dm + 3072 : 32768 + lw * 5120;
  CS ck;
  const int task = type == 1 ? (rt * 4 + cg) * 4 + lw : min(156 + lw * 64 + cg * 8 + rt, 234);
  const bool act = !gu && task < 234;
  auto issue = [&](int s, int a0 = 0, int a1 = 0) {
    LDSP char* d = sm + (s % 3) * SLOT;
#pragma unroll
    for (int i = 0; i < 13; i++) {
      if (i < 5) blds16(rx, vo[i], s * 128, d + dx + i * 1024);
      else if (i < 8) blds16(rm, vo[i], s * mstep, d + dm + (i - 5) * 1024);
      else blds16(rw, vo[i], s * wstep, d + dw + (i - 8) * 1024);
      if (i >= 1 && a0 + i - 1 < a1 && (type == 1 || act)) {
        FENCE(); cs_add(ck, a0 + i - 1);
        asm volatile("" : "+v"(ck.c[0]), "+v"(ck.c[1]), "+v"(ck.c[2]), "+v"(ck.c[3]), "+v"(ck.c[4]), "+v"(ck.c[5]), "+v"(ck.c[6]), "+v"(ck.c[7]));
        FENCE();
      }
    }
    FENCE();
  };
  auto csl = [&](int i0, int i1) {
    if (type == 1 || act) {
#pragma unroll
      for (int i = i0; i < i1; i++) cs_load(ck, i);
      FENCE();
    }
  };
  issue(0); issue(1);
  if (!gu) cs_init(p, ck, task, lane);
  constexpr bool q1 = type == 1;
  STAMP(12);
  WAITV(13); BAR(); STAMP(13);
  if (type == 2) {
    bst128(u4{EMPTY, EMPTY, EMPTY, EMPTY}, rws, t * 16, WS_KVP + (rt * 8 + cg) * 7168);
    bst128(u4{EMPTY, EMPTY, EMPTY, EMPTY}, rws, min(t + 256, 419) * 16, WS_KVP + (rt * 8 + cg) * 7168);
    FENCE();
  }
  issue(2);
  const rsrc_t rd_ = mkrs(p.wd, type == 1 ? 0 : F * D * 2), ro_ = mkrs(p.wo, type == 1 ? 0 : D * D * 2);
  const unsigned bo = (unsigned)((lane >> 3) * 1280 + cg2 * 128 + (((lane & 7) ^ (((lane >> 4) & 1) << 2)) << 4));
  auto bdma = [&](int i) {
#pragma unroll
    for (int h = 0; h < 2; h++) blds16(rd_, bo, (lw * 384 + i * 16 + h * 8) * 1280, sm + (17 - i) * 8192 + lw * 2048 + h * 1024);
  };
  u4 br[46];
  auto breg = [&](int i0, int i1) {
#pragma unroll
    for (int i = i0; i < i1; i++) {
      const int st = 11 + (i >> 1);
      br[i] = st < 24 ? bld128<0>(rd_, bo, (lw * 384 + st * 16 + (i & 1) * 8) * 1280) : bld128<0>(ro_, bo, (lw * 160 + (st - 24) * 16 + (i & 1) * 8) * 1280);
    }
    FENCE();
  };
  auto bput = [&](int i0, int i1) {
#pragma unroll
    for (int i = i0; i < i1; i++) {
      const int st = 11 + (i >> 1), sl = st < 18 ? st : st - 18;
      *(LDSP u4*)(sm + (17 - sl) * 8192 + lw * 2048 + (i & 1) * 1024 + lane * 16) = br[i];
    }
  };
  auto bgu = [&](int i0, int i1) { if (gu) breg(i0, i1); };
  auto bkv = [&](int i0, int i1) { if (type == 2) breg(i0, i1); };
#define WAITG(a, b, c) do { if (gu) WAITV(a); else if (type == 1) WAITV(b); else WAITV(c); } while (0)
#define WAITA(a, b, c, d) do { if (type == 2 && act) WAITV(d); else WAITG(a, b, c); } while (0)
  bgu(0, 4); csl(0, q1 ? 11 : 8); WAITA(17, 24, 15, 23); BAR(); STAMP(14);
  issue(3); bgu(4, 8); csl(q1 ? 11 : 8, q1 ? 22 : 16); WAITA(21, 35, 13, 29); BAR(); STAMP(19);
  const u64 tag = lds_tag(sm);
  if (t < (type == 2 ? 2 : 1)) st_flag(p.ws, t ? WS_FKV + (cg * 8 + rt) * 8 : WS_F0 + b * 8, tag);
  FENCE();
  issue(4, 0, q1 ? 11 : 0); bgu(8, 12); csl(22, q1 ? 32 : 0); WAITA(21, 34, 13, 21); BAR(); STAMP(15);
  issue(5, q1 ? 11 : 0, q1 ? 22 : 8); bgu(12, 16); WAITG(21, 23, 13); BAR(); STAMP(16);
  issue(6, q1 ? 22 : 8, q1 ? 32 : 16); bgu(16, 20);
  if (type == 1 || act) cs_store(ck, rws, lane, (unsigned)tag);
  WAITA(21, 14, 13, 14); BAR(); STAMP(6);
  issue(7); bgu(20, 24); bkv(0, 4);
  unsigned sto = min(row0 + t, S - 1) * 16, cso;
  {
    const int tc = gu ? t : min(t, 159);
    cso = 8 * (gu ? 1920 + (tc >> 7) * 1536 + cg * 128 + (tc & 127)
                  : (type == 1 ? cg * 160 + tc : cg * 80 + tc + (tc < 80 ? 640 : (tc < 96 ? 1264 : 1184))));
  }
  asm volatile("" : "+v"(sto), "+v"(cso));
  WAITA(21, 14, 17, 18); BAR(); STAMP(7);
  issue(8); bgu(24, 28); bkv(4, 8); WAITG(21, 13, 21); BAR(); STAMP(17);
  issue(9); bgu(28, 32); bkv(8, 12); WAITG(21, 13, 21); BAR(); STAMP(18);
  u4 stv; u2 csv[3];
  auto aux = [&]() {
    stv = bld128<SC1>(rws, sto, WS_STATS);
#pragma unroll
    for (int i = 0; i < 3; i++) csv[i] = bld64<SC1>(rws, cso, WS_CS + i * NCOL * 8);
    FENCE();
  };
  aux();
#pragma unroll
  for (int i = 5; i < 11; i++) bdma(i);
  FENCE();
  if (type == 1) *(LDSP u2*)(sm + IMG + t * 8) = u2{0, 0};
  WAITG(20, 16, 20); BAR();
  if (type != 1) {
#pragma unroll
    for (int i = 0; i < 5; i++) bdma(i);
    FENCE();
  }
  auto fresh = [&]() {
    const unsigned tg = (unsigned)tag;
    return __all(stv[1] == tg && stv[3] == tg && csv[0][1] == tg && csv[1][1] == tg && csv[2][1] == tg);
  };
  if (!fresh()) {
#pragma unroll 1
    for (int it = 0; it < (1 << 18); it++) { aux(); if (fresh()) break; }
  }
  *(LDSP unsigned*)(sm + AUX + t * 4) = stv[0];
  *(LDSP unsigned*)(sm + AUX + 1024 + t * 4) = stv[2];
  *(LDSP float*)(sm + AUX + 2048 + t * 4) = (__uint_as_float(csv[0][0]) + __uint_as_float(csv[1][0])) + __uint_as_float(csv[2][0]);
  if (type == 1) WAITV(0);
  FENCE();
  bgu(32, 36);
  WAITL0(); BAR();
  STAMP(3);
  if (type == 1) {
    const unsigned fk = WS_FKV + (cg * 16 + (lane & 15)) * 8;
    wait_flag(p.ws, fk, tag, ld_flag(p.ws, fk));
    u4 pv[4][8];
    unsigned off[4], da[4];
#pragma unroll
    for (int m = 0; m < 4; m++) {
      const int g = min(t + m * 256, 999), dd = g / 25, e5 = g - dd * 25, hg = dd >= 20, dq = dd - hg * 20;
      const int eg = (dq / 5) * 20 + (e5 < 20 ? e5 : (e5 - 20) * 4);
      off[m] = WS_KVP + (cg * 2 + hg) * 7168 + (dq * 21 + min(e5, 20)) * 16;
      da[m] = IMG + hg * 25600 + (dq >> 2) * 1024 + (dq & 1) * 512 + ((dq >> 1) & 1) * 8 + (eg >> 4) * 5120 + (eg & 15) * 16 + (e5 < 20 ? 0 : 256);
      asm volatile("" : "+v"(off[m]), "+v"(da[m]));
#pragma unroll
      for (int i = 0; i < 8; i++) asm volatile("" : "=v"(pv[m][i]));
    }
    const u4 rk = {(unsigned)(size_t)p.ws, (unsigned)((size_t)p.ws >> 32) & 0xffffu, WS_END, 0x00020000u};
    auto fetch = [&](unsigned set) {
#pragma unroll
      for (int i = 0; i < 8; i++)
        if (set >> i & 1)
          asm volatile("buffer_load_dwordx4 %0, %4, %8, %9 offen sc1\n\tbuffer_load_dwordx4 %1, %5, %8, %9 offen sc1\n\t"
                       "buffer_load_dwordx4 %2, %6, %8, %9 offen sc1\n\tbuffer_load_dwordx4 %3, %7, %8, %9 offen sc1"
                       : "+v"(pv[0][i]), "+v"(pv[1][i]), "+v"(pv[2][i]), "+v"(pv[3][i])
                       : "v"(off[0]), "v"(off[1]), "v"(off[2]), "v"(off[3]), "s"(rk), "s"(i * 8 * 7168) : "memory");
    };
    auto landed = [&]() {
      WAITV(0);
#pragma unroll
      for (int i = 0; i < 8; i++) asm volatile("" : "+v"(pv[0][i]), "+v"(pv[1][i]), "+v"(pv[2][i]), "+v"(pv[3][i]));
    };
    {
      const unsigned ho = WS_KVP + ((lane & 7) * 8 + cg * 2 + ((lane >> 3) & 1)) * 7168 + 419 * 16;
      unsigned got = 0;
#pragma unroll 1
      for (int it = 0; it < (1 << 18) && got != 255; it++) {
        const unsigned mk = (unsigned)__ballot(bld32<SC1>(rws, ho, 0) != EMPTY), now = mk & (mk >> 8) & 255 & ~got;
        fetch(now);
        got |= now;
      }
    }
    STAMP(4);
    f4 sum[4];
#pragma unroll 1
    for (int it = 0; it < (1 << 18); it++) {
      landed();
      bool nan = false;
#pragma unroll
      for (int m = 0; m < 4; m++) {
        sum[m] = __builtin_bit_cast(f4, pv[m][0]);
#pragma unroll
        for (int i = 1; i < 8; i++) sum[m] += __builtin_bit_cast(f4, pv[m][i]);
        nan = nan || __builtin_isunordered(sum[m][0], sum[m][1]) || __builtin_isunordered(sum[m][2], sum[m][3]);
      }
      if (!__any(nan)) break;
      unsigned mx = 0;
#pragma unroll
      for (int i = 0; i < 8; i++)
#pragma unroll
        for (int m = 0; m < 4; m++) mx = max(max(mx, max(pv[m][i][0], pv[m][i][1])), max(pv[m][i][2], pv[m][i][3]));
      if (!__any(mx == EMPTY)) break;
      fetch(255);
    }
    STAMP(5);
#pragma unroll
    for (int m = 0; m < 4; m++) *(LDSP u2*)(sm + da[m]) = u2{pk2(sum[m][0], sum[m][1]), pk2(sum[m][2], sum[m][3])};
    WAITL0(); BAR();
    BAR();
    return;
  }
  __builtin_amdgcn_s_setprio(0);
  if (gu) {
    BAR();
    breg(36, 46);
    WAITV(14);
    STAMP(4);
  } else {
    breg(12, 30);
    STAMP(4);
    BAR(); kv_publish(sm, rws, rt, cg); WAITL0();
    WAITV(18);
  }
  BAR();
  bput(0, 14);
  FENCE();
  bkv(30, 46);
  WAITL0(); BAR();
  bput(14, 30);
  WAITL0(); BAR();
  bput(30, 46);
  WAITL0(); BAR();
  unsigned go[2]; u4 xv[2];
#pragma unroll
  for (int i = 0; i < 2; i++) {
    const int item = i * 256 + t;
    go[i] = (unsigned)((row2 + (item >> 3)) * 1280 + cg2 * 128 + (item & 7) * 16);
    xv[i] = *(const GLBP u4*)(p.x + go[i]);
  }
  BAR();
#pragma unroll
  for (int i = 0; i < 2; i++) p2_out(p, sm, i * 256 + t, xv[i], go[i]);
}

__global__ __launch_bounds__(512, 2) void fused_kernel(Params p) {
  extern __shared__ __attribute__((aligned(16))) char smem[];
  LDSP char* sm = (LDSP char*)smem;
  const int lane = threadIdx.x & 63, w = __builtin_amdgcn_readfirstlane(threadIdx.x >> 6), b = blockIdx.x;
  STAMPR(0); STAMP(1);
  u64 pc0 = __builtin_amdgcn_s_getpc();
  asm volatile("" : "+s"(pc0));
  const rsrc_t rws = mkrs(p.ws, WS_END);
  int type, rt, cg, n2;
  {
    const int c = b & 7, j = b >> 3, a = c & 1, q = c >> 1;
    if (j < 12) {
      rt = 4 * a + j / 3; const int m = j % 3;
      if (m == 0) { type = 1; cg = q; } else { type = 2; cg = 2 * q + m - 1; }
      n2 = (j / 3) * 2 + m - 1;
    } else {
      const int n = j - 12 + (a ? 19 : 0);
      type = (!a && j == 31) ? 3 : 0; rt = n / 3; cg = 3 * q + n % 3;
      n2 = j - 4;
    }
    n2 = max(n2 + q * 55 + a * 27, 0);
  }
  const int row0 = type == 0 ? min(rt * 160, S - 160) : rt * 256;
  const int rt2 = n2 / 10, cg2 = n2 - rt2 * 10, row2 = min(rt2 * 96, S - 96);
  if (type == 3) {
    u2 xs[5];
    const u64 tag = row_load(p, xs, w & 3, lane) + 1;
    const int task = 128 + ((b >> 1) & 3) + w * 4;
    CS ck;
    cs_init(p, ck, task < 156 ? task : 234, lane);
#pragma unroll
    for (int i = 0; i < 32; i++) cs_load(ck, i);
    FENCE();
    if (w < 4) row_stats(xs, rws, w, lane, (unsigned)tag);
#pragma unroll
    for (int i = 0; i < 32; i++) cs_add(ck, i);
    if (task < 156) cs_store(ck, rws, lane, (unsigned)tag);
    STAMP(3);
    BAR();
    if (w != 0) return;
    if (lane == 0) st_flag(p.ws, WS_F0 + b * 8, tag);
    if (b != 248) return;
#pragma unroll 1
    for (int i = 0; i < 4; i++) wait_flag(p.ws, WS_F0 + (i * 64 + lane) * 8, tag, tag - 1);
    if (lane == 0) st_flag(p.ws, WS_GEN, tag);
    return;
  }
  if (w >= 4) {
    __builtin_amdgcn_s_setprio(3);
    if (type == 2) loader<2>(p, sm, rws, w - 4, lane, rt, cg, row0, row2, cg2);
    else if (type == 0) loader<0>(p, sm, rws, w - 4, lane, rt, cg, row0, row2, cg2);
    else loader<1>(p, sm, rws, w - 4, lane, rt, cg, row0, row2, cg2);
  } else {
    if (type == 0) k1_mfma<0>(p, sm, rws, w, lane, rt, cg, row0, pc0);
    else if (type == 1) k1_mfma<1>(p, sm, rws, w, lane, rt, cg, row0, pc0);
    else k1_mfma<2>(p, sm, rws, w, lane, rt, cg, row0, pc0);
    STAMP(7);
    if (type != 1) p2_mfma(p, sm, rws, w, lane, row2, cg2);
  }
  STAMP(11); STAMPR(2);
}

extern "C" void kernel_launch(void* const* d_in, const int* in_sizes, int n_in, void* d_out, int out_size,
                              void* d_ws, size_t ws_size, hipStream_t stream) {
  Params p;
  p.x = (const char*)d_in[0]; p.wqkv = (const char*)d_in[1]; p.wo = (const char*)d_in[2];
  p.wg = (const char*)d_in[4]; p.wu = (const char*)d_in[5]; p.wd = (const char*)d_in[6];
  p.out = (char*)d_out; p.ws = (char*)d_ws;
  (void)hipFuncSetAttribute((const void*)fused_kernel, hipFuncAttributeMaxDynamicSharedMemorySize, LDS_BYTES);
  fused_kernel<<<256, 512, LDS_BYTES, stream>>>(p);
}
```

```cpp
#include <hip/hip_runtime.h>
#include <hip/hip_bf16.h>
#include <stdint.h>

typedef __attribute__((ext_vector_type(8))) __bf16 bf8;
typedef __attribute__((ext_vector_type(2))) __bf16 bf2;
typedef __attribute__((ext_vector_type(4))) short s4;
typedef __attribute__((ext_vector_type(2))) float f2;
typedef __attribute__((ext_vector_type(4))) float f4;
typedef __attribute__((ext_vector_type(16))) float f16v;
typedef __attribute__((ext_vector_type(2))) unsigned u2;
typedef __attribute__((ext_vector_type(4))) unsigned u4;
typedef unsigned long long u64;
typedef __amdgpu_buffer_rsrc_t rsrc_t;
#define LDSP __attribute__((address_space(3)))
#define GLBP __attribute__((address_space(1)))
#define DI __device__ __forceinline__

constexpr int S = 2048, D = 640, F = 1536;
constexpr int NCOL = 4992;
constexpr unsigned WS_GEN = 0;
constexpr unsigned WS_F0 = 4096;
constexpr unsigned WS_FH = 8192;
constexpr unsigned WS_FKV = 12288;
constexpr unsigned WS_FO = 16384;
constexpr unsigned WS_STATS = 32768;
constexpr unsigned WS_CS = 65536;
constexpr unsigned WS_KVP = 327680;
constexpr unsigned WS_H = 1 << 20;
constexpr unsigned WS_O = WS_H + S * F * 2;
constexpr unsigned WS_END = WS_O + S * D * 2;

constexpr int SLOT = 52 * 1024;
constexpr int LDS_BYTES = 160 * 1024;
constexpr int AUX = 3 * SLOT;
constexpr int IMG = 54 * 1024;
constexpr int SC1 = 16;
constexpr unsigned CODE_BYTES = 62 * 1024;

DI rsrc_t mkrs(const void* b, unsigned n) { return __builtin_amdgcn_make_buffer_rsrc((void*)b, (short)0, (int)n, 0x00020000); }
DI void blds16(rsrc_t rs, unsigned voff, unsigned soff, LDSP char* l) {
  __builtin_amdgcn_raw_ptr_buffer_load_lds(rs, (LDSP void*)l, 16, voff, soff, 0, 0);
}
template <int AUXB> DI u4 bld128(rsrc_t rs, unsigned voff, unsigned soff) { return __builtin_amdgcn_raw_buffer_load_b128(rs, voff, soff, AUXB); }
template <int AUXB> DI u2 bld64(rsrc_t rs, unsigned voff, unsigned soff) { return __builtin_amdgcn_raw_buffer_load_b64(rs, voff, soff, AUXB); }
template <int AUXB> DI unsigned bld32(rsrc_t rs, unsigned voff, unsigned soff) { return __builtin_amdgcn_raw_buffer_load_b32(rs, voff, soff, AUXB); }
DI void bst128(u4 v, rsrc_t rs, unsigned voff, unsigned soff) { __builtin_amdgcn_raw_buffer_store_b128(v, rs, voff, soff, SC1); }
DI void bst64(u2 v, rsrc_t rs, unsigned voff, unsigned soff) { __builtin_amdgcn_raw_buffer_store_b64(v, rs, voff, soff, SC1); }
DI u4 lds_r128(LDSP char* p) { return *(LDSP u4*)p; }
DI u2 lds_tr(LDSP char* p) { return __builtin_bit_cast(u2, __builtin_amdgcn_ds_read_tr16_b64_v4i16((LDSP s4*)p)); }
DI f16v mfma32(u4 a, u4 b, f16v c) { return __builtin_amdgcn_mfma_f32_32x32x16_bf16(__builtin_bit_cast(bf8, a), __builtin_bit_cast(bf8, b), c, 0, 0, 0); }
DI unsigned pk2(float lo, float hi) { f2 v = {lo, hi}; return __builtin_bit_cast(unsigned, __builtin_convertvector(v, bf2)); }
DI float blo(unsigned v) { return __uint_as_float(v << 16); }
DI float bhi(unsigned v) { return __uint_as_float(v & 0xffff0000u); }
DI float exp2c(float u) { return __builtin_amdgcn_fmed3f(__builtin_amdgcn_exp2f(u), 0.f, 1.f); }
constexpr float LOG2E = 1.44269504088896f, LN2 = 0.693147180559945f;
constexpr float PHC = 1e-6f * LOG2E;
DI unsigned phi2(f2 u) {
  const f2 e = {exp2c(u[0]), exp2c(u[1])};
  const f2 v = f2{fmaxf(u[0], PHC), fmaxf(u[1], PHC)} * LN2 + e;
  return pk2(v[0], v[1]);
}
DI u64 ld_flag(const char* ws, unsigned off) { return __hip_atomic_load((const GLBP u64*)(ws + off), __ATOMIC_RELAXED, __HIP_MEMORY_SCOPE_AGENT); }
DI void st_flag(char* ws, unsigned off, u64 v) { __hip_atomic_store((GLBP u64*)(ws + off), v, __ATOMIC_RELAXED, __HIP_MEMORY_SCOPE_AGENT); }
#define BAR() __builtin_amdgcn_s_barrier()
#define WAITV(n) asm volatile("s_waitcnt vmcnt(" #n ")" ::: "memory")
#define WAITL0() asm volatile("s_waitcnt lgkmcnt(0)" ::: "memory")
#define SCHED0() __builtin_amdgcn_sched_barrier(0)
#define FENCE() do { asm volatile("" ::: "memory"); __builtin_amdgcn_sched_barrier(0); } while (0)

#define STAMP(i)
#define STAMPR(i)

struct Params {
  const char* x; const char* wqkv; const char* wo; const char* wg; const char* wu; const char* wd;
  char* out; char* ws;
};

DI void wait_flag(const char* ws, unsigned off, u64 tag, u64 first) {
  u64 v = first;
#pragma unroll 1
  for (int it = 0; it < (1 << 18); it++) {
    if (__all(v == tag)) break;
    __builtin_amdgcn_s_sleep(1);
    v = ld_flag(ws, off);
  }
  FENCE();
}
DI u64 lds_tag(LDSP char* sm) {
  const u2 t = *(LDSP u2*)(sm + AUX + 3072);
  return (u64)(unsigned)__builtin_amdgcn_readfirstlane(t[0]) | ((u64)(unsigned)__builtin_amdgcn_readfirstlane(t[1]) << 32);
}

template <int CTL> DI float dppadd(float v) { return v + __int_as_float(__builtin_amdgcn_update_dpp(0, __float_as_int(v), CTL, 0xf, 0xf, true)); }
DI float swap16add(float v) { const u2 t = __builtin_amdgcn_permlane16_swap(__float_as_uint(v), __float_as_uint(v), false, false); return __uint_as_float(t[0]) + __uint_as_float(t[1]); }
DI float sum32(float v) { return swap16add(dppadd<0x121>(dppadd<0x122>(dppadd<0x124>(dppadd<0x128>(v))))); }
constexpr unsigned EMPTY = 0xffffffffu;

DI u64 row_load(const Params& p, u2 (&xs)[5], int w, int lane) {
  const u64 gen = ld_flag(p.ws, WS_GEN);
  const rsrc_t rx = mkrs(p.x, S * D * 2);
  const unsigned xo = (unsigned)((blockIdx.x * 8 + w * 2 + (lane >> 5)) * 1280 + (lane & 31) * 8);
#pragma unroll
  for (int i = 0; i < 5; i++) xs[i] = bld64<0>(rx, xo + i * 256, 0);
  FENCE();
  return gen;
}
DI void row_stats(const u2 (&xs)[5], rsrc_t rws, int w, int lane, unsigned tag) {
  const int b = blockIdx.x;
  float s = 0;
#pragma unroll
  for (int i = 0; i < 5; i++) s += (blo(xs[i][0]) + bhi(xs[i][0])) + (blo(xs[i][1]) + bhi(xs[i][1]));
  s = sum32(s);
  const float mu = s * (1.f / 640.f);
  float q = 0;
#pragma unroll
  for (int i = 0; i < 5; i++) {
    const float a = blo(xs[i][0]) - mu, c = bhi(xs[i][0]) - mu, e = blo(xs[i][1]) - mu, g = bhi(xs[i][1]) - mu;
    q += a * a + c * c + e * e + g * g;
  }
  q = sum32(q);
  const float rs = rsqrtf(q * (1.f / 640.f) + 1e-5f);
  if ((lane & 31) == 0) bst128(u4{__float_as_uint(mu), tag, __float_as_uint(rs), tag}, rws, (b * 8 + w * 2 + (lane >> 5)) * 16, WS_STATS);
}
struct CS { rsrc_t rs; unsigned vo, step, dst; u4 d[32]; float c[8]; };
DI void cs_init(const Params& p, CS& k, int task, int lane) {
  const int kp = task / 78, cu = task - kp * 78;
  const char* base = cu < 30 ? p.wqkv + cu * 128 : (cu < 54 ? p.wg + (cu - 30) * 128 : p.wu + (cu - 54) * 128);
  const unsigned stride = cu < 30 ? 3840 : 3072;
  k.rs = mkrs(base, task < 234 ? 640 * stride - stride + 128 : 0);
  k.vo = (unsigned)(kp * 256 + (lane >> 3)) * stride + (lane & 7) * 16;
  k.step = 8 * stride;
  k.dst = (kp * NCOL + cu * 64 + (lane & 7) * 8 + (lane >> 5) * 4 + ((lane >> 3) & 1) * 2) * 8;
#pragma unroll
  for (int j = 0; j < 8; j++) k.c[j] = 0;
}
DI void cs_load(CS& k, int i) { k.d[i] = bld128<0>(k.rs, k.vo, i * k.step); }
DI void cs_add(CS& k, int i) {
#pragma unroll
  for (int j = 0; j < 4; j++) { k.c[2 * j] += blo(k.d[i][j]); k.c[2 * j + 1] += bhi(k.d[i][j]); }
}
DI void cs_store(CS& k, rsrc_t rws, int lane, unsigned tag) {
  float e[4];
#pragma unroll
  for (int j = 0; j < 4; j++) {
    const u2 t = __builtin_amdgcn_permlane32_swap(__float_as_uint(k.c[j]), __float_as_uint(k.c[4 + j]), false, false);
    e[j] = dppadd<0x128>(swap16add(__uint_as_float(t[0]) + __uint_as_float(t[1])));
  }
  if ((lane & 16) == 0) bst128(u4{__float_as_uint(lane & 8 ? e[2] : e[0]), tag, __float_as_uint(lane & 8 ? e[3] : e[1]), tag}, rws, k.dst, WS_CS);
}

template <int TYPE>
DI void k1_main(LDSP char* sm, int w, int lane, f16v (&acc)[10]) {
  constexpr int NXF = TYPE == 0 ? 5 : 2, NWF = 7 - NXF;
  constexpr int XB = TYPE == 0 ? 160 * 128 : 256 * 128;
  const int r = lane & 31, hh = lane >> 5, blk = (lane >> 4) & 1, q = (lane & 15) >> 2, pp = lane & 3;
  unsigned xa[4];
#pragma unroll
  for (int k = 0; k < 4; k++) xa[k] = (unsigned)(((TYPE == 0 ? 0 : w * 64) + r) * 128 + (((2 * k + hh) ^ ((r >> 1) & 7)) << 4));
  unsigned wa[2];
  if (TYPE == 0) {
#pragma unroll
    for (int ft = 0; ft < 2; ft++)
      wa[ft] = (unsigned)(XB + blk * 16384 + (8 * hh + q) * 256 + (((w * 4 + ft * 2 + (pp >> 1)) ^ (q << 2) ^ (blk << 1)) << 4) + (pp & 1) * 8);
  } else {
    wa[0] = (unsigned)(XB + (8 * hh + q) * 320 + blk * 32 + pp * 8); wa[1] = 0;
  }
  u4 xf[2][NXF], wf[2][NWF];
  auto rd = [&](int buf, LDSP char* base, int k) {
#pragma unroll
    for (int i = 0; i < NWF; i++) {
      u2 lo, hi;
      if (TYPE == 0) { lo = lds_tr(base + wa[i] + k * 4096); hi = lds_tr(base + wa[i] + k * 4096 + 1024); }
      else { lo = lds_tr(base + wa[0] + k * 5120 + i * 64); hi = lds_tr(base + wa[0] + k * 5120 + i * 64 + 1280); }
      wf[buf][i] = u4{lo[0], lo[1], hi[0], hi[1]};
    }
#pragma unroll
    for (int i = 0; i < NXF; i++) xf[buf][i] = lds_r128(base + xa[k] + i * 4096);
  };
  auto mm = [&](int buf) {
#pragma unroll
    for (int i = 0; i < NXF; i++)
#pragma unroll
      for (int j = 0; j < NWF; j++) {
        if (TYPE == 2) acc[i * NWF + j] = mfma32(xf[buf][i], wf[buf][j], acc[i * NWF + j]);
        else acc[i * NWF + j] = mfma32(wf[buf][j], xf[buf][i], acc[i * NWF + j]);
      }
  };
  auto inter = [&]() {
    constexpr int NR = NXF + 2 * NWF;
#pragma unroll
    for (int i = 0; i < 6; i++) {
      __builtin_amdgcn_sched_group_barrier(0x8, 1, 0);
      __builtin_amdgcn_sched_group_barrier(0x100, (NR + 5) / 6, 0);
    }
    __builtin_amdgcn_sched_group_barrier(0x8, 4, 0);
  };
  BAR();
  rd(0, sm, 0);
  int slot = 0;
#pragma unroll 1
  for (int s = 0; s < 9; s++) {
    LDSP char* base = sm + slot * SLOT;
    slot = slot == 2 ? 0 : slot + 1;
    SCHED0();
    rd(1, base, 1); mm(0); inter(); SCHED0();
    rd(0, base, 2); mm(1); inter(); SCHED0();
    rd(1, base, 3); mm(0); inter(); SCHED0();
    WAITL0(); BAR(); SCHED0();
    rd(0, sm + slot * SLOT, 0); mm(1); inter(); SCHED0();
  }
  SCHED0();
  rd(1, sm, 1); mm(0); inter(); SCHED0();
  rd(0, sm, 2); mm(1); inter(); SCHED0();
  rd(1, sm, 3); mm(0); inter(); SCHED0();
  mm(1); SCHED0();
}

DI void kv_publish(LDSP char* sm, rsrc_t rws, int rt, int cg) {
  const int t = threadIdx.x;
  if (t < 420) {
    LDSP char* src = sm + t * 16;
    const f4 s = (*(LDSP f4*)src + *(LDSP f4*)(src + 8192)) + (*(LDSP f4*)(src + 16384) + *(LDSP f4*)(src + 24576));
    u4 v = __builtin_bit_cast(u4, s);
#pragma unroll
    for (int j = 0; j < 4; j++) v[j] = min(v[j], EMPTY - 1);
    bst128(v, rws, t * 16, WS_KVP + (rt * 8 + cg) * 7168);
  }
}

template <int TYPE>
DI void k1_mfma(const Params& p, LDSP char* sm, rsrc_t rws, int w, int lane, int rt, int cg, int row0, u64 pc0) {
  u2 xs[5];
  const u64 gen = row_load(p, xs, w, lane);
  const int hh = lane >> 5, r = lane & 31;
  f16v acc[10];
#pragma unroll
  for (int i = 0; i < 10; i++)
#pragma unroll
    for (int j = 0; j < 16; j++) acc[i][j] = 0.f;
#pragma unroll
  for (int i = 0; i < 10; i++) asm volatile("" : "+v"(acc[i]));
  FENCE();
  {
    const u64 tag = gen + 1;
    row_stats(xs, rws, w, lane, (unsigned)tag);
    if (threadIdx.x == 0) *(LDSP u2*)(sm + AUX + 3072) = u2{(unsigned)tag, (unsigned)(tag >> 32)};
    const u4 rc = {(unsigned)pc0, (unsigned)(pc0 >> 32) & 0xffffu, CODE_BYTES, 0x00020000u};
    asm volatile("s_mov_b32 m0, %0\n\ts_nop 0\n\tbuffer_load_dword %1, %2, 0 offen lds\n\ts_waitcnt lgkmcnt(0)"
                 :: "s"((unsigned)(size_t)(sm + AUX + 3328)), "v"(min(threadIdx.x * 128 + ((blockIdx.x >> 3) & 1) * 32768, CODE_BYTES - 128)), "s"(rc) : "memory", "m0");
    STAMP(12);
  }
  k1_main<TYPE>(sm, w, lane, acc);
  STAMP(3);
  BAR(); FENCE();
  STAMP(19);
  if (TYPE == 0) {
    f4 cs[2][4];
#pragma unroll
    for (int ft = 0; ft < 2; ft++)
#pragma unroll
      for (int qd = 0; qd < 4; qd++)
        cs[ft][qd] = *(LDSP f4*)(sm + AUX + 2048 + ((qd >> 1) * 128 + w * 32 + ft * 16 + (qd & 1) * 8 + hh * 4) * 4);
    const unsigned dst = (unsigned)(((row0 >> 5) * 96 + cg * 8 + w * 2) * 1024 + lane * 16);
#pragma unroll
    for (int tt = 0; tt < 5; tt++) {
      const float mu = *(LDSP float*)(sm + AUX + (tt * 32 + r) * 4);
      if (tt == 0 && rt == 12) continue;
#pragma unroll
      for (int ft = 0; ft < 2; ft++) {
        const f16v a = acc[tt * 2 + ft];
        float h[8];
#pragma unroll
        for (int j = 0; j < 8; j++) h[j] = (a[j] - mu * cs[ft][j >> 2][j & 3]) * (a[8 + j] - mu * cs[ft][2 + (j >> 2)][j & 3]);
        bst128(u4{pk2(h[0], h[1]), pk2(h[2], h[3]), pk2(h[4], h[5]), pk2(h[6], h[7])}, rws, dst + ft * 1024, WS_H + tt * 98304);
      }
    }
    STAMP(4);
    FENCE(); BAR();
    WAITV(0); BAR();
    if (threadIdx.x == 0) st_flag(p.ws, WS_FH + (rt * 16 + cg) * 8, lds_tag(sm));
  } else if (TYPE == 1) {
    u4 pq[2][10];
#pragma unroll
    for (int tt = 0; tt < 2; tt++) {
      const float mu = *(LDSP float*)(sm + AUX + (w * 64 + tt * 32 + r) * 4);
      const float rl = *(LDSP float*)(sm + AUX + 1024 + (w * 64 + tt * 32 + r) * 4) * LOG2E;
#pragma unroll
      for (int ft = 0; ft < 5; ft++) {
        const f16v a = acc[tt * 5 + ft];
#pragma unroll
        for (int qd = 0; qd < 4; qd++) {
          const f4 cs = *(LDSP f4*)(sm + AUX + 2048 + (ft * 32 + qd * 8 + hh * 4) * 4);
#pragma unroll
          for (int j = 0; j < 2; j++) {
            const f2 u = (f2{a[qd * 4 + 2 * j], a[qd * 4 + 2 * j + 1]} - f2{cs[2 * j], cs[2 * j + 1]} * mu) * rl + PHC;
            pq[tt][ft * 2 + (qd >> 1)][(qd & 1) * 2 + j] = phi2(u);
          }
        }
        asm volatile("" : "+v"(pq[tt][ft * 2]), "+v"(pq[tt][ft * 2 + 1]));
      }
    }
    STAMP(4);
    SCHED0(); BAR(); SCHED0();
    STAMP(5);
    const unsigned dst = (unsigned)((((row0 >> 5) + w * 2) * 40 + cg * 10) * 1024 + lane * 16);
    f16v oa[2][2];
    u4 af[2][3];
    auto rdi = [&](int n) {
      const int hg = n / 5, T = n % 5, k0 = T < 2 ? 0 : T - 1, k1 = T == 0 ? 1 : (T == 4 ? 4 : T + 1);
#pragma unroll
      for (int k = k0; k <= k1; k++) af[n & 1][k - k0] = lds_r128(sm + IMG + (hg * 25 + T * 5 + k) * 1024 + lane * 16);
    };
    auto omm = [&](int n) {
      const int hg = n / 5, T = n % 5, k0 = T < 2 ? 0 : T - 1, k1 = T == 0 ? 1 : (T == 4 ? 4 : T + 1);
#pragma unroll
      for (int tt = 0; tt < 2; tt++)
#pragma unroll
        for (int j = 0; j < 16; j++) oa[n & 1][tt][j] = 0.f;
#pragma unroll
      for (int k = k0; k <= k1; k++)
#pragma unroll
        for (int tt = 0; tt < 2; tt++) oa[n & 1][tt] = mfma32(af[n & 1][k - k0], pq[tt][hg * 5 + k], oa[n & 1][tt]);
    };
    rdi(0); rdi(1); omm(0); SCHED0();
#pragma unroll
    for (int n = 0; n < 10; n++) {
      if (n + 1 < 10) omm(n + 1);
      if (n + 2 < 10) rdi(n + 2);
#pragma unroll
      for (int tt = 0; tt < 2; tt++) {
        const f16v a = oa[n & 1][tt];
        const float d0 = __builtin_amdgcn_rcpf(a[8] + 1e-6f), d1 = __builtin_amdgcn_rcpf(a[12] + 1e-6f);
        bst128(u4{pk2(a[0] * d0, a[1] * d0), pk2(a[2] * d0, a[3] * d0), pk2(a[4] * d1, a[5] * d1), pk2(a[6] * d1, a[7] * d1)},
               rws, dst + (n & 3) * 1024, WS_O + tt * 40960 + (n >> 2) * 4096);
      }
#pragma unroll
      for (int i = 0; i < 6; i++) { __builtin_amdgcn_sched_group_barrier(0x8, 1, 0); __builtin_amdgcn_sched_group_barrier(0x2, 5, 0); }
      SCHED0();
    }
    STAMP(6);
    WAITV(0); BAR();
    if (threadIdx.x == 0) st_flag(p.ws, WS_FO + (rt * 16 + cg) * 8, lds_tag(sm));
  } else {
    float cs[5];
#pragma unroll
    for (int ct = 0; ct < 5; ct++) cs[ct] = *(LDSP float*)(sm + AUX + 2048 + (ct * 32 + r) * 4);
    const bool isk = r < 16;
    const float kmul = isk ? 1.f : 0.f, kadd = isk ? 1e-6f : 0.f, klo = isk ? 1e-6f : -__builtin_inff();
    u4 fr[4][5];
#pragma unroll
    for (int g = 0; g < 4; g++) {
      f2 mu[4], rs[4], rl[4];
#pragma unroll
      for (int hq = 0; hq < 2; hq++) {
        LDSP char* sp = sm + AUX + (w * 64 + g * 16 + hq * 8 + hh * 4) * 4;
        const f4 a = *(LDSP f4*)sp, b = *(LDSP f4*)(sp + 1024);
        mu[hq * 2] = f2{a[0], a[1]}; mu[hq * 2 + 1] = f2{a[2], a[3]};
        rs[hq * 2] = f2{b[0], b[1]}; rs[hq * 2 + 1] = f2{b[2], b[3]};
      }
#pragma unroll
      for (int j = 0; j < 4; j++) rl[j] = rs[j] * LOG2E;
#pragma unroll
      for (int ct = 0; ct < 5; ct++) {
#pragma unroll
        for (int j = 0; j < 4; j++) {
          const f16v& a = acc[(g >> 1) * 5 + ct];
          const f2 y = f2{a[(g & 1) * 8 + 2 * j], a[(g & 1) * 8 + 2 * j + 1]} - mu[j] * cs[ct];
          if (ct < 2) fr[g][ct][j] = phi2(y * rl[j] + PHC);
          else if (ct == 2) {
            const f2 u = y * rl[j], t = y * rs[j] + kadd, e = {exp2c(u[0]), exp2c(u[1])};
            const f2 v = e * kmul + f2{fmaxf(t[0], klo), fmaxf(t[1], klo)};
            fr[g][ct][j] = pk2(v[0], v[1]);
          } else { const f2 v = y * rs[j]; fr[g][ct][j] = pk2(v[0], v[1]); }
        }
      }
      SCHED0();
    }
    f16v z[8];
#pragma unroll
    for (int i = 0; i < 8; i++)
#pragma unroll
      for (int j = 0; j < 16; j++) z[i][j] = 0.f;
#pragma unroll
    for (int g = 0; g < 4; g++) {
      u4 mb;
#pragma unroll
      for (int j = 0; j < 4; j++) mb[j] = isk ? 0x3f803f80u : fr[g][2][j];
      z[0] = mfma32(fr[g][0], fr[g][3], z[0]);
      z[1] = mfma32(fr[g][0], fr[g][4], z[1]);
      z[2] = mfma32(fr[g][0], mb, z[2]);
      z[3] = mfma32(fr[g][1], fr[g][3], z[3]);
      z[4] = mfma32(fr[g][1], fr[g][4], z[4]);
      z[5] = mfma32(fr[g][1], mb, z[5]);
      z[6] = mfma32(fr[g][2], fr[g][4], z[6]);
      z[7] = mfma32(fr[g][2], mb, z[7]);
    }
    unsigned pa[3], pm[3];
#pragma unroll
    for (int vt = 0; vt < 3; vt++) {
      const int e = vt == 2 ? 48 + r : vt * 32 + r, he = e / 20;
      int ee = e - he * 20; unsigned m = 31u << (5 * he);
      if (vt == 2 && isk) { ee = 20; m = r == 0 ? 0xfffffu : 0u; }
      pa[vt] = (unsigned)(w * 8192 + ee * 16 + hh * 336); pm[vt] = m >> hh;
    }
    SCHED0();
#pragma unroll
    for (int i = 0; i < 8; i++)
#pragma unroll
      for (int qd = 0; qd < 4; qd++) {
        const int kt = i / 3, vt = i < 6 ? i % 3 : i - 5, c = kt * 8 + qd * 2;
        const bool need = (i == 1) ? qd >= 2 : (i == 3) ? qd == 0 : (i >= 6) ? qd < 2 : true;
        if (need && (pm[vt] & (1u << c)))
          *(LDSP f4*)(sm + pa[vt] + c * 336) = f4{z[i][qd * 4], z[i][qd * 4 + 1], z[i][qd * 4 + 2], z[i][qd * 4 + 3]};
      }
    STAMP(4);
    WAITL0(); BAR();
    kv_publish(sm, rws, rt, cg);
    WAITL0(); BAR();
  }
}

DI void p2_out(const Params& p, LDSP char* sm, int item, u4 xv, unsigned go) {
  const int row = item >> 3, c = item & 7;
  LDSP char* src = sm + (row >> 3) * 8192 + (row & 7) * 256;
  const unsigned c0 = ((2 * c) ^ (row & 7)) << 4, c1 = c0 ^ 16;
  f4 a = *(LDSP f4*)(src + c0), b = *(LDSP f4*)(src + c1);
#pragma unroll
  for (int i = 1; i < 4; i++) { a += *(LDSP f4*)(src + i * 2048 + c0); b += *(LDSP f4*)(src + i * 2048 + c1); }
  *(GLBP u4*)(p.out + go) = u4{pk2(blo(xv[0]) + a[0], bhi(xv[0]) + a[1]), pk2(blo(xv[1]) + a[2], bhi(xv[1]) + a[3]),
                               pk2(blo(xv[2]) + b[0], bhi(xv[2]) + b[1]), pk2(blo(xv[3]) + b[2], bhi(xv[3]) + b[3])};
}

constexpr int PD = 8;
DI void p2_mfma(const Params& p, LDSP char* sm, rsrc_t rws, int w, int lane, int row2, int cg2) {
  const int hh = lane >> 5, r = lane & 31, blk = (lane >> 4) & 1, q = (lane & 15) >> 2, pp = lane & 3;
  const u64 tag = lds_tag(sm);
  const unsigned fh = WS_FH + (((row2 + ((lane >> 2) & 1) * 95) / 160) * 16 + 3 * w + min(lane & 3, 2)) * 8;
  const unsigned fo = WS_FO + (((row2 + (lane & 1) * 95) >> 8) * 16 + w) * 8;
  float r2[3];
  wait_flag(p.ws, fh, tag, ld_flag(p.ws, fh));
  STAMP(8);
  const unsigned ah = (unsigned)(((row2 >> 5) * 96 + w * 24) * 1024 + lane * 16), ao = (unsigned)(((row2 >> 5) * 40 + w * 10) * 1024 + lane * 16);
  u4 af[PD][3], bf[2][2];
  auto lda = [&](int i) {
#pragma unroll
    for (int tt = 0; tt < 3; tt++) {
      if (i < 24) af[i % PD][tt] = bld128<SC1>(rws, ah + (i & 3) * 1024, WS_H + tt * 98304 + (i >> 2) * 4096);
      else af[i % PD][tt] = bld128<SC1>(rws, ao + (i & 3) * 1024, WS_O + tt * 40960 + ((i - 24) >> 2) * 4096);
    }
  };
  LDSP char* ba[2][3];
#pragma unroll
  for (int ct = 0; ct < 2; ct++)
#pragma unroll
    for (int sg = 0; sg < 3; sg++) {
      ba[ct][sg] = sm + sg * 49152 + w * 2048 + (4 * hh + q) * 128 + (((ct * 4 + blk * 2 + (pp >> 1)) ^ ((q >> 1) << 2)) << 4) + (pp & 1) * 8;
      asm volatile("" : "+v"(ba[ct][sg]));
    }
  auto ldb = [&](int i) {
    const int sl = 17 - (i < 18 ? i : i - 18);
#pragma unroll
    for (int ct = 0; ct < 2; ct++) {
      LDSP char* base = ba[ct][sl / 6] + (sl % 6) * 8192;
      const u2 lo = lds_tr(base), hi = lds_tr(base + 1024);
      bf[i & 1][ct] = u4{lo[0], lo[1], hi[0], hi[1]};
    }
  };
  f16v acc[6];
#pragma unroll
  for (int i = 0; i < 6; i++)
#pragma unroll
    for (int j = 0; j < 16; j++) acc[i][j] = 0.f;
#pragma unroll
  for (int i = 0; i < PD; i++) lda(i);
  ldb(0);
  u64 ofl = 0;
  auto step = [&](int i, bool pre) {
    SCHED0();
    if (i == 7 || i == 15 || i == 23) { STAMP(12 + i / 4); WAITL0(); BAR(); STAMP(13 + i / 4); SCHED0(); }
    if (i + 1 < 34) ldb(i + 1);
#pragma unroll
    for (int tt = 0; tt < 3; tt++)
#pragma unroll
      for (int ct = 0; ct < 2; ct++) acc[tt * 2 + ct] = mfma32(bf[i & 1][ct], af[i % PD][tt], acc[tt * 2 + ct]);
    SCHED0();
    if (pre) lda(i + PD);
    if (i == 23) {
#pragma unroll
      for (int tt = 0; tt < 3; tt++) { acc[tt * 2] *= r2[tt] * r2[tt]; acc[tt * 2 + 1] *= r2[tt] * r2[tt]; }
#pragma unroll
      for (int k = 0; k < 6; k++) asm volatile("" : "+v"(acc[k]));
      SCHED0();
    }
  };
  constexpr int OCHK = 20;
#pragma unroll
  for (int i = 0; i < OCHK; i++) {
    step(i, i + PD < 24);
    if (i + PD == 24) {
#pragma unroll
      for (int tt = 0; tt < 3; tt++) r2[tt] = __uint_as_float(bld32<SC1>(rws, (row2 + tt * 32 + r) * 16 + 8, WS_STATS));
      ofl = ld_flag(p.ws, fo);
    }
  }
  STAMP(5);
  const bool early = __all(ofl == tag);
  FENCE();
  if (early) {
#pragma unroll
    for (int j = 24; j < OCHK + PD; j++) lda(j);
  }
#pragma unroll
  for (int i = OCHK; i < 24; i++) step(i, false);
  if (!early) {
    wait_flag(p.ws, fo, tag, ld_flag(p.ws, fo));
#pragma unroll
    for (int j = 24; j < OCHK + PD; j++) lda(j);
  }
  STAMP(9);
#pragma unroll
  for (int j = OCHK + PD; j < 24 + PD; j++) lda(j);
#pragma unroll
  for (int i = 24; i < 34; i++) step(i, i + PD < 34);
  STAMP(10);
  const int item = 512 + threadIdx.x;
  const unsigned go = (unsigned)((row2 + (item >> 3)) * 1280 + cg2 * 128 + (item & 7) * 16);
  const u4 xv = *(const GLBP u4*)(p.x + go);
#pragma unroll
  for (int tt = 0; tt < 3; tt++)
#pragma unroll
    for (int ct = 0; ct < 2; ct++)
#pragma unroll
      for (int qd = 0; qd < 4; qd++)
        *(LDSP f4*)(sm + (tt * 4 + (r >> 3)) * 8192 + w * 2048 + (r & 7) * 256 + (((ct * 8 + qd * 2 + hh) ^ (r & 7)) << 4)) =
            f4{acc[tt * 2 + ct][qd * 4], acc[tt * 2 + ct][qd * 4 + 1], acc[tt * 2 + ct][qd * 4 + 2], acc[tt * 2 + ct][qd * 4 + 3]};
  WAITL0(); BAR();
  p2_out(p, sm, item, xv, go);
}

template <int type>
DI void loader(const Params& p, LDSP char* sm, rsrc_t rws, int lw, int lane, int rt, int cg, int row0, int row2, int cg2) {
  const int b = blockIdx.x, t = lw * 64 + lane;
  constexpr bool gu = type == 0;
  unsigned vo[13];
  {
    const int x0 = gu ? lw * 5 : lw * 8;
    const unsigned xr = (unsigned)((row0 + x0 * 8 + (lane >> 3)) * 1280), xs = (unsigned)(((lane & 7) ^ (lane >> 4) ^ ((x0 & 1) << 2)) << 4);
#pragma unroll
    for (int i = 0; i < 8; i++) vo[i] = xr + (xs ^ ((i & 1) << 6)) + i * 10240;
    if (gu) {
      const unsigned g0 = (unsigned)(((lw & 1) * 32 + (lane >> 4)) * 3072 + (((lane & 15) ^ ((lane >> 4) << 2) ^ (lw & 2)) << 4));
#pragma unroll
      for (int i = 5; i < 13; i++) vo[i] = g0 + (i - 5) * 12288;
    } else {
#pragma unroll
      for (int i = 0; i < 5; i++) {
        const int e = i * 64 + lane, rq = (e * 205) >> 12, pos = e - rq * 20;
        const int adj = type == 1 ? 0 : (pos < 10 ? 0 : (pos < 12 ? 1248 : 1088));
        vo[8 + i] = (unsigned)((lw * 16 + rq) * 3840 + pos * 16 + adj);
      }
    }
  }
  const unsigned wofs = gu ? cg * 256 : (type == 1 ? cg * 320 : 1280 + cg * 160);
  const char* wbase = (gu ? ((lw >> 1) ? p.wu : p.wg) : p.wqkv) + wofs;
  const unsigned wstep = gu ? 64 * 3072 : 64 * 3840, wlen = wstep * 10 - wofs, mstep = gu ? wstep : 128;
  const rsrc_t rx = mkrs(p.x, S * D * 2), rw = mkrs(wbase, wlen), rm = mkrs(gu ? wbase : p.x, gu ? wlen : S * D * 2);
  const int dx = gu ? lw * 5120 : lw * 8192, dm = gu ? 20480 + lw * 8192 : dx + 5120, dw = gu ? dm + 3072 : 32768 + lw * 5120;
  CS ck;
  const int task = type == 1 ? (rt * 4 + cg) * 4 + lw : min(156 + lw * 64 + cg * 8 + rt, 234);
  const bool act = !gu && task < 234;
  auto issue = [&](int s, int a0 = 0, int a1 = 0) {
    LDSP char* d = sm + (s % 3) * SLOT;
#pragma unroll
    for (int i = 0; i < 13; i++) {
      if (i < 5) blds16(rx, vo[i], s * 128, d + dx + i * 1024);
      else if (i < 8) blds16(rm, vo[i], s * mstep, d + dm + (i - 5) * 1024);
      else blds16(rw, vo[i], s * wstep, d + dw + (i - 8) * 1024);
      if (i >= 1 && a0 + i - 1 < a1 && (type == 1 || act)) {
        FENCE(); cs_add(ck, a0 + i - 1);
        asm volatile("" : "+v"(ck.c[0]), "+v"(ck.c[1]), "+v"(ck.c[2]), "+v"(ck.c[3]), "+v"(ck.c[4]), "+v"(ck.c[5]), "+v"(ck.c[6]), "+v"(ck.c[7]));
        FENCE();
      }
    }
    FENCE();
  };
  auto csl = [&](int i0, int i1) {
    if (type == 1 || act) {
#pragma unroll
      for (int i = i0; i < i1; i++) cs_load(ck, i);
      FENCE();
    }
  };
  issue(0); issue(1);
  if (!gu) cs_init(p, ck, task, lane);
  constexpr bool q1 = type == 1;
  STAMP(12);
  WAITV(13); BAR(); STAMP(13);
  if (type == 2) {
    bst128(u4{EMPTY, EMPTY, EMPTY, EMPTY}, rws, t * 16, WS_KVP + (rt * 8 + cg) * 7168);
    bst128(u4{EMPTY, EMPTY, EMPTY, EMPTY}, rws, min(t + 256, 419) * 16, WS_KVP + (rt * 8 + cg) * 7168);
    FENCE();
  }
  issue(2);
  const rsrc_t rd_ = mkrs(p.wd, type == 1 ? 0 : F * D * 2), ro_ = mkrs(p.wo, type == 1 ? 0 : D * D * 2);
  const unsigned bo = (unsigned)((lane >> 3) * 1280 + cg2 * 128 + (((lane & 7) ^ (((lane >> 4) & 1) << 2)) << 4));
  auto bdma = [&](int i) {
#pragma unroll
    for (int h = 0; h < 2; h++) blds16(rd_, bo, (lw * 384 + i * 16 + h * 8) * 1280, sm + (17 - i) * 8192 + lw * 2048 + h * 1024);
  };
  u4 br[46];
  auto breg = [&](int i0, int i1) {
#pragma unroll
    for (int i = i0; i < i1; i++) {
      const int st = 11 + (i >> 1);
      br[i] = st < 24 ? bld128<0>(rd_, bo, (lw * 384 + st * 16 + (i & 1) * 8) * 1280) : bld128<0>(ro_, bo, (lw * 160 + (st - 24) * 16 + (i & 1) * 8) * 1280);
    }
    FENCE();
  };
  auto bput = [&](int i0, int i1) {
#pragma unroll
    for (int i = i0; i < i1; i++) {
      const int st = 11 + (i >> 1), sl = st < 18 ? st : st - 18;
      *(LDSP u4*)(sm + (17 - sl) * 8192 + lw * 2048 + (i & 1) * 1024 + lane * 16) = br[i];
    }
  };
  auto bgu = [&](int i0, int i1) { if (gu) breg(i0, i1); };
  auto bkv = [&](int i0, int i1) { if (type == 2) breg(i0, i1); };
#define WAITG(a, b, c) do { if (gu) WAITV(a); else if (type == 1) WAITV(b); else WAITV(c); } while (0)
#define WAITA(a, b, c, d) do { if (type == 2 && act) WAITV(d); else WAITG(a, b, c); } while (0)
  csl(0, q1 ? 11 : 8); WAITA(13, 24, 15, 23); BAR(); STAMP(14);
  issue(3); csl(q1 ? 11 : 8, q1 ? 22 : 16); WAITA(13, 35, 13, 29); BAR(); STAMP(19);
  const u64 tag = lds_tag(sm);
  if (t < (type == 2 ? 2 : 1)) st_flag(p.ws, t ? WS_FKV + (cg * 8 + rt) * 8 : WS_F0 + b * 8, tag);
  FENCE();
  issue(4, 0, q1 ? 11 : 0); bgu(0, 4); csl(22, q1 ? 32 : 0); WAITA(17, 34, 13, 21); BAR(); STAMP(15);
  issue(5, q1 ? 11 : 0, q1 ? 22 : 8); bgu(4, 8); WAITG(21, 23, 13); BAR(); STAMP(16);
  issue(6, q1 ? 22 : 8, q1 ? 32 : 16); bgu(8, 12);
  if (type == 1 || act) cs_store(ck, rws, lane, (unsigned)tag);
  WAITA(21, 14, 13, 14); BAR(); STAMP(6);
  issue(7); bgu(12, 16); bkv(0, 4);
  unsigned sto = min(row0 + t, S - 1) * 16, cso;
  {
    const int tc = gu ? t : min(t, 159);
    cso = 8 * (gu ? 1920 + (tc >> 7) * 1536 + cg * 128 + (tc & 127)
                  : (type == 1 ? cg * 160 + tc : cg * 80 + tc + (tc < 80 ? 640 : (tc < 96 ? 1264 : 1184))));
  }
  asm volatile("" : "+v"(sto), "+v"(cso));
  WAITA(21, 14, 17, 18); BAR(); STAMP(7);
  issue(8); bgu(16, 20); bkv(4, 8); WAITG(21, 13, 21); BAR(); STAMP(17);
  issue(9); bgu(20, 24); bkv(8, 12); WAITG(21, 13, 21); BAR(); STAMP(18);
  u4 stv; u2 csv[3];
  auto aux = [&]() {
    stv = bld128<SC1>(rws, sto, WS_STATS);
#pragma unroll
    for (int i = 0; i < 3; i++) csv[i] = bld64<SC1>(rws, cso, WS_CS + i * NCOL * 8);
    FENCE();
  };
  aux();
#pragma unroll
  for (int i = 5; i < 11; i++) bdma(i);
  FENCE();
  if (type == 1) *(LDSP u2*)(sm + IMG + t * 8) = u2{0, 0};
  WAITG(20, 16, 20); BAR();
  if (type != 1) {
#pragma unroll
    for (int i = 0; i < 5; i++) bdma(i);
    FENCE();
  }
  auto fresh = [&]() {
    const unsigned tg = (unsigned)tag;
    return __all(stv[1] == tg && stv[3] == tg && csv[0][1] == tg && csv[1][1] == tg && csv[2][1] == tg);
  };
  if (!fresh()) {
#pragma unroll 1
    for (int it = 0; it < (1 << 18); it++) { aux(); if (fresh()) break; }
  }
  *(LDSP unsigned*)(sm + AUX + t * 4) = stv[0];
  *(LDSP unsigned*)(sm + AUX + 1024 + t * 4) = stv[2];
  *(LDSP float*)(sm + AUX + 2048 + t * 4) = (__uint_as_float(csv[0][0]) + __uint_as_float(csv[1][0])) + __uint_as_float(csv[2][0]);
  if (type == 1) WAITV(0);
  FENCE();
  bgu(24, 28);
  WAITL0(); BAR();
  STAMP(3);
  if (type == 1) {
    const unsigned fk = WS_FKV + (cg * 16 + (lane & 15)) * 8;
    wait_flag(p.ws, fk, tag, ld_flag(p.ws, fk));
    u4 pv[4][8];
    unsigned off[4], da[4];
#pragma unroll
    for (int m = 0; m < 4; m++) {
      const int g = min(t + m * 256, 999), dd = g / 25, e5 = g - dd * 25, hg = dd >= 20, dq = dd - hg * 20;
      const int eg = (dq / 5) * 20 + (e5 < 20 ? e5 : (e5 - 20) * 4);
      off[m] = WS_KVP + (cg * 2 + hg) * 7168 + (dq * 21 + min(e5, 20)) * 16;
      da[m] = IMG + hg * 25600 + (dq >> 2) * 1024 + (dq & 1) * 512 + ((dq >> 1) & 1) * 8 + (eg >> 4) * 5120 + (eg & 15) * 16 + (e5 < 20 ? 0 : 256);
      asm volatile("" : "+v"(off[m]), "+v"(da[m]));
#pragma unroll
      for (int i = 0; i < 8; i++) asm volatile("" : "=v"(pv[m][i]));
    }
    const u4 rk = {(unsigned)(size_t)p.ws, (unsigned)((size_t)p.ws >> 32) & 0xffffu, WS_END, 0x00020000u};
    auto fetch = [&](unsigned set) {
#pragma unroll
      for (int i = 0; i < 8; i++)
        if (set >> i & 1)
          asm volatile("buffer_load_dwordx4 %0, %4, %8, %9 offen sc1\n\tbuffer_load_dwordx4 %1, %5, %8, %9 offen sc1\n\t"
                       "buffer_load_dwordx4 %2, %6, %8, %9 offen sc1\n\tbuffer_load_dwordx4 %3, %7, %8, %9 offen sc1"
                       : "+v"(pv[0][i]), "+v"(pv[1][i]), "+v"(pv[2][i]), "+v"(pv[3][i])
                       : "v"(off[0]), "v"(off[1]), "v"(off[2]), "v"(off[3]), "s"(rk), "s"(i * 8 * 7168) : "memory");
    };
    auto landed = [&]() {
      WAITV(0);
#pragma unroll
      for (int i = 0; i < 8; i++) asm volatile("" : "+v"(pv[0][i]), "+v"(pv[1][i]), "+v"(pv[2][i]), "+v"(pv[3][i]));
    };
    {
      const unsigned ho = WS_KVP + ((lane & 7) * 8 + cg * 2 + ((lane >> 3) & 1)) * 7168 + 419 * 16;
      unsigned got = 0;
#pragma unroll 1
      for (int it = 0; it < (1 << 18) && got != 255; it++) {
        const unsigned mk = (unsigned)__ballot(bld32<SC1>(rws, ho, 0) != EMPTY), now = mk & (mk >> 8) & 255 & ~got;
        fetch(now);
        got |= now;
      }
    }
    STAMP(4);
    f4 sum[4];
#pragma unroll 1
    for (int it = 0; it < (1 << 18); it++) {
      landed();
      bool nan = false;
#pragma unroll
      for (int m = 0; m < 4; m++) {
        sum[m] = __builtin_bit_cast(f4, pv[m][0]);
#pragma unroll
        for (int i = 1; i < 8; i++) sum[m] += __builtin_bit_cast(f4, pv[m][i]);
        nan = nan || __builtin_isunordered(sum[m][0], sum[m][1]) || __builtin_isunordered(sum[m][2], sum[m][3]);
      }
      if (!__any(nan)) break;
      unsigned mx = 0;
#pragma unroll
      for (int i = 0; i < 8; i++)
#pragma unroll
        for (int m = 0; m < 4; m++) mx = max(max(mx, max(pv[m][i][0], pv[m][i][1])), max(pv[m][i][2], pv[m][i][3]));
      if (!__any(mx == EMPTY)) break;
      fetch(255);
    }
    STAMP(5);
#pragma unroll
    for (int m = 0; m < 4; m++) *(LDSP u2*)(sm + da[m]) = u2{pk2(sum[m][0], sum[m][1]), pk2(sum[m][2], sum[m][3])};
    WAITL0(); BAR();
    BAR();
    return;
  }
  __builtin_amdgcn_s_setprio(0);
  if (gu) {
    BAR();
    breg(28, 38);
    WAITV(14);
    STAMP(4);
  } else {
    breg(12, 30);
    STAMP(4);
    BAR(); kv_publish(sm, rws, rt, cg); WAITL0();
    WAITV(18);
  }
  BAR();
  bput(0, 14);
  FENCE();
  bkv(30, 46); bgu(38, 46);
  WAITL0(); BAR();
  bput(14, 30);
  WAITL0(); BAR();
  bput(30, 46);
  WAITL0(); BAR();
  unsigned go[2]; u4 xv[2];
#pragma unroll
  for (int i = 0; i < 2; i++) {
    const int item = i * 256 + t;
    go[i] = (unsigned)((row2 + (item >> 3)) * 1280 + cg2 * 128 + (item & 7) * 16);
    xv[i] = *(const GLBP u4*)(p.x + go[i]);
  }
  BAR();
#pragma unroll
  for (int i = 0; i < 2; i++) p2_out(p, sm, i * 256 + t, xv[i], go[i]);
}

__global__ __launch_bounds__(512, 2) void fused_kernel(Params p) {
  extern __shared__ __attribute__((aligned(16))) char smem[];
  LDSP char* sm = (LDSP char*)smem;
  const int lane = threadIdx.x & 63, w = __builtin_amdgcn_readfirstlane(threadIdx.x >> 6), b = blockIdx.x;
  STAMPR(0); STAMP(1);
  u64 pc0 = __builtin_amdgcn_s_getpc();
  asm volatile("" : "+s"(pc0));
  const rsrc_t rws = mkrs(p.ws, WS_END);
  int type, rt, cg, n2;
  {
    const int c = b & 7, j = b >> 3, a = c & 1, q = c >> 1;
    if (j < 12) {
      rt = 4 * a + j / 3; const int m = j % 3;
      if (m == 0) { type = 1; cg = q; } else { type = 2; cg = 2 * q + m - 1; }
      n2 = (j / 3) * 2 + m - 1;
    } else {
      const int n = j - 12 + (a ? 19 : 0);
      type = (!a && j == 31) ? 3 : 0; rt = n / 3; cg = 3 * q + n % 3;
      n2 = j - 4;
    }
    n2 = max(n2 + q * 55 + a * 27, 0);
  }
  const int row0 = type == 0 ? min(rt * 160, S - 160) : rt * 256;
  const int rt2 = n2 / 10, cg2 = n2 - rt2 * 10, row2 = min(rt2 * 96, S - 96);
  if (type == 3) {
    u2 xs[5];
    const u64 tag = row_load(p, xs, w & 3, lane) + 1;
    const int task = 128 + ((b >> 1) & 3) + w * 4;
    CS ck;
    cs_init(p, ck, task < 156 ? task : 234, lane);
#pragma unroll
    for (int i = 0; i < 32; i++) cs_load(ck, i);
    FENCE();
    if (w < 4) row_stats(xs, rws, w, lane, (unsigned)tag);
#pragma unroll
    for (int i = 0; i < 32; i++) cs_add(ck, i);
    if (task < 156) cs_store(ck, rws, lane, (unsigned)tag);
    STAMP(3);
    BAR();
    if (w != 0) return;
    if (lane == 0) st_flag(p.ws, WS_F0 + b * 8, tag);
    if (b != 248) return;
#pragma unroll 1
    for (int i = 0; i < 4; i++) wait_flag(p.ws, WS_F0 + (i * 64 + lane) * 8, tag, tag - 1);
    if (lane == 0) st_flag(p.ws, WS_GEN, tag);
    return;
  }
  if (w >= 4) {
    __builtin_amdgcn_s_setprio(3);
    if (type == 2) loader<2>(p, sm, rws, w - 4, lane, rt, cg, row0, row2, cg2);
    else if (type == 0) loader<0>(p, sm, rws, w - 4, lane, rt, cg, row0, row2, cg2);
    else loader<1>(p, sm, rws, w - 4, lane, rt, cg, row0, row2, cg2);
  } else {
    if (type == 0) k1_mfma<0>(p, sm, rws, w, lane, rt, cg, row0, pc0);
    else if (type == 1) k1_mfma<1>(p, sm, rws, w, lane, rt, cg, row0, pc0);
    else k1_mfma<2>(p, sm, rws, w, lane, rt, cg, row0, pc0);
    STAMP(7);
    if (type != 1) p2_mfma(p, sm, rws, w, lane, row2, cg2);
  }
  STAMP(11); STAMPR(2);
}

extern "C" void kernel_launch(void* const* d_in, const int* in_sizes, int n_in, void* d_out, int out_size,
                              void* d_ws, size_t ws_size, hipStream_t stream) {
  Params p;
  p.x = (const char*)d_in[0]; p.wqkv = (const char*)d_in[1]; p.wo = (const char*)d_in[2];
  p.wg = (const char*)d_in[4]; p.wu = (const char*)d_in[5]; p.wd = (const char*)d_in[6];
  p.out = (char*)d_out; p.ws = (char*)d_ws;
  (void)hipFuncSetAttribute((const void*)fused_kernel, hipFuncAttributeMaxDynamicSharedMemorySize, LDS_BYTES);
  fused_kernel<<<256, 512, LDS_BYTES, stream>>>(p);
}
```

```cpp
#include <hip/hip_runtime.h>
#include <hip/hip_bf16.h>
#include <stdint.h>

typedef __attribute__((ext_vector_type(8))) __bf16 bf8;
typedef __attribute__((ext_vector_type(2))) __bf16 bf2;
typedef __attribute__((ext_vector_type(4))) short s4;
typedef __attribute__((ext_vector_type(2))) float f2;
typedef __attribute__((ext_vector_type(4))) float f4;
typedef __attribute__((ext_vector_type(16))) float f16v;
typedef __attribute__((ext_vector_type(2))) unsigned u2;
typedef __attribute__((ext_vector_type(4))) unsigned u4;
typedef unsigned long long u64;
typedef __amdgpu_buffer_rsrc_t rsrc_t;
#define LDSP __attribute__((address_space(3)))
#define GLBP __attribute__((address_space(1)))
#define DI __device__ __forceinline__

constexpr int S = 2048, D = 640, F = 1536;
constexpr int NCOL = 4992;
constexpr unsigned WS_GEN = 0;
constexpr unsigned WS_F0 = 4096;
constexpr unsigned WS_FH = 8192;
constexpr unsigned WS_FKV = 12288;
constexpr unsigned WS_FO = 16384;
constexpr unsigned WS_STATS = 32768;
constexpr unsigned WS_CS = 65536;
constexpr unsigned WS_KVP = 327680;
constexpr unsigned WS_H = 1 << 20;
constexpr unsigned WS_O = WS_H + S * F * 2;
constexpr unsigned WS_END = WS_O + S * D * 2;

constexpr int SLOT = 52 * 1024;
constexpr int LDS_BYTES = 160 * 1024;
constexpr int AUX = 3 * SLOT;
constexpr int IMG = 54 * 1024;
constexpr int SC1 = 16;
constexpr unsigned CODE_BYTES = 62 * 1024;

DI rsrc_t mkrs(const void* b, unsigned n) { return __builtin_amdgcn_make_buffer_rsrc((void*)b, (short)0, (int)n, 0x00020000); }
DI void blds16(rsrc_t rs, unsigned voff, unsigned soff, LDSP char* l) {
  __builtin_amdgcn_raw_ptr_buffer_load_lds(rs, (LDSP void*)l, 16, voff, soff, 0, 0);
}
template <int AUXB> DI u4 bld128(rsrc_t rs, unsigned voff, unsigned soff) { return __builtin_amdgcn_raw_buffer_load_b128(rs, voff, soff, AUXB); }
template <int AUXB> DI u2 bld64(rsrc_t rs, unsigned voff, unsigned soff) { return __builtin_amdgcn_raw_buffer_load_b64(rs, voff, soff, AUXB); }
template <int AUXB> DI unsigned bld32(rsrc_t rs, unsigned voff, unsigned soff) { return __builtin_amdgcn_raw_buffer_load_b32(rs, voff, soff, AUXB); }
DI void bst128(u4 v, rsrc_t rs, unsigned voff, unsigned soff) { __builtin_amdgcn_raw_buffer_store_b128(v, rs, voff, soff, SC1); }
DI void bst64(u2 v, rsrc_t rs, unsigned voff, unsigned soff) { __builtin_amdgcn_raw_buffer_store_b64(v, rs, voff, soff, SC1); }
DI u4 lds_r128(LDSP char* p) { return *(LDSP u4*)p; }
DI u2 lds_tr(LDSP char* p) { return __builtin_bit_cast(u2, __builtin_amdgcn_ds_read_tr16_b64_v4i16((LDSP s4*)p)); }
DI f16v mfma32(u4 a, u4 b, f16v c) { return __builtin_amdgcn_mfma_f32_32x32x16_bf16(__builtin_bit_cast(bf8, a), __builtin_bit_cast(bf8, b), c, 0, 0, 0); }
DI unsigned pk2(float lo, float hi) { f2 v = {lo, hi}; return __builtin_bit_cast(unsigned, __builtin_convertvector(v, bf2)); }
DI float blo(unsigned v) { return __uint_as_float(v << 16); }
DI float bhi(unsigned v) { return __uint_as_float(v & 0xffff0000u); }
DI float exp2c(float u) { return __builtin_amdgcn_fmed3f(__builtin_amdgcn_exp2f(u), 0.f, 1.f); }
constexpr float LOG2E = 1.44269504088896f, LN2 = 0.693147180559945f;
constexpr float PHC = 1e-6f * LOG2E;
DI unsigned phi2(f2 u) {
  const f2 e = {exp2c(u[0]), exp2c(u[1])};
  const f2 v = f2{fmaxf(u[0], PHC), fmaxf(u[1], PHC)} * LN2 + e;
  return pk2(v[0], v[1]);
}
DI u64 ld_flag(const char* ws, unsigned off) { return __hip_atomic_load((const GLBP u64*)(ws + off), __ATOMIC_RELAXED, __HIP_MEMORY_SCOPE_AGENT); }
DI void st_flag(char* ws, unsigned off, u64 v) { __hip_atomic_store((GLBP u64*)(ws + off), v, __ATOMIC_RELAXED, __HIP_MEMORY_SCOPE_AGENT); }
#define BAR() __builtin_amdgcn_s_barrier()
#define WAITV(n) asm volatile("s_waitcnt vmcnt(" #n ")" ::: "memory")
#define WAITL0() asm volatile("s_waitcnt lgkmcnt(0)" ::: "memory")
#define SCHED0() __builtin_amdgcn_sched_barrier(0)
#define FENCE() do { asm volatile("" ::: "memory"); __builtin_amdgcn_sched_barrier(0); } while (0)

#define STAMP(i)
#define STAMPR(i)

struct Params {
  const char* x; const char* wqkv; const char* wo; const char* wg; const char* wu; const char* wd;
  char* out; char* ws;
};

DI void wait_flag(const char* ws, unsigned off, u64 tag, u64 first) {
  u64 v = first;
#pragma unroll 1
  for (int it = 0; it < (1 << 18); it++) {
    if (__all(v == tag)) break;
    __builtin_amdgcn_s_sleep(1);
    v = ld_flag(ws, off);
  }
  FENCE();
}
DI u64 lds_tag(LDSP char* sm) {
  const u2 t = *(LDSP u2*)(sm + AUX + 3072);
  return (u64)(unsigned)__builtin_amdgcn_readfirstlane(t[0]) | ((u64)(unsigned)__builtin_amdgcn_readfirstlane(t[1]) << 32);
}

template <int CTL> DI float dppadd(float v) { return v + __int_as_float(__builtin_amdgcn_update_dpp(0, __float_as_int(v), CTL, 0xf, 0xf, true)); }
DI float swap16add(float v) { const u2 t = __builtin_amdgcn_permlane16_swap(__float_as_uint(v), __float_as_uint(v), false, false); return __uint_as_float(t[0]) + __uint_as_float(t[1]); }
DI float sum32(float v) { return swap16add(dppadd<0x121>(dppadd<0x122>(dppadd<0x124>(dppadd<0x128>(v))))); }
constexpr unsigned EMPTY = 0xffffffffu;

DI u64 row_load(const Params& p, u2 (&xs)[5], int w, int lane) {
  const u64 gen = ld_flag(p.ws, WS_GEN);
  const rsrc_t rx = mkrs(p.x, S * D * 2);
  const unsigned xo = (unsigned)((blockIdx.x * 8 + w * 2 + (lane >> 5)) * 1280 + (lane & 31) * 8);
#pragma unroll
  for (int i = 0; i < 5; i++) xs[i] = bld64<0>(rx, xo + i * 256, 0);
  FENCE();
  return gen;
}
DI void row_stats(const u2 (&xs)[5], rsrc_t rws, int w, int lane, unsigned tag) {
  const int b = blockIdx.x;
  float s = 0;
#pragma unroll
  for (int i = 0; i < 5; i++) s += (blo(xs[i][0]) + bhi(xs[i][0])) + (blo(xs[i][1]) + bhi(xs[i][1]));
  s = sum32(s);
  const float mu = s * (1.f / 640.f);
  float q = 0;
#pragma unroll
  for (int i = 0; i < 5; i++) {
    const float a = blo(xs[i][0]) - mu, c = bhi(xs[i][0]) - mu, e = blo(xs[i][1]) - mu, g = bhi(xs[i][1]) - mu;
    q += a * a + c * c + e * e + g * g;
  }
  q = sum32(q);
  const float rs = rsqrtf(q * (1.f / 640.f) + 1e-5f);
  if ((lane & 31) == 0) bst128(u4{__float_as_uint(mu), tag, __float_as_uint(rs), tag}, rws, (b * 8 + w * 2 + (lane >> 5)) * 16, WS_STATS);
}
struct CS { rsrc_t rs; unsigned vo, step, dst; u4 d[32]; float c[8]; };
DI void cs_init(const Params& p, CS& k, int task, int lane) {
  const int kp = task / 78, cu = task - kp * 78;
  const char* base = cu < 30 ? p.wqkv + cu * 128 : (cu < 54 ? p.wg + (cu - 30) * 128 : p.wu + (cu - 54) * 128);
  const unsigned stride = cu < 30 ? 3840 : 3072;
  k.rs = mkrs(base, task < 234 ? 640 * stride - stride + 128 : 0);
  k.vo = (unsigned)(kp * 256 + (lane >> 3)) * stride + (lane & 7) * 16;
  k.step = 8 * stride;
  k.dst = (kp * NCOL + cu * 64 + (lane & 7) * 8 + (lane >> 5) * 4 + ((lane >> 3) & 1) * 2) * 8;
#pragma unroll
  for (int j = 0; j < 8; j++) k.c[j] = 0;
}
DI void cs_load(CS& k, int i) { k.d[i] = bld128<0>(k.rs, k.vo, i * k.step); }
DI void cs_add(CS& k, int i) {
#pragma unroll
  for (int j = 0; j < 4; j++) { k.c[2 * j] += blo(k.d[i][j]); k.c[2 * j + 1] += bhi(k.d[i][j]); }
}
DI void cs_store(CS& k, rsrc_t rws, int lane, unsigned tag) {
  float e[4];
#pragma unroll
  for (int j = 0; j < 4; j++) {
    const u2 t = __builtin_amdgcn_permlane32_swap(__float_as_uint(k.c[j]), __float_as_uint(k.c[4 + j]), false, false);
    e[j] = dppadd<0x128>(swap16add(__uint_as_float(t[0]) + __uint_as_float(t[1])));
  }
  if ((lane & 16) == 0) bst128(u4{__float_as_uint(lane & 8 ? e[2] : e[0]), tag, __float_as_uint(lane & 8 ? e[3] : e[1]), tag}, rws, k.dst, WS_CS);
}

template <int TYPE>
DI void k1_main(LDSP char* sm, int w, int lane, f16v (&acc)[10]) {
  constexpr int NXF = TYPE == 0 ? 5 : 2, NWF = 7 - NXF;
  constexpr int XB = TYPE == 0 ? 160 * 128 : 256 * 128;
  const int r = lane & 31, hh = lane >> 5, blk = (lane >> 4) & 1, q = (lane & 15) >> 2, pp = lane & 3;
  unsigned xa[4];
#pragma unroll
  for (int k = 0; k < 4; k++) xa[k] = (unsigned)(((TYPE == 0 ? 0 : w * 64) + r) * 128 + (((2 * k + hh) ^ ((r >> 1) & 7)) << 4));
  unsigned wa[2];
  if (TYPE == 0) {
#pragma unroll
    for (int ft = 0; ft < 2; ft++)
      wa[ft] = (unsigned)(XB + blk * 16384 + (8 * hh + q) * 256 + (((w * 4 + ft * 2 + (pp >> 1)) ^ (q << 2) ^ (blk << 1)) << 4) + (pp & 1) * 8);
  } else {
    wa[0] = (unsigned)(XB + (8 * hh + q) * 320 + blk * 32 + pp * 8); wa[1] = 0;
  }
  u4 xf[2][NXF], wf[2][NWF];
  auto rd = [&](int buf, LDSP char* base, int k) {
#pragma unroll
    for (int i = 0; i < NWF; i++) {
      u2 lo, hi;
      if (TYPE == 0) { lo = lds_tr(base + wa[i] + k * 4096); hi = lds_tr(base + wa[i] + k * 4096 + 1024); }
      else { lo = lds_tr(base + wa[0] + k * 5120 + i * 64); hi = lds_tr(base + wa[0] + k * 5120 + i * 64 + 1280); }
      wf[buf][i] = u4{lo[0], lo[1], hi[0], hi[1]};
    }
#pragma unroll
    for (int i = 0; i < NXF; i++) xf[buf][i] = lds_r128(base + xa[k] + i * 4096);
  };
  auto mm = [&](int buf) {
#pragma unroll
    for (int i = 0; i < NXF; i++)
#pragma unroll
      for (int j = 0; j < NWF; j++) {
        if (TYPE == 2) acc[i * NWF + j] = mfma32(xf[buf][i], wf[buf][j], acc[i * NWF + j]);
        else acc[i * NWF + j] = mfma32(wf[buf][j], xf[buf][i], acc[i * NWF + j]);
      }
  };
  auto inter = [&]() {
    constexpr int NR = NXF + 2 * NWF;
#pragma unroll
    for (int i = 0; i < 6; i++) {
      __builtin_amdgcn_sched_group_barrier(0x8, 1, 0);
      __builtin_amdgcn_sched_group_barrier(0x100, (NR + 5) / 6, 0);
    }
    __builtin_amdgcn_sched_group_barrier(0x8, 4, 0);
  };
  BAR();
  rd(0, sm, 0);
  int slot = 0;
#pragma unroll 1
  for (int s = 0; s < 9; s++) {
    LDSP char* base = sm + slot * SLOT;
    slot = slot == 2 ? 0 : slot + 1;
    SCHED0();
    rd(1, base, 1); mm(0); inter(); SCHED0();
    rd(0, base, 2); mm(1); inter(); SCHED0();
    rd(1, base, 3); mm(0); inter(); SCHED0();
    WAITL0(); BAR(); SCHED0();
    rd(0, sm + slot * SLOT, 0); mm(1); inter(); SCHED0();
  }
  SCHED0();
  rd(1, sm, 1); mm(0); inter(); SCHED0();
  rd(0, sm, 2); mm(1); inter(); SCHED0();
  rd(1, sm, 3); mm(0); inter(); SCHED0();
  mm(1); SCHED0();
}

DI void kv_publish(LDSP char* sm, rsrc_t rws, int rt, int cg) {
  const int t = threadIdx.x;
  if (t < 420) {
    LDSP char* src = sm + t * 16;
    const f4 s = (*(LDSP f4*)src + *(LDSP f4*)(src + 8192)) + (*(LDSP f4*)(src + 16384) + *(LDSP f4*)(src + 24576));
    u4 v = __builtin_bit_cast(u4, s);
#pragma unroll
    for (int j = 0; j < 4; j++) v[j] = min(v[j], EMPTY - 1);
    bst128(v, rws, t * 16, WS_KVP + (rt * 8 + cg) * 7168);
  }
}

template <int TYPE>
DI void k1_mfma(const Params& p, LDSP char* sm, rsrc_t rws, int w, int lane, int rt, int cg, int row0) {
  u2 xs[5];
  const u64 gen = row_load(p, xs, w, lane);
  const int hh = lane >> 5, r = lane & 31;
  f16v acc[10];
#pragma unroll
  for (int i = 0; i < 10; i++)
#pragma unroll
    for (int j = 0; j < 16; j++) acc[i][j] = 0.f;
#pragma unroll
  for (int i = 0; i < 10; i++) asm volatile("" : "+v"(acc[i]));
  FENCE();
  {
    const u64 tag = gen + 1;
    row_stats(xs, rws, w, lane, (unsigned)tag);
    if (threadIdx.x == 0) *(LDSP u2*)(sm + AUX + 3072) = u2{(unsigned)tag, (unsigned)(tag >> 32)};
    WAITL0();
    const u64 wa = (u64)p.ws, oa = (u64)p.out;
    const u4 r1 = {(unsigned)wa, (unsigned)(wa >> 32) & 0xffffu, WS_END, 0x00020000u}, r2 = {(unsigned)oa, (unsigned)(oa >> 32) & 0xffffu, S * D * 2, 0x00020000u};
    if (TYPE == 0 && (blockIdx.x >> 3) == 12)
    asm volatile("s_mov_b32 m0, %4\n\ts_nop 0\n\tbuffer_load_dword %0, %2, 0 offen sc1 lds\n\tbuffer_load_dword %1, %3, 0 offen sc1 lds"
                 :: "v"(threadIdx.x * 38912), "v"(threadIdx.x * 10240), "s"(r1), "s"(r2), "s"((unsigned)(size_t)(sm + AUX + 3328)) : "memory", "m0");
    STAMP(12);
  }
  k1_main<TYPE>(sm, w, lane, acc);
  STAMP(3);
  BAR(); FENCE();
  STAMP(19);
  if (TYPE == 0) {
    f4 cs[2][4];
#pragma unroll
    for (int ft = 0; ft < 2; ft++)
#pragma unroll
      for (int qd = 0; qd < 4; qd++)
        cs[ft][qd] = *(LDSP f4*)(sm + AUX + 2048 + ((qd >> 1) * 128 + w * 32 + ft * 16 + (qd & 1) * 8 + hh * 4) * 4);
    const unsigned dst = (unsigned)(((row0 >> 5) * 96 + cg * 8 + w * 2) * 1024 + lane * 16);
#pragma unroll
    for (int tt = 0; tt < 5; tt++) {
      const float mu = *(LDSP float*)(sm + AUX + (tt * 32 + r) * 4);
      if (tt == 0 && rt == 12) continue;
#pragma unroll
      for (int ft = 0; ft < 2; ft++) {
        const f16v a = acc[tt * 2 + ft];
        float h[8];
#pragma unroll
        for (int j = 0; j < 8; j++) h[j] = (a[j] - mu * cs[ft][j >> 2][j & 3]) * (a[8 + j] - mu * cs[ft][2 + (j >> 2)][j & 3]);
        bst128(u4{pk2(h[0], h[1]), pk2(h[2], h[3]), pk2(h[4], h[5]), pk2(h[6], h[7])}, rws, dst + ft * 1024, WS_H + tt * 98304);
      }
    }
    STAMP(4);
    FENCE(); BAR();
    WAITV(0); BAR();
    if (threadIdx.x == 0) st_flag(p.ws, WS_FH + (rt * 16 + cg) * 8, lds_tag(sm));
  } else if (TYPE == 1) {
    u4 pq[2][10];
#pragma unroll
    for (int tt = 0; tt < 2; tt++) {
      const float mu = *(LDSP float*)(sm + AUX + (w * 64 + tt * 32 + r) * 4);
      const float rl = *(LDSP float*)(sm + AUX + 1024 + (w * 64 + tt * 32 + r) * 4) * LOG2E;
#pragma unroll
      for (int ft = 0; ft < 5; ft++) {
        const f16v a = acc[tt * 5 + ft];
#pragma unroll
        for (int qd = 0; qd < 4; qd++) {
          const f4 cs = *(LDSP f4*)(sm + AUX + 2048 + (ft * 32 + qd * 8 + hh * 4) * 4);
#pragma unroll
          for (int j = 0; j < 2; j++) {
            const f2 u = (f2{a[qd * 4 + 2 * j], a[qd * 4 + 2 * j + 1]} - f2{cs[2 * j], cs[2 * j + 1]} * mu) * rl + PHC;
            pq[tt][ft * 2 + (qd >> 1)][(qd & 1) * 2 + j] = phi2(u);
          }
        }
        asm volatile("" : "+v"(pq[tt][ft * 2]), "+v"(pq[tt][ft * 2 + 1]));
      }
    }
    STAMP(4);
    SCHED0(); BAR(); SCHED0();
    STAMP(5);
    const unsigned dst = (unsigned)((((row0 >> 5) + w * 2) * 40 + cg * 10) * 1024 + lane * 16);
    f16v oa[2][2];
    u4 af[2][3];
    auto rdi = [&](int n) {
      const int hg = n / 5, T = n % 5, k0 = T < 2 ? 0 : T - 1, k1 = T == 0 ? 1 : (T == 4 ? 4 : T + 1);
#pragma unroll
      for (int k = k0; k <= k1; k++) af[n & 1][k - k0] = lds_r128(sm + IMG + (hg * 25 + T * 5 + k) * 1024 + lane * 16);
    };
    auto omm = [&](int n) {
      const int hg = n / 5, T = n % 5, k0 = T < 2 ? 0 : T - 1, k1 = T == 0 ? 1 : (T == 4 ? 4 : T + 1);
#pragma unroll
      for (int tt = 0; tt < 2; tt++)
#pragma unroll
        for (int j = 0; j < 16; j++) oa[n & 1][tt][j] = 0.f;
#pragma unroll
      for (int k = k0; k <= k1; k++)
#pragma unroll
        for (int tt = 0; tt < 2; tt++) oa[n & 1][tt] = mfma32(af[n & 1][k - k0], pq[tt][hg * 5 + k], oa[n & 1][tt]);
    };
    rdi(0); rdi(1); omm(0); SCHED0();
#pragma unroll
    for (int n = 0; n < 10; n++) {
      if (n + 1 < 10) omm(n + 1);
      if (n + 2 < 10) rdi(n + 2);
#pragma unroll
      for (int tt = 0; tt < 2; tt++) {
        const f16v a = oa[n & 1][tt];
        const float d0 = __builtin_amdgcn_rcpf(a[8] + 1e-6f), d1 = __builtin_amdgcn_rcpf(a[12] + 1e-6f);
        bst128(u4{pk2(a[0] * d0, a[1] * d0), pk2(a[2] * d0, a[3] * d0), pk2(a[4] * d1, a[5] * d1), pk2(a[6] * d1, a[7] * d1)},
               rws, dst + (n & 3) * 1024, WS_O + tt * 40960 + (n >> 2) * 4096);
      }
#pragma unroll
      for (int i = 0; i < 6; i++) { __builtin_amdgcn_sched_group_barrier(0x8, 1, 0); __builtin_amdgcn_sched_group_barrier(0x2, 5, 0); }
      SCHED0();
    }
    STAMP(6);
    WAITV(0); BAR();
    if (threadIdx.x == 0) st_flag(p.ws, WS_FO + (rt * 16 + cg) * 8, lds_tag(sm));
  } else {
    float cs[5];
#pragma unroll
    for (int ct = 0; ct < 5; ct++) cs[ct] = *(LDSP float*)(sm + AUX + 2048 + (ct * 32 + r) * 4);
    const bool isk = r < 16;
    const float kmul = isk ? 1.f : 0.f, kadd = isk ? 1e-6f : 0.f, klo = isk ? 1e-6f : -__builtin_inff();
    u4 fr[4][5];
#pragma unroll
    for (int g = 0; g < 4; g++) {
      f2 mu[4], rs[4], rl[4];
#pragma unroll
      for (int hq = 0; hq < 2; hq++) {
        LDSP char* sp = sm + AUX + (w * 64 + g * 16 + hq * 8 + hh * 4) * 4;
        const f4 a = *(LDSP f4*)sp, b = *(LDSP f4*)(sp + 1024);
        mu[hq * 2] = f2{a[0], a[1]}; mu[hq * 2 + 1] = f2{a[2], a[3]};
        rs[hq * 2] = f2{b[0], b[1]}; rs[hq * 2 + 1] = f2{b[2], b[3]};
      }
#pragma unroll
      for (int j = 0; j < 4; j++) rl[j] = rs[j] * LOG2E;
#pragma unroll
      for (int ct = 0; ct < 5; ct++) {
#pragma unroll
        for (int j = 0; j < 4; j++) {
          const f16v& a = acc[(g >> 1) * 5 + ct];
          const f2 y = f2{a[(g & 1) * 8 + 2 * j], a[(g & 1) * 8 + 2 * j + 1]} - mu[j] * cs[ct];
          if (ct < 2) fr[g][ct][j] = phi2(y * rl[j] + PHC);
          else if (ct == 2) {
            const f2 u = y * rl[j], t = y * rs[j] + kadd, e = {exp2c(u[0]), exp2c(u[1])};
            const f2 v = e * kmul + f2{fmaxf(t[0], klo), fmaxf(t[1], klo)};
            fr[g][ct][j] = pk2(v[0], v[1]);
          } else { const f2 v = y * rs[j]; fr[g][ct][j] = pk2(v[0], v[1]); }
        }
      }
      SCHED0();
    }
    f16v z[8];
#pragma unroll
    for (int i = 0; i < 8; i++)
#pragma unroll
      for (int j = 0; j < 16; j++) z[i][j] = 0.f;
#pragma unroll
    for (int g = 0; g < 4; g++) {
      u4 mb;
#pragma unroll
      for (int j = 0; j < 4; j++) mb[j] = isk ? 0x3f803f80u : fr[g][2][j];
      z[0] = mfma32(fr[g][0], fr[g][3], z[0]);
      z[1] = mfma32(fr[g][0], fr[g][4], z[1]);
      z[2] = mfma32(fr[g][0], mb, z[2]);
      z[3] = mfma32(fr[g][1], fr[g][3], z[3]);
      z[4] = mfma32(fr[g][1], fr[g][4], z[4]);
      z[5] = mfma32(fr[g][1], mb, z[5]);
      z[6] = mfma32(fr[g][2], fr[g][4], z[6]);
      z[7] = mfma32(fr[g][2], mb, z[7]);
    }
    unsigned pa[3], pm[3];
#pragma unroll
    for (int vt = 0; vt < 3; vt++) {
      const int e = vt == 2 ? 48 + r : vt * 32 + r, he = e / 20;
      int ee = e - he * 20; unsigned m = 31u << (5 * he);
      if (vt == 2 && isk) { ee = 20; m = r == 0 ? 0xfffffu : 0u; }
      pa[vt] = (unsigned)(w * 8192 + ee * 16 + hh * 336); pm[vt] = m >> hh;
    }
    SCHED0();
#pragma unroll
    for (int i = 0; i < 8; i++)
#pragma unroll
      for (int qd = 0; qd < 4; qd++) {
        const int kt = i / 3, vt = i < 6 ? i % 3 : i - 5, c = kt * 8 + qd * 2;
        const bool need = (i == 1) ? qd >= 2 : (i == 3) ? qd == 0 : (i >= 6) ? qd < 2 : true;
        if (need && (pm[vt] & (1u << c)))
          *(LDSP f4*)(sm + pa[vt] + c * 336) = f4{z[i][qd * 4], z[i][qd * 4 + 1], z[i][qd * 4 + 2], z[i][qd * 4 + 3]};
      }
    STAMP(4);
    WAITL0(); BAR();
    kv_publish(sm, rws, rt, cg);
    WAITL0(); BAR();
  }
}

DI void p2_out(const Params& p, LDSP char* sm, int item, u4 xv, unsigned go) {
  const int row = item >> 3, c = item & 7;
  LDSP char* src = sm + (row >> 3) * 8192 + (row & 7) * 256;
  const unsigned c0 = ((2 * c) ^ (row & 7)) << 4, c1 = c0 ^ 16;
  f4 a = *(LDSP f4*)(src + c0), b = *(LDSP f4*)(src + c1);
#pragma unroll
  for (int i = 1; i < 4; i++) { a += *(LDSP f4*)(src + i * 2048 + c0); b += *(LDSP f4*)(src + i * 2048 + c1); }
  bst128(u4{pk2(blo(xv[0]) + a[0], bhi(xv[0]) + a[1]), pk2(blo(xv[1]) + a[2], bhi(xv[1]) + a[3]),
            pk2(blo(xv[2]) + b[0], bhi(xv[2]) + b[1]), pk2(blo(xv[3]) + b[2], bhi(xv[3]) + b[3])}, mkrs(p.out, S * D * 2), go, 0);
}

constexpr int PD = 8;
DI void p2_mfma(const Params& p, LDSP char* sm, rsrc_t rws, int w, int lane, int row2, int cg2) {
  const int hh = lane >> 5, r = lane & 31, blk = (lane >> 4) & 1, q = (lane & 15) >> 2, pp = lane & 3;
  const u64 tag = lds_tag(sm);
  const unsigned fh = WS_FH + (((row2 + ((lane >> 2) & 1) * 95) / 160) * 16 + 3 * w + min(lane & 3, 2)) * 8;
  const unsigned fo = WS_FO + (((row2 + (lane & 1) * 95) >> 8) * 16 + w) * 8;
  float r2[3];
  wait_flag(p.ws, fh, tag, ld_flag(p.ws, fh));
  STAMP(8);
  const unsigned ah = (unsigned)(((row2 >> 5) * 96 + w * 24) * 1024 + lane * 16), ao = (unsigned)(((row2 >> 5) * 40 + w * 10) * 1024 + lane * 16);
  u4 af[PD][3], bf[2][2];
  auto lda = [&](int i) {
#pragma unroll
    for (int tt = 0; tt < 3; tt++) {
      if (i < 24) af[i % PD][tt] = bld128<SC1>(rws, ah + (i & 3) * 1024, WS_H + tt * 98304 + (i >> 2) * 4096);
      else af[i % PD][tt] = bld128<SC1>(rws, ao + (i & 3) * 1024, WS_O + tt * 40960 + ((i - 24) >> 2) * 4096);
    }
  };
  LDSP char* ba[2][3];
#pragma unroll
  for (int ct = 0; ct < 2; ct++)
#pragma unroll
    for (int sg = 0; sg < 3; sg++) {
      ba[ct][sg] = sm + sg * 49152 + w * 2048 + (4 * hh + q) * 128 + (((ct * 4 + blk * 2 + (pp >> 1)) ^ ((q >> 1) << 2)) << 4) + (pp & 1) * 8;
      asm volatile("" : "+v"(ba[ct][sg]));
    }
  auto ldb = [&](int i) {
    const int sl = 17 - (i < 18 ? i : i - 18);
#pragma unroll
    for (int ct = 0; ct < 2; ct++) {
      LDSP char* base = ba[ct][sl / 6] + (sl % 6) * 8192;
      const u2 lo = lds_tr(base), hi = lds_tr(base + 1024);
      bf[i & 1][ct] = u4{lo[0], lo[1], hi[0], hi[1]};
    }
  };
  f16v acc[6];
#pragma unroll
  for (int i = 0; i < 6; i++)
#pragma unroll
    for (int j = 0; j < 16; j++) acc[i][j] = 0.f;
#pragma unroll
  for (int i = 0; i < PD; i++) lda(i);
  ldb(0);
  u64 ofl = 0;
  auto step = [&](int i, bool pre) {
    SCHED0();
    if (i == 7 || i == 15 || i == 23) { STAMP(12 + i / 4); WAITL0(); BAR(); STAMP(13 + i / 4); SCHED0(); }
    if (i + 1 < 34) ldb(i + 1);
#pragma unroll
    for (int tt = 0; tt < 3; tt++)
#pragma unroll
      for (int ct = 0; ct < 2; ct++) acc[tt * 2 + ct] = mfma32(bf[i & 1][ct], af[i % PD][tt], acc[tt * 2 + ct]);
    SCHED0();
    if (pre) lda(i + PD);
    if (i == 23) {
#pragma unroll
      for (int tt = 0; tt < 3; tt++) { acc[tt * 2] *= r2[tt] * r2[tt]; acc[tt * 2 + 1] *= r2[tt] * r2[tt]; }
#pragma unroll
      for (int k = 0; k < 6; k++) asm volatile("" : "+v"(acc[k]));
      SCHED0();
    }
  };
  constexpr int OCHK = 20;
#pragma unroll
  for (int i = 0; i < OCHK; i++) {
    step(i, i + PD < 24);
    if (i + PD == 24) {
#pragma unroll
      for (int tt = 0; tt < 3; tt++) r2[tt] = __uint_as_float(bld32<SC1>(rws, (row2 + tt * 32 + r) * 16 + 8, WS_STATS));
      ofl = ld_flag(p.ws, fo);
    }
  }
  STAMP(5);
  const bool early = __all(ofl == tag);
  FENCE();
  if (early) {
#pragma unroll
    for (int j = 24; j < OCHK + PD; j++) lda(j);
  }
#pragma unroll
  for (int i = OCHK; i < 24; i++) step(i, false);
  if (!early) {
    wait_flag(p.ws, fo, tag, ld_flag(p.ws, fo));
#pragma unroll
    for (int j = 24; j < OCHK + PD; j++) lda(j);
  }
  STAMP(9);
#pragma unroll
  for (int j = OCHK + PD; j < 24 + PD; j++) lda(j);
#pragma unroll
  for (int i = 24; i < 34; i++) step(i, i + PD < 34);
  STAMP(10);
  const int item = 512 + threadIdx.x;
  const unsigned go = (unsigned)((row2 + (item >> 3)) * 1280 + cg2 * 128 + (item & 7) * 16);
  const u4 xv = *(const GLBP u4*)(p.x + go);
#pragma unroll
  for (int tt = 0; tt < 3; tt++)
#pragma unroll
    for (int ct = 0; ct < 2; ct++)
#pragma unroll
      for (int qd = 0; qd < 4; qd++)
        *(LDSP f4*)(sm + (tt * 4 + (r >> 3)) * 8192 + w * 2048 + (r & 7) * 256 + (((ct * 8 + qd * 2 + hh) ^ (r & 7)) << 4)) =
            f4{acc[tt * 2 + ct][qd * 4], acc[tt * 2 + ct][qd * 4 + 1], acc[tt * 2 + ct][qd * 4 + 2], acc[tt * 2 + ct][qd * 4 + 3]};
  WAITL0(); BAR();
  p2_out(p, sm, item, xv, go);
}

template <int type>
DI void loader(const Params& p, LDSP char* sm, rsrc_t rws, int lw, int lane, int rt, int cg, int row0, int row2, int cg2) {
  const int b = blockIdx.x, t = lw * 64 + lane;
  constexpr bool gu = type == 0;
  unsigned vo[13];
  {
    const int x0 = gu ? lw * 5 : lw * 8;
    const unsigned xr = (unsigned)((row0 + x0 * 8 + (lane >> 3)) * 1280), xs = (unsigned)(((lane & 7) ^ (lane >> 4) ^ ((x0 & 1) << 2)) << 4);
#pragma unroll
    for (int i = 0; i < 8; i++) vo[i] = xr + (xs ^ ((i & 1) << 6)) + i * 10240;
    if (gu) {
      const unsigned g0 = (unsigned)(((lw & 1) * 32 + (lane >> 4)) * 3072 + (((lane & 15) ^ ((lane >> 4) << 2) ^ (lw & 2)) << 4));
#pragma unroll
      for (int i = 5; i < 13; i++) vo[i] = g0 + (i - 5) * 12288;
    } else {
#pragma unroll
      for (int i = 0; i < 5; i++) {
        const int e = i * 64 + lane, rq = (e * 205) >> 12, pos = e - rq * 20;
        const int adj = type == 1 ? 0 : (pos < 10 ? 0 : (pos < 12 ? 1248 : 1088));
        vo[8 + i] = (unsigned)((lw * 16 + rq) * 3840 + pos * 16 + adj);
      }
    }
  }
  const unsigned wofs = gu ? cg * 256 : (type == 1 ? cg * 320 : 1280 + cg * 160);
  const char* wbase = (gu ? ((lw >> 1) ? p.wu : p.wg) : p.wqkv) + wofs;
  const unsigned wstep = gu ? 64 * 3072 : 64 * 3840, wlen = wstep * 10 - wofs, mstep = gu ? wstep : 128;
  const rsrc_t rx = mkrs(p.x, S * D * 2), rw = mkrs(wbase, wlen), rm = mkrs(gu ? wbase : p.x, gu ? wlen : S * D * 2);
  const int dx = gu ? lw * 5120 : lw * 8192, dm = gu ? 20480 + lw * 8192 : dx + 5120, dw = gu ? dm + 3072 : 32768 + lw * 5120;
  CS ck;
  const int task = type == 1 ? (rt * 4 + cg) * 4 + lw : min(156 + lw * 64 + cg * 8 + rt, 234);
  const bool act = !gu && task < 234;
  auto issue = [&](int s, int a0 = 0, int a1 = 0) {
    LDSP char* d = sm + (s % 3) * SLOT;
#pragma unroll
    for (int i = 0; i < 13; i++) {
      if (i < 5) blds16(rx, vo[i], s * 128, d + dx + i * 1024);
      else if (i < 8) blds16(rm, vo[i], s * mstep, d + dm + (i - 5) * 1024);
      else blds16(rw, vo[i], s * wstep, d + dw + (i - 8) * 1024);
      if (i >= 1 && a0 + i - 1 < a1 && (type == 1 || act)) {
        FENCE(); cs_add(ck, a0 + i - 1);
        asm volatile("" : "+v"(ck.c[0]), "+v"(ck.c[1]), "+v"(ck.c[2]), "+v"(ck.c[3]), "+v"(ck.c[4]), "+v"(ck.c[5]), "+v"(ck.c[6]), "+v"(ck.c[7]));
        FENCE();
      }
    }
    FENCE();
  };
  auto csl = [&](int i0, int i1) {
    if (type == 1 || act) {
#pragma unroll
      for (int i = i0; i < i1; i++) cs_load(ck, i);
      FENCE();
    }
  };
  issue(0); issue(1);
  if (!gu) cs_init(p, ck, task, lane);
  constexpr bool q1 = type == 1;
  STAMP(12);
  WAITV(13); BAR(); STAMP(13);
  if (type == 2) {
    bst128(u4{EMPTY, EMPTY, EMPTY, EMPTY}, rws, t * 16, WS_KVP + (rt * 8 + cg) * 7168);
    bst128(u4{EMPTY, EMPTY, EMPTY, EMPTY}, rws, min(t + 256, 419) * 16, WS_KVP + (rt * 8 + cg) * 7168);
    FENCE();
  }
  issue(2);
  const rsrc_t rd_ = mkrs(p.wd, type == 1 ? 0 : F * D * 2), ro_ = mkrs(p.wo, type == 1 ? 0 : D * D * 2);
  const unsigned bo = (unsigned)((lane >> 3) * 1280 + cg2 * 128 + (((lane & 7) ^ (((lane >> 4) & 1) << 2)) << 4));
  auto bdma = [&](int i) {
#pragma unroll
    for (int h = 0; h < 2; h++) blds16(rd_, bo, (lw * 384 + i * 16 + h * 8) * 1280, sm + (17 - i) * 8192 + lw * 2048 + h * 1024);
  };
  u4 br[46];
  auto breg = [&](int i0, int i1) {
#pragma unroll
    for (int i = i0; i < i1; i++) {
      const int st = 11 + (i >> 1);
      br[i] = st < 24 ? bld128<0>(rd_, bo, (lw * 384 + st * 16 + (i & 1) * 8) * 1280) : bld128<0>(ro_, bo, (lw * 160 + (st - 24) * 16 + (i & 1) * 8) * 1280);
    }
    FENCE();
  };
  auto bput = [&](int i0, int i1) {
#pragma unroll
    for (int i = i0; i < i1; i++) {
      const int st = 11 + (i >> 1), sl = st < 18 ? st : st - 18;
      *(LDSP u4*)(sm + (17 - sl) * 8192 + lw * 2048 + (i & 1) * 1024 + lane * 16) = br[i];
    }
  };
  auto bgu = [&](int i0, int i1) { if (gu) breg(i0, i1); };
  auto bkv = [&](int i0, int i1) { if (type == 2) breg(i0, i1); };
#define WAITG(a, b, c) do { if (gu) WAITV(a); else if (type == 1) WAITV(b); else WAITV(c); } while (0)
#define WAITA(a, b, c, d) do { if (type == 2 && act) WAITV(d); else WAITG(a, b, c); } while (0)
  csl(0, q1 ? 11 : 8); WAITA(13, 24, 15, 23); BAR(); STAMP(14);
  issue(3); csl(q1 ? 11 : 8, q1 ? 22 : 16); WAITA(13, 35, 13, 29); BAR(); STAMP(19);
  const u64 tag = lds_tag(sm);
  if (t < (type == 2 ? 2 : 1)) st_flag(p.ws, t ? WS_FKV + (cg * 8 + rt) * 8 : WS_F0 + b * 8, tag);
  FENCE();
  issue(4, 0, q1 ? 11 : 0); bgu(0, 4); csl(22, q1 ? 32 : 0); WAITA(17, 34, 13, 21); BAR(); STAMP(15);
  issue(5, q1 ? 11 : 0, q1 ? 22 : 8); bgu(4, 8); WAITG(21, 23, 13); BAR(); STAMP(16);
  issue(6, q1 ? 22 : 8, q1 ? 32 : 16); bgu(8, 12);
  if (type == 1 || act) cs_store(ck, rws, lane, (unsigned)tag);
  WAITA(21, 14, 13, 14); BAR(); STAMP(6);
  issue(7); bgu(12, 16); bkv(0, 4);
  unsigned sto = min(row0 + t, S - 1) * 16, cso;
  {
    const int tc = gu ? t : min(t, 159);
    cso = 8 * (gu ? 1920 + (tc >> 7) * 1536 + cg * 128 + (tc & 127)
                  : (type == 1 ? cg * 160 + tc : cg * 80 + tc + (tc < 80 ? 640 : (tc < 96 ? 1264 : 1184))));
  }
  asm volatile("" : "+v"(sto), "+v"(cso));
  WAITA(21, 14, 17, 18); BAR(); STAMP(7);
  issue(8); bgu(16, 20); bkv(4, 8); WAITG(21, 13, 21); BAR(); STAMP(17);
  issue(9); bgu(20, 24); bkv(8, 12); WAITG(21, 13, 21); BAR(); STAMP(18);
  u4 stv; u2 csv[3];
  auto aux = [&]() {
    stv = bld128<SC1>(rws, sto, WS_STATS);
#pragma unroll
    for (int i = 0; i < 3; i++) csv[i] = bld64<SC1>(rws, cso, WS_CS + i * NCOL * 8);
    FENCE();
  };
  aux();
#pragma unroll
  for (int i = 5; i < 11; i++) bdma(i);
  FENCE();
  if (type == 1) *(LDSP u2*)(sm + IMG + t * 8) = u2{0, 0};
  WAITG(20, 16, 20); BAR();
  if (type != 1) {
#pragma unroll
    for (int i = 0; i < 5; i++) bdma(i);
    FENCE();
  }
  auto fresh = [&]() {
    const unsigned tg = (unsigned)tag;
    return __all(stv[1] == tg && stv[3] == tg && csv[0][1] == tg && csv[1][1] == tg && csv[2][1] == tg);
  };
  if (!fresh()) {
#pragma unroll 1
    for (int it = 0; it < (1 << 18); it++) { aux(); if (fresh()) break; }
  }
  *(LDSP unsigned*)(sm + AUX + t * 4) = stv[0];
  *(LDSP unsigned*)(sm + AUX + 1024 + t * 4) = stv[2];
  *(LDSP float*)(sm + AUX + 2048 + t * 4) = (__uint_as_float(csv[0][0]) + __uint_as_float(csv[1][0])) + __uint_as_float(csv[2][0]);
  if (type == 1) WAITV(0);
  FENCE();
  bgu(24, 28);
  WAITL0(); BAR();
  STAMP(3);
  if (type == 1) {
    const unsigned fk = WS_FKV + (cg * 16 + (lane & 15)) * 8;
    wait_flag(p.ws, fk, tag, ld_flag(p.ws, fk));
    u4 pv[4][8];
    unsigned off[4], da[4];
#pragma unroll
    for (int m = 0; m < 4; m++) {
      const int g = min(t + m * 256, 999), dd = g / 25, e5 = g - dd * 25, hg = dd >= 20, dq = dd - hg * 20;
      const int eg = (dq / 5) * 20 + (e5 < 20 ? e5 : (e5 - 20) * 4);
      off[m] = WS_KVP + (cg * 2 + hg) * 7168 + (dq * 21 + min(e5, 20)) * 16;
      da[m] = IMG + hg * 25600 + (dq >> 2) * 1024 + (dq & 1) * 512 + ((dq >> 1) & 1) * 8 + (eg >> 4) * 5120 + (eg & 15) * 16 + (e5 < 20 ? 0 : 256);
      asm volatile("" : "+v"(off[m]), "+v"(da[m]));
#pragma unroll
      for (int i = 0; i < 8; i++) asm volatile("" : "=v"(pv[m][i]));
    }
    const u4 rk = {(unsigned)(size_t)p.ws, (unsigned)((size_t)p.ws >> 32) & 0xffffu, WS_END, 0x00020000u};
    auto fetch = [&](unsigned set) {
#pragma unroll
      for (int i = 0; i < 8; i++)
        if (set >> i & 1)
          asm volatile("buffer_load_dwordx4 %0, %4, %8, %9 offen sc1\n\tbuffer_load_dwordx4 %1, %5, %8, %9 offen sc1\n\t"
                       "buffer_load_dwordx4 %2, %6, %8, %9 offen sc1\n\tbuffer_load_dwordx4 %3, %7, %8, %9 offen sc1"
                       : "+v"(pv[0][i]), "+v"(pv[1][i]), "+v"(pv[2][i]), "+v"(pv[3][i])
                       : "v"(off[0]), "v"(off[1]), "v"(off[2]), "v"(off[3]), "s"(rk), "s"(i * 8 * 7168) : "memory");
    };
    auto landed = [&]() {
      WAITV(0);
#pragma unroll
      for (int i = 0; i < 8; i++) asm volatile("" : "+v"(pv[0][i]), "+v"(pv[1][i]), "+v"(pv[2][i]), "+v"(pv[3][i]));
    };
    {
      const unsigned ho = WS_KVP + ((lane & 7) * 8 + cg * 2 + ((lane >> 3) & 1)) * 7168 + 419 * 16;
      unsigned got = 0;
#pragma unroll 1
      for (int it = 0; it < (1 << 18) && got != 255; it++) {
        const unsigned mk = (unsigned)__ballot(bld32<SC1>(rws, ho, 0) != EMPTY), now = mk & (mk >> 8) & 255 & ~got;
        fetch(now);
        got |= now;
      }
    }
    STAMP(4);
    f4 sum[4];
#pragma unroll 1
    for (int it = 0; it < (1 << 18); it++) {
      landed();
      bool nan = false;
#pragma unroll
      for (int m = 0; m < 4; m++) {
        sum[m] = __builtin_bit_cast(f4, pv[m][0]);
#pragma unroll
        for (int i = 1; i < 8; i++) sum[m] += __builtin_bit_cast(f4, pv[m][i]);
        nan = nan || __builtin_isunordered(sum[m][0], sum[m][1]) || __builtin_isunordered(sum[m][2], sum[m][3]);
      }
      if (!__any(nan)) break;
      unsigned mx = 0;
#pragma unroll
      for (int i = 0; i < 8; i++)
#pragma unroll
        for (int m = 0; m < 4; m++) mx = max(max(mx, max(pv[m][i][0], pv[m][i][1])), max(pv[m][i][2], pv[m][i][3]));
      if (!__any(mx == EMPTY)) break;
      fetch(255);
    }
    STAMP(5);
#pragma unroll
    for (int m = 0; m < 4; m++) *(LDSP u2*)(sm + da[m]) = u2{pk2(sum[m][0], sum[m][1]), pk2(sum[m][2], sum[m][3])};
    WAITL0(); BAR();
    BAR();
    return;
  }
  __builtin_amdgcn_s_setprio(0);
  if (gu) {
    BAR();
    breg(28, 38);
    WAITV(14);
    STAMP(4);
  } else {
    breg(12, 30);
    STAMP(4);
    BAR(); kv_publish(sm, rws, rt, cg); WAITL0();
    WAITV(18);
  }
  BAR();
  bput(0, 14);
  FENCE();
  bkv(30, 46); bgu(38, 46);
  WAITL0(); BAR();
  bput(14, 30);
  WAITL0(); BAR();
  bput(30, 46);
  WAITL0(); BAR();
  unsigned go[2]; u4 xv[2];
#pragma unroll
  for (int i = 0; i < 2; i++) {
    const int item = i * 256 + t;
    go[i] = (unsigned)((row2 + (item >> 3)) * 1280 + cg2 * 128 + (item & 7) * 16);
    xv[i] = *(const GLBP u4*)(p.x + go[i]);
  }
  BAR();
#pragma unroll
  for (int i = 0; i < 2; i++) p2_out(p, sm, i * 256 + t, xv[i], go[i]);
}

__global__ __launch_bounds__(512, 2) void fused_kernel(Params p) {
  extern __shared__ __attribute__((aligned(16))) char smem[];
  LDSP char* sm = (LDSP char*)smem;
  const int lane = threadIdx.x & 63, w = __builtin_amdgcn_readfirstlane(threadIdx.x >> 6), b = blockIdx.x;
  STAMPR(0); STAMP(1);
  u64 pc0 = __builtin_amdgcn_s_getpc();
  if (w < 4) {
    const u4 rc = {(unsigned)pc0, (unsigned)(pc0 >> 32) & 0xffffu, CODE_BYTES, 0x00020000u};
    asm volatile("s_mov_b32 m0, %0\n\ts_nop 0\n\tbuffer_load_dword %1, %2, 0 offen lds"
                 :: "s"((unsigned)(size_t)(sm + AUX + 3328)), "v"(min(threadIdx.x * 128 + ((blockIdx.x >> 3) & 1) * 32768, CODE_BYTES - 128)), "s"(rc) : "memory", "m0");
  }
  FENCE();
  const rsrc_t rws = mkrs(p.ws, WS_END);
  int type, rt, cg, n2;
  {
    const int c = b & 7, j = b >> 3, a = c & 1, q = c >> 1;
    if (j < 12) {
      rt = 4 * a + j / 3; const int m = j % 3;
      if (m == 0) { type = 1; cg = q; } else { type = 2; cg = 2 * q + m - 1; }
      n2 = (j / 3) * 2 + m - 1;
    } else {
      const int n = j - 12 + (a ? 19 : 0);
      type = (!a && j == 31) ? 3 : 0; rt = n / 3; cg = 3 * q + n % 3;
      n2 = j - 4;
    }
    n2 = max(n2 + q * 55 + a * 27, 0);
  }
  const int row0 = type == 0 ? min(rt * 160, S - 160) : rt * 256;
  const int rt2 = n2 / 10, cg2 = n2 - rt2 * 10, row2 = min(rt2 * 96, S - 96);
  if (type == 3) {
    u2 xs[5];
    const u64 tag = row_load(p, xs, w & 3, lane) + 1;
    const int task = 128 + ((b >> 1) & 3) + w * 4;
    CS ck;
    cs_init(p, ck, task < 156 ? task : 234, lane);
#pragma unroll
    for (int i = 0; i < 32; i++) cs_load(ck, i);
    FENCE();
    if (w < 4) row_stats(xs, rws, w, lane, (unsigned)tag);
#pragma unroll
    for (int i = 0; i < 32; i++) cs_add(ck, i);
    if (task < 156) cs_store(ck, rws, lane, (unsigned)tag);
    STAMP(3);
    BAR();
    if (w != 0) return;
    if (lane == 0) st_flag(p.ws, WS_F0 + b * 8, tag);
    if (b != 248) return;
#pragma unroll 1
    for (int i = 0; i < 4; i++) wait_flag(p.ws, WS_F0 + (i * 64 + lane) * 8, tag, tag - 1);
    if (lane == 0) st_flag(p.ws, WS_GEN, tag);
    return;
  }
  if (w >= 4) {
    __builtin_amdgcn_s_setprio(3);
    if (type == 2) loader<2>(p, sm, rws, w - 4, lane, rt, cg, row0, row2, cg2);
    else if (type == 0) loader<0>(p, sm, rws, w - 4, lane, rt, cg, row0, row2, cg2);
    else loader<1>(p, sm, rws, w - 4, lane, rt, cg, row0, row2, cg2);
  } else {
    if (type == 0) k1_mfma<0>(p, sm, rws, w, lane, rt, cg, row0);
    else if (type == 1) k1_mfma<1>(p, sm, rws, w, lane, rt, cg, row0);
    else k1_mfma<2>(p, sm, rws, w, lane, rt, cg, row0);
    STAMP(7);
    if (type != 1) p2_mfma(p, sm, rws, w, lane, row2, cg2);
  }
  STAMP(11); STAMPR(2);
}

extern "C" void kernel_launch(void* const* d_in, const int* in_sizes, int n_in, void* d_out, int out_size,
                              void* d_ws, size_t ws_size, hipStream_t stream) {
  Params p;
  p.x = (const char*)d_in[0]; p.wqkv = (const char*)d_in[1]; p.wo = (const char*)d_in[2];
  p.wg = (const char*)d_in[4]; p.wu = (const char*)d_in[5]; p.wd = (const char*)d_in[6];
  p.out = (char*)d_out; p.ws = (char*)d_ws;
  (void)hipFuncSetAttribute((const void*)fused_kernel, hipFuncAttributeMaxDynamicSharedMemorySize, LDS_BYTES);
  fused_kernel<<<256, 512, LDS_BYTES, stream>>>(p);
}
```

```cpp
#include <hip/hip_runtime.h>
#include <hip/hip_bf16.h>
#include <stdint.h>

typedef __attribute__((ext_vector_type(8))) __bf16 bf8;
typedef __attribute__((ext_vector_type(2))) __bf16 bf2;
typedef __attribute__((ext_vector_type(4))) short s4;
typedef __attribute__((ext_vector_type(2))) float f2;
typedef __attribute__((ext_vector_type(4))) float f4;
typedef __attribute__((ext_vector_type(16))) float f16v;
typedef __attribute__((ext_vector_type(2))) unsigned u2;
typedef __attribute__((ext_vector_type(4))) unsigned u4;
typedef unsigned long long u64;
typedef __amdgpu_buffer_rsrc_t rsrc_t;
#define LDSP __attribute__((address_space(3)))
#define GLBP __attribute__((address_space(1)))
#define DI __device__ __forceinline__

constexpr int S = 2048, D = 640, F = 1536;
constexpr int NCOL = 4992;
constexpr unsigned WS_GEN = 0;
constexpr unsigned WS_F0 = 4096;
constexpr unsigned WS_FH = 8192;
constexpr unsigned WS_FKV = 12288;
constexpr unsigned WS_FO = 16384;
constexpr unsigned WS_STATS = 32768;
constexpr unsigned WS_CS = 65536;
constexpr unsigned WS_KVP = 327680;
constexpr unsigned WS_H = 1 << 20;
constexpr unsigned WS_O = WS_H + S * F * 2;
constexpr unsigned WS_END = WS_O + S * D * 2;

constexpr int SLOT = 52 * 1024;
constexpr int LDS_BYTES = 160 * 1024;
constexpr int AUX = 3 * SLOT;
constexpr int IMG = 54 * 1024;
constexpr int SC1 = 16;
constexpr unsigned CODE_BYTES = 62 * 1024;

DI rsrc_t mkrs(const void* b, unsigned n) { return __builtin_amdgcn_make_buffer_rsrc((void*)b, (short)0, (int)n, 0x00020000); }
DI void blds16(rsrc_t rs, unsigned voff, unsigned soff, LDSP char* l) {
  __builtin_amdgcn_raw_ptr_buffer_load_lds(rs, (LDSP void*)l, 16, voff, soff, 0, 0);
}
template <int AUXB> DI u4 bld128(rsrc_t rs, unsigned voff, unsigned soff) { return __builtin_amdgcn_raw_buffer_load_b128(rs, voff, soff, AUXB); }
template <int AUXB> DI u2 bld64(rsrc_t rs, unsigned voff, unsigned soff) { return __builtin_amdgcn_raw_buffer_load_b64(rs, voff, soff, AUXB); }
template <int AUXB> DI unsigned bld32(rsrc_t rs, unsigned voff, unsigned soff) { return __builtin_amdgcn_raw_buffer_load_b32(rs, voff, soff, AUXB); }
DI void bst128(u4 v, rsrc_t rs, unsigned voff, unsigned soff) { __builtin_amdgcn_raw_buffer_store_b128(v, rs, voff, soff, SC1); }
DI void bst64(u2 v, rsrc_t rs, unsigned voff, unsigned soff) { __builtin_amdgcn_raw_buffer_store_b64(v, rs, voff, soff, SC1); }
DI u4 lds_r128(LDSP char* p) { return *(LDSP u4*)p; }
DI u2 lds_tr(LDSP char* p) { return __builtin_bit_cast(u2, __builtin_amdgcn_ds_read_tr16_b64_v4i16((LDSP s4*)p)); }
DI f16v mfma32(u4 a, u4 b, f16v c) { return __builtin_amdgcn_mfma_f32_32x32x16_bf16(__builtin_bit_cast(bf8, a), __builtin_bit_cast(bf8, b), c, 0, 0, 0); }
DI unsigned pk2(float lo, float hi) { f2 v = {lo, hi}; return __builtin_bit_cast(unsigned, __builtin_convertvector(v, bf2)); }
DI float blo(unsigned v) { return __uint_as_float(v << 16); }
DI float bhi(unsigned v) { return __uint_as_float(v & 0xffff0000u); }
DI float exp2c(float u) { return __builtin_amdgcn_fmed3f(__builtin_amdgcn_exp2f(u), 0.f, 1.f); }
constexpr float LOG2E = 1.44269504088896f, LN2 = 0.693147180559945f;
constexpr float PHC = 1e-6f * LOG2E;
DI unsigned phi2(f2 u) {
  const f2 e = {exp2c(u[0]), exp2c(u[1])};
  const f2 v = f2{fmaxf(u[0], PHC), fmaxf(u[1], PHC)} * LN2 + e;
  return pk2(v[0], v[1]);
}
DI u64 ld_flag(const char* ws, unsigned off) { return __hip_atomic_load((const GLBP u64*)(ws + off), __ATOMIC_RELAXED, __HIP_MEMORY_SCOPE_AGENT); }
DI void st_flag(char* ws, unsigned off, u64 v) { __hip_atomic_store((GLBP u64*)(ws + off), v, __ATOMIC_RELAXED, __HIP_MEMORY_SCOPE_AGENT); }
#define BAR() __builtin_amdgcn_s_barrier()
#define WAITV(n) asm volatile("s_waitcnt vmcnt(" #n ")" ::: "memory")
#define WAITL0() asm volatile("s_waitcnt lgkmcnt(0)" ::: "memory")
#define SCHED0() __builtin_amdgcn_sched_barrier(0)
#define FENCE() do { asm volatile("" ::: "memory"); __builtin_amdgcn_sched_barrier(0); } while (0)

#define STAMP(i)
#define STAMPR(i)

struct Params {
  const char* x; const char* wqkv; const char* wo; const char* wg; const char* wu; const char* wd;
  char* out; char* ws;
};

DI void wait_flag(const char* ws, unsigned off, u64 tag, u64 first) {
  u64 v = first;
#pragma unroll 1
  for (int it = 0; it < (1 << 18); it++) {
    if (__all(v == tag)) break;
    __builtin_amdgcn_s_sleep(1);
    v = ld_flag(ws, off);
  }
  FENCE();
}
DI u64 lds_tag(LDSP char* sm) {
  const u2 t = *(LDSP u2*)(sm + AUX + 3072);
  return (u64)(unsigned)__builtin_amdgcn_readfirstlane(t[0]) | ((u64)(unsigned)__builtin_amdgcn_readfirstlane(t[1]) << 32);
}

template <int CTL> DI float dppadd(float v) { return v + __int_as_float(__builtin_amdgcn_update_dpp(0, __float_as_int(v), CTL, 0xf, 0xf, true)); }
DI float swap16add(float v) { const u2 t = __builtin_amdgcn_permlane16_swap(__float_as_uint(v), __float_as_uint(v), false, false); return __uint_as_float(t[0]) + __uint_as_float(t[1]); }
DI float sum32(float v) { return swap16add(dppadd<0x121>(dppadd<0x122>(dppadd<0x124>(dppadd<0x128>(v))))); }
constexpr unsigned EMPTY = 0xffffffffu;

DI u64 row_load(const Params& p, u2 (&xs)[5], int w, int lane) {
  const u64 gen = ld_flag(p.ws, WS_GEN);
  const rsrc_t rx = mkrs(p.x, S * D * 2);
  const unsigned xo = (unsigned)((blockIdx.x * 8 + w * 2 + (lane >> 5)) * 1280 + (lane & 31) * 8);
#pragma unroll
  for (int i = 0; i < 5; i++) xs[i] = bld64<0>(rx, xo + i * 256, 0);
  FENCE();
  return gen;
}
DI void row_stats(const u2 (&xs)[5], rsrc_t rws, int w, int lane, unsigned tag) {
  const int b = blockIdx.x;
  float s = 0;
#pragma unroll
  for (int i = 0; i < 5; i++) s += (blo(xs[i][0]) + bhi(xs[i][0])) + (blo(xs[i][1]) + bhi(xs[i][1]));
  s = sum32(s);
  const float mu = s * (1.f / 640.f);
  float q = 0;
#pragma unroll
  for (int i = 0; i < 5; i++) {
    const float a = blo(xs[i][0]) - mu, c = bhi(xs[i][0]) - mu, e = blo(xs[i][1]) - mu, g = bhi(xs[i][1]) - mu;
    q += a * a + c * c + e * e + g * g;
  }
  q = sum32(q);
  const float rs = rsqrtf(q * (1.f / 640.f) + 1e-5f);
  if ((lane & 31) == 0) bst128(u4{__float_as_uint(mu), tag, __float_as_uint(rs), tag}, rws, (b * 8 + w * 2 + (lane >> 5)) * 16, WS_STATS);
}
struct CS { rsrc_t rs; unsigned vo, step, dst; u4 d[32]; float c[8]; };
DI void cs_init(const Params& p, CS& k, int task, int lane) {
  const int kp = task / 78, cu = task - kp * 78;
  const char* base = cu < 30 ? p.wqkv + cu * 128 : (cu < 54 ? p.wg + (cu - 30) * 128 : p.wu + (cu - 54) * 128);
  const unsigned stride = cu < 30 ? 3840 : 3072;
  k.rs = mkrs(base, task < 234 ? 640 * stride - stride + 128 : 0);
  k.vo = (unsigned)(kp * 256 + (lane >> 3)) * stride + (lane & 7) * 16;
  k.step = 8 * stride;
  k.dst = (kp * NCOL + cu * 64 + (lane & 7) * 8 + (lane >> 5) * 4 + ((lane >> 3) & 1) * 2) * 8;
#pragma unroll
  for (int j = 0; j < 8; j++) k.c[j] = 0;
}
DI void cs_load(CS& k, int i) { k.d[i] = bld128<0>(k.rs, k.vo, i * k.step); }
DI void cs_add(CS& k, int i) {
#pragma unroll
  for (int j = 0; j < 4; j++) { k.c[2 * j] += blo(k.d[i][j]); k.c[2 * j + 1] += bhi(k.d[i][j]); }
}
DI void cs_store(CS& k, rsrc_t rws, int lane, unsigned tag) {
  float e[4];
#pragma unroll
  for (int j = 0; j < 4; j++) {
    const u2 t = __builtin_amdgcn_permlane32_swap(__float_as_uint(k.c[j]), __float_as_uint(k.c[4 + j]), false, false);
    e[j] = dppadd<0x128>(swap16add(__uint_as_float(t[0]) + __uint_as_float(t[1])));
  }
  if ((lane & 16) == 0) bst128(u4{__float_as_uint(lane & 8 ? e[2] : e[0]), tag, __float_as_uint(lane & 8 ? e[3] : e[1]), tag}, rws, k.dst, WS_CS);
}

template <int TYPE>
DI void k1_main(LDSP char* sm, int w, int lane, f16v (&acc)[10]) {
  constexpr int NXF = TYPE == 0 ? 5 : 2, NWF = 7 - NXF;
  constexpr int XB = TYPE == 0 ? 160 * 128 : 256 * 128;
  const int r = lane & 31, hh = lane >> 5, blk = (lane >> 4) & 1, q = (lane & 15) >> 2, pp = lane & 3;
  unsigned xa[4];
#pragma unroll
  for (int k = 0; k < 4; k++) xa[k] = (unsigned)(((TYPE == 0 ? 0 : w * 64) + r) * 128 + (((2 * k + hh) ^ ((r >> 1) & 7)) << 4));
  unsigned wa[2];
  if (TYPE == 0) {
#pragma unroll
    for (int ft = 0; ft < 2; ft++)
      wa[ft] = (unsigned)(XB + blk * 16384 + (8 * hh + q) * 256 + (((w * 4 + ft * 2 + (pp >> 1)) ^ (q << 2) ^ (blk << 1)) << 4) + (pp & 1) * 8);
  } else {
    wa[0] = (unsigned)(XB + (8 * hh + q) * 320 + blk * 32 + pp * 8); wa[1] = 0;
  }
  u4 xf[2][NXF], wf[2][NWF];
  auto rd = [&](int buf, LDSP char* base, int k) {
#pragma unroll
    for (int i = 0; i < NWF; i++) {
      u2 lo, hi;
      if (TYPE == 0) { lo = lds_tr(base + wa[i] + k * 4096); hi = lds_tr(base + wa[i] + k * 4096 + 1024); }
      else { lo = lds_tr(base + wa[0] + k * 5120 + i * 64); hi = lds_tr(base + wa[0] + k * 5120 + i * 64 + 1280); }
      wf[buf][i] = u4{lo[0], lo[1], hi[0], hi[1]};
    }
#pragma unroll
    for (int i = 0; i < NXF; i++) xf[buf][i] = lds_r128(base + xa[k] + i * 4096);
  };
  auto mm = [&](int buf) {
#pragma unroll
    for (int i = 0; i < NXF; i++)
#pragma unroll
      for (int j = 0; j < NWF; j++) {
        if (TYPE == 2) acc[i * NWF + j] = mfma32(xf[buf][i], wf[buf][j], acc[i * NWF + j]);
        else acc[i * NWF + j] = mfma32(wf[buf][j], xf[buf][i], acc[i * NWF + j]);
      }
  };
  auto inter = [&]() {
    constexpr int NR = NXF + 2 * NWF;
#pragma unroll
    for (int i = 0; i < 6; i++) {
      __builtin_amdgcn_sched_group_barrier(0x8, 1, 0);
      __builtin_amdgcn_sched_group_barrier(0x100, (NR + 5) / 6, 0);
    }
    __builtin_amdgcn_sched_group_barrier(0x8, 4, 0);
  };
  BAR();
  rd(0, sm, 0);
  int slot = 0;
#pragma unroll 1
  for (int s = 0; s < 9; s++) {
    LDSP char* base = sm + slot * SLOT;
    slot = slot == 2 ? 0 : slot + 1;
    SCHED0();
    rd(1, base, 1); mm(0); inter(); SCHED0();
    rd(0, base, 2); mm(1); inter(); SCHED0();
    rd(1, base, 3); mm(0); inter(); SCHED0();
    WAITL0(); BAR(); SCHED0();
    rd(0, sm + slot * SLOT, 0); mm(1); inter(); SCHED0();
  }
  SCHED0();
  rd(1, sm, 1); mm(0); inter(); SCHED0();
  rd(0, sm, 2); mm(1); inter(); SCHED0();
  rd(1, sm, 3); mm(0); inter(); SCHED0();
  mm(1); SCHED0();
}

DI void kv_publish(LDSP char* sm, rsrc_t rws, int rt, int cg) {
  const int t = threadIdx.x;
  if (t < 420) {
    LDSP char* src = sm + t * 16;
    const f4 s = (*(LDSP f4*)src + *(LDSP f4*)(src + 8192)) + (*(LDSP f4*)(src + 16384) + *(LDSP f4*)(src + 24576));
    u4 v = __builtin_bit_cast(u4, s);
#pragma unroll
    for (int j = 0; j < 4; j++) v[j] = min(v[j], EMPTY - 1);
    bst128(v, rws, t * 16, WS_KVP + (rt * 8 + cg) * 7168);
  }
}

template <int TYPE>
DI void k1_mfma(const Params& p, LDSP char* sm, rsrc_t rws, int w, int lane, int rt, int cg, int row0) {
  u2 xs[5];
  const u64 gen = row_load(p, xs, w, lane);
  const int hh = lane >> 5, r = lane & 31;
  f16v acc[10];
#pragma unroll
  for (int i = 0; i < 10; i++)
#pragma unroll
    for (int j = 0; j < 16; j++) acc[i][j] = 0.f;
#pragma unroll
  for (int i = 0; i < 10; i++) asm volatile("" : "+v"(acc[i]));
  FENCE();
  {
    const u64 tag = gen + 1;
    row_stats(xs, rws, w, lane, (unsigned)tag);
    if (threadIdx.x == 0) *(LDSP u2*)(sm + AUX + 3072) = u2{(unsigned)tag, (unsigned)(tag >> 32)};
    WAITL0();
    const u64 wa = (u64)p.ws, oa = (u64)p.out;
    const u4 r1 = {(unsigned)wa, (unsigned)(wa >> 32) & 0xffffu, WS_END, 0x00020000u}, r2 = {(unsigned)oa, (unsigned)(oa >> 32) & 0xffffu, S * D * 2, 0x00020000u};
    if (TYPE == 0 && (blockIdx.x >> 3) == 12)
    asm volatile("s_mov_b32 m0, %4\n\ts_nop 0\n\tbuffer_load_dword %0, %2, 0 offen sc1 lds\n\tbuffer_load_dword %1, %3, 0 offen sc1 lds"
                 :: "v"(threadIdx.x * 38912), "v"(threadIdx.x * 10240), "s"(r1), "s"(r2), "s"((unsigned)(size_t)(sm + AUX + 3328)) : "memory", "m0");
    STAMP(12);
  }
  k1_main<TYPE>(sm, w, lane, acc);
  STAMP(3);
  BAR(); FENCE();
  STAMP(19);
  if (TYPE == 0) {
    f4 cs[2][4];
#pragma unroll
    for (int ft = 0; ft < 2; ft++)
#pragma unroll
      for (int qd = 0; qd < 4; qd++)
        cs[ft][qd] = *(LDSP f4*)(sm + AUX + 2048 + ((qd >> 1) * 128 + w * 32 + ft * 16 + (qd & 1) * 8 + hh * 4) * 4);
    const unsigned dst = (unsigned)(((row0 >> 5) * 96 + cg * 8 + w * 2) * 1024 + lane * 16);
#pragma unroll
    for (int tt = 0; tt < 5; tt++) {
      const float mu = *(LDSP float*)(sm + AUX + (tt * 32 + r) * 4);
      if (tt == 0 && rt == 12) continue;
#pragma unroll
      for (int ft = 0; ft < 2; ft++) {
        const f16v a = acc[tt * 2 + ft];
        float h[8];
#pragma unroll
        for (int j = 0; j < 8; j++) h[j] = (a[j] - mu * cs[ft][j >> 2][j & 3]) * (a[8 + j] - mu * cs[ft][2 + (j >> 2)][j & 3]);
        bst128(u4{pk2(h[0], h[1]), pk2(h[2], h[3]), pk2(h[4], h[5]), pk2(h[6], h[7])}, rws, dst + ft * 1024, WS_H + tt * 98304);
      }
    }
    STAMP(4);
    FENCE(); BAR();
    WAITV(0); BAR();
    if (threadIdx.x == 0) st_flag(p.ws, WS_FH + (rt * 16 + cg) * 8, lds_tag(sm));
  } else if (TYPE == 1) {
    u4 pq[2][10];
#pragma unroll
    for (int tt = 0; tt < 2; tt++) {
      const float mu = *(LDSP float*)(sm + AUX + (w * 64 + tt * 32 + r) * 4);
      const float rl = *(LDSP float*)(sm + AUX + 1024 + (w * 64 + tt * 32 + r) * 4) * LOG2E;
#pragma unroll
      for (int ft = 0; ft < 5; ft++) {
        const f16v a = acc[tt * 5 + ft];
#pragma unroll
        for (int qd = 0; qd < 4; qd++) {
          const f4 cs = *(LDSP f4*)(sm + AUX + 2048 + (ft * 32 + qd * 8 + hh * 4) * 4);
#pragma unroll
          for (int j = 0; j < 2; j++) {
            const f2 u = (f2{a[qd * 4 + 2 * j], a[qd * 4 + 2 * j + 1]} - f2{cs[2 * j], cs[2 * j + 1]} * mu) * rl + PHC;
            pq[tt][ft * 2 + (qd >> 1)][(qd & 1) * 2 + j] = phi2(u);
          }
        }
        asm volatile("" : "+v"(pq[tt][ft * 2]), "+v"(pq[tt][ft * 2 + 1]));
      }
    }
    STAMP(4);
    SCHED0(); BAR(); SCHED0();
    STAMP(5);
    const unsigned dst = (unsigned)((((row0 >> 5) + w * 2) * 40 + cg * 10) * 1024 + lane * 16);
    f16v oa[2][2];
    u4 af[2][3];
    auto rdi = [&](int n) {
      const int hg = n / 5, T = n % 5, k0 = T < 2 ? 0 : T - 1, k1 = T == 0 ? 1 : (T == 4 ? 4 : T + 1);
#pragma unroll
      for (int k = k0; k <= k1; k++) af[n & 1][k - k0] = lds_r128(sm + IMG + (hg * 25 + T * 5 + k) * 1024 + lane * 16);
    };
    auto omm = [&](int n) {
      const int hg = n / 5, T = n % 5, k0 = T < 2 ? 0 : T - 1, k1 = T == 0 ? 1 : (T == 4 ? 4 : T + 1);
#pragma unroll
      for (int tt = 0; tt < 2; tt++)
#pragma unroll
        for (int j = 0; j < 16; j++) oa[n & 1][tt][j] = 0.f;
#pragma unroll
      for (int k = k0; k <= k1; k++)
#pragma unroll
        for (int tt = 0; tt < 2; tt++) oa[n & 1][tt] = mfma32(af[n & 1][k - k0], pq[tt][hg * 5 + k], oa[n & 1][tt]);
    };
    rdi(0); rdi(1); omm(0); SCHED0();
#pragma unroll
    for (int n = 0; n < 10; n++) {
      if (n + 1 < 10) omm(n + 1);
      if (n + 2 < 10) rdi(n + 2);
#pragma unroll
      for (int tt = 0; tt < 2; tt++) {
        const f16v a = oa[n & 1][tt];
        const float d0 = __builtin_amdgcn_rcpf(a[8] + 1e-6f), d1 = __builtin_amdgcn_rcpf(a[12] + 1e-6f);
        bst128(u4{pk2(a[0] * d0, a[1] * d0), pk2(a[2] * d0, a[3] * d0), pk2(a[4] * d1, a[5] * d1), pk2(a[6] * d1, a[7] * d1)},
               rws, dst + (n & 3) * 1024, WS_O + tt * 40960 + (n >> 2) * 4096);
      }
#pragma unroll
      for (int i = 0; i < 6; i++) { __builtin_amdgcn_sched_group_barrier(0x8, 1, 0); __builtin_amdgcn_sched_group_barrier(0x2, 5, 0); }
      SCHED0();
    }
    STAMP(6);
    WAITV(0); BAR();
    if (threadIdx.x == 0) st_flag(p.ws, WS_FO + (rt * 16 + cg) * 8, lds_tag(sm));
  } else {
    float cs[5];
#pragma unroll
    for (int ct = 0; ct < 5; ct++) cs[ct] = *(LDSP float*)(sm + AUX + 2048 + (ct * 32 + r) * 4);
    const bool isk = r < 16;
    const float kmul = isk ? 1.f : 0.f, kadd = isk ? 1e-6f : 0.f, klo = isk ? 1e-6f : -__builtin_inff();
    u4 fr[4][5];
#pragma unroll
    for (int g = 0; g < 4; g++) {
      f2 mu[4], rs[4], rl[4];
#pragma unroll
      for (int hq = 0; hq < 2; hq++) {
        LDSP char* sp = sm + AUX + (w * 64 + g * 16 + hq * 8 + hh * 4) * 4;
        const f4 a = *(LDSP f4*)sp, b = *(LDSP f4*)(sp + 1024);
        mu[hq * 2] = f2{a[0], a[1]}; mu[hq * 2 + 1] = f2{a[2], a[3]};
        rs[hq * 2] = f2{b[0], b[1]}; rs[hq * 2 + 1] = f2{b[2], b[3]};
      }
#pragma unroll
      for (int j = 0; j < 4; j++) rl[j] = rs[j] * LOG2E;
#pragma unroll
      for (int ct = 0; ct < 5; ct++) {
#pragma unroll
        for (int j = 0; j < 4; j++) {
          const f16v& a = acc[(g >> 1) * 5 + ct];
          const f2 y = f2{a[(g & 1) * 8 + 2 * j], a[(g & 1) * 8 + 2 * j + 1]} - mu[j] * cs[ct];
          if (ct < 2) fr[g][ct][j] = phi2(y * rl[j] + PHC);
          else if (ct == 2) {
            const f2 u = y * rl[j], t = y * rs[j] + kadd, e = {exp2c(u[0]), exp2c(u[1])};
            const f2 v = e * kmul + f2{fmaxf(t[0], klo), fmaxf(t[1], klo)};
            fr[g][ct][j] = pk2(v[0], v[1]);
          } else { const f2 v = y * rs[j]; fr[g][ct][j] = pk2(v[0], v[1]); }
        }
      }
      SCHED0();
    }
    f16v z[8];
#pragma unroll
    for (int i = 0; i < 8; i++)
#pragma unroll
      for (int j = 0; j < 16; j++) z[i][j] = 0.f;
#pragma unroll
    for (int g = 0; g < 4; g++) {
      u4 mb;
#pragma unroll
      for (int j = 0; j < 4; j++) mb[j] = isk ? 0x3f803f80u : fr[g][2][j];
      z[0] = mfma32(fr[g][0], fr[g][3], z[0]);
      z[1] = mfma32(fr[g][0], fr[g][4], z[1]);
      z[2] = mfma32(fr[g][0], mb, z[2]);
      z[3] = mfma32(fr[g][1], fr[g][3], z[3]);
      z[4] = mfma32(fr[g][1], fr[g][4], z[4]);
      z[5] = mfma32(fr[g][1], mb, z[5]);
      z[6] = mfma32(fr[g][2], fr[g][4], z[6]);
      z[7] = mfma32(fr[g][2], mb, z[7]);
    }
    unsigned pa[3], pm[3];
#pragma unroll
    for (int vt = 0; vt < 3; vt++) {
      const int e = vt == 2 ? 48 + r : vt * 32 + r, he = e / 20;
      int ee = e - he * 20; unsigned m = 31u << (5 * he);
      if (vt == 2 && isk) { ee = 20; m = r == 0 ? 0xfffffu : 0u; }
      pa[vt] = (unsigned)(w * 8192 + ee * 16 + hh * 336); pm[vt] = m >> hh;
    }
    SCHED0();
#pragma unroll
    for (int i = 0; i < 8; i++)
#pragma unroll
      for (int qd = 0; qd < 4; qd++) {
        const int kt = i / 3, vt = i < 6 ? i % 3 : i - 5, c = kt * 8 + qd * 2;
        const bool need = (i == 1) ? qd >= 2 : (i == 3) ? qd == 0 : (i >= 6) ? qd < 2 : true;
        if (need && (pm[vt] & (1u << c)))
          *(LDSP f4*)(sm + pa[vt] + c * 336) = f4{z[i][qd * 4], z[i][qd * 4 + 1], z[i][qd * 4 + 2], z[i][qd * 4 + 3]};
      }
    STAMP(4);
    WAITL0(); BAR();
    kv_publish(sm, rws, rt, cg);
    WAITL0(); BAR();
  }
}

DI void p2_out(const Params& p, LDSP char* sm, int item, u4 xv, unsigned go) {
  const int row = item >> 3, c = item & 7;
  LDSP char* src = sm + (row >> 3) * 8192 + (row & 7) * 256;
  const unsigned c0 = ((2 * c) ^ (row & 7)) << 4, c1 = c0 ^ 16;
  f4 a = *(LDSP f4*)(src + c0), b = *(LDSP f4*)(src + c1);
#pragma unroll
  for (int i = 1; i < 4; i++) { a += *(LDSP f4*)(src + i * 2048 + c0); b += *(LDSP f4*)(src + i * 2048 + c1); }
  bst128(u4{pk2(blo(xv[0]) + a[0], bhi(xv[0]) + a[1]), pk2(blo(xv[1]) + a[2], bhi(xv[1]) + a[3]),
            pk2(blo(xv[2]) + b[0], bhi(xv[2]) + b[1]), pk2(blo(xv[3]) + b[2], bhi(xv[3]) + b[3])}, mkrs(p.out, S * D * 2), go, 0);
}

constexpr int PD = 8;
DI void p2_mfma(const Params& p, LDSP char* sm, rsrc_t rws, int w, int lane, int row2, int cg2) {
  const int hh = lane >> 5, r = lane & 31, blk = (lane >> 4) & 1, q = (lane & 15) >> 2, pp = lane & 3;
  const u64 tag = lds_tag(sm);
  const unsigned fh = WS_FH + (((row2 + ((lane >> 2) & 1) * 95) / 160) * 16 + 3 * w + min(lane & 3, 2)) * 8;
  const unsigned fo = WS_FO + (((row2 + (lane & 1) * 95) >> 8) * 16 + w) * 8;
  float r2[3];
  wait_flag(p.ws, fh, tag, ld_flag(p.ws, fh));
  STAMP(8);
  const unsigned ah = (unsigned)(((row2 >> 5) * 96 + w * 24) * 1024 + lane * 16), ao = (unsigned)(((row2 >> 5) * 40 + w * 10) * 1024 + lane * 16);
  u4 af[PD][3], bf[2][2];
  auto lda = [&](int i) {
#pragma unroll
    for (int tt = 0; tt < 3; tt++) {
      if (i < 24) af[i % PD][tt] = bld128<SC1>(rws, ah + (i & 3) * 1024, WS_H + tt * 98304 + (i >> 2) * 4096);
      else af[i % PD][tt] = bld128<SC1>(rws, ao + (i & 3) * 1024, WS_O + tt * 40960 + ((i - 24) >> 2) * 4096);
    }
  };
  LDSP char* ba[2][3];
#pragma unroll
  for (int ct = 0; ct < 2; ct++)
#pragma unroll
    for (int sg = 0; sg < 3; sg++) {
      ba[ct][sg] = sm + sg * 49152 + w * 2048 + (4 * hh + q) * 128 + (((ct * 4 + blk * 2 + (pp >> 1)) ^ ((q >> 1) << 2)) << 4) + (pp & 1) * 8;
      asm volatile("" : "+v"(ba[ct][sg]));
    }
  auto ldb = [&](int i) {
    const int sl = 17 - (i < 18 ? i : i - 18);
#pragma unroll
    for (int ct = 0; ct < 2; ct++) {
      LDSP char* base = ba[ct][sl / 6] + (sl % 6) * 8192;
      const u2 lo = lds_tr(base), hi = lds_tr(base + 1024);
      bf[i & 1][ct] = u4{lo[0], lo[1], hi[0], hi[1]};
    }
  };
  f16v acc[6];
#pragma unroll
  for (int i = 0; i < 6; i++)
#pragma unroll
    for (int j = 0; j < 16; j++) acc[i][j] = 0.f;
#pragma unroll
  for (int i = 0; i < PD; i++) lda(i);
  ldb(0);
  u64 ofl = 0;
  auto step = [&](int i, bool pre) {
    SCHED0();
    if (i == 7 || i == 15 || i == 23) { STAMP(12 + i / 4); WAITL0(); BAR(); STAMP(13 + i / 4); SCHED0(); }
    if (i + 1 < 34) ldb(i + 1);
#pragma unroll
    for (int tt = 0; tt < 3; tt++)
#pragma unroll
      for (int ct = 0; ct < 2; ct++) acc[tt * 2 + ct] = mfma32(bf[i & 1][ct], af[i % PD][tt], acc[tt * 2 + ct]);
    SCHED0();
    if (pre) lda(i + PD);
    if (i == 23) {
#pragma unroll
      for (int tt = 0; tt < 3; tt++) { acc[tt * 2] *= r2[tt] * r2[tt]; acc[tt * 2 + 1] *= r2[tt] * r2[tt]; }
#pragma unroll
      for (int k = 0; k < 6; k++) asm volatile("" : "+v"(acc[k]));
      SCHED0();
    }
  };
  constexpr int OCHK = 20;
#pragma unroll
  for (int i = 0; i < OCHK; i++) {
    step(i, i + PD < 24);
    if (i + PD == 24) {
#pragma unroll
      for (int tt = 0; tt < 3; tt++) r2[tt] = __uint_as_float(bld32<SC1>(rws, (row2 + tt * 32 + r) * 16 + 8, WS_STATS));
      ofl = ld_flag(p.ws, fo);
    }
  }
  STAMP(5);
  const bool early = __all(ofl == tag);
  FENCE();
  if (early) {
#pragma unroll
    for (int j = 24; j < OCHK + PD; j++) lda(j);
  }
#pragma unroll
  for (int i = OCHK; i < 24; i++) step(i, false);
  if (!early) {
    wait_flag(p.ws, fo, tag, ld_flag(p.ws, fo));
#pragma unroll
    for (int j = 24; j < OCHK + PD; j++) lda(j);
  }
  STAMP(9);
#pragma unroll
  for (int j = OCHK + PD; j < 24 + PD; j++) lda(j);
#pragma unroll
  for (int i = 24; i < 34; i++) step(i, i + PD < 34);
  STAMP(10);
  const int item = 512 + threadIdx.x;
  const unsigned go = (unsigned)((row2 + (item >> 3)) * 1280 + cg2 * 128 + (item & 7) * 16);
  const u4 xv = *(const GLBP u4*)(p.x + go);
#pragma unroll
  for (int tt = 0; tt < 3; tt++)
#pragma unroll
    for (int ct = 0; ct < 2; ct++)
#pragma unroll
      for (int qd = 0; qd < 4; qd++)
        *(LDSP f4*)(sm + (tt * 4 + (r >> 3)) * 8192 + w * 2048 + (r & 7) * 256 + (((ct * 8 + qd * 2 + hh) ^ (r & 7)) << 4)) =
            f4{acc[tt * 2 + ct][qd * 4], acc[tt * 2 + ct][qd * 4 + 1], acc[tt * 2 + ct][qd * 4 + 2], acc[tt * 2 + ct][qd * 4 + 3]};
  WAITL0(); BAR();
  p2_out(p, sm, item, xv, go);
}

template <int type>
DI void loader(const Params& p, LDSP char* sm, rsrc_t rws, int lw, int lane, int rt, int cg, int row0, int row2, int cg2) {
  const int b = blockIdx.x, t = lw * 64 + lane;
  constexpr bool gu = type == 0;
  unsigned vo[13];
  {
    const int x0 = gu ? lw * 5 : lw * 8;
    const unsigned xr = (unsigned)((row0 + x0 * 8 + (lane >> 3)) * 1280), xs = (unsigned)(((lane & 7) ^ (lane >> 4) ^ ((x0 & 1) << 2)) << 4);
#pragma unroll
    for (int i = 0; i < 8; i++) vo[i] = xr + (xs ^ ((i & 1) << 6)) + i * 10240;
    if (gu) {
      const unsigned g0 = (unsigned)(((lw & 1) * 32 + (lane >> 4)) * 3072 + (((lane & 15) ^ ((lane >> 4) << 2) ^ (lw & 2)) << 4));
#pragma unroll
      for (int i = 5; i < 13; i++) vo[i] = g0 + (i - 5) * 12288;
    } else {
#pragma unroll
      for (int i = 0; i < 5; i++) {
        const int e = i * 64 + lane, rq = (e * 205) >> 12, pos = e - rq * 20;
        const int adj = type == 1 ? 0 : (pos < 10 ? 0 : (pos < 12 ? 1248 : 1088));
        vo[8 + i] = (unsigned)((lw * 16 + rq) * 3840 + pos * 16 + adj);
      }
    }
  }
  const unsigned wofs = gu ? cg * 256 : (type == 1 ? cg * 320 : 1280 + cg * 160);
  const char* wbase = (gu ? ((lw >> 1) ? p.wu : p.wg) : p.wqkv) + wofs;
  const unsigned wstep = gu ? 64 * 3072 : 64 * 3840, wlen = wstep * 10 - wofs, mstep = gu ? wstep : 128;
  const rsrc_t rx = mkrs(p.x, S * D * 2), rw = mkrs(wbase, wlen), rm = mkrs(gu ? wbase : p.x, gu ? wlen : S * D * 2);
  const int dx = gu ? lw * 5120 : lw * 8192, dm = gu ? 20480 + lw * 8192 : dx + 5120, dw = gu ? dm + 3072 : 32768 + lw * 5120;
  CS ck;
  const int task = type == 1 ? (rt * 4 + cg) * 4 + lw : min(156 + lw * 64 + cg * 8 + rt, 234);
  const bool act = !gu && task < 234;
  auto issue = [&](int s, int a0 = 0, int a1 = 0) {
    LDSP char* d = sm + (s % 3) * SLOT;
#pragma unroll
    for (int i = 0; i < 13; i++) {
      if (i < 5) blds16(rx, vo[i], s * 128, d + dx + i * 1024);
      else if (i < 8) blds16(rm, vo[i], s * mstep, d + dm + (i - 5) * 1024);
      else blds16(rw, vo[i], s * wstep, d + dw + (i - 8) * 1024);
      if (i >= 1 && a0 + i - 1 < a1 && (type == 1 || act)) {
        FENCE(); cs_add(ck, a0 + i - 1);
        asm volatile("" : "+v"(ck.c[0]), "+v"(ck.c[1]), "+v"(ck.c[2]), "+v"(ck.c[3]), "+v"(ck.c[4]), "+v"(ck.c[5]), "+v"(ck.c[6]), "+v"(ck.c[7]));
        FENCE();
      }
    }
    FENCE();
  };
  auto csl = [&](int i0, int i1) {
    if (type == 1 || act) {
#pragma unroll
      for (int i = i0; i < i1; i++) cs_load(ck, i);
      FENCE();
    }
  };
  issue(0); issue(1);
  if (!gu) cs_init(p, ck, task, lane);
  constexpr bool q1 = type == 1;
  STAMP(12);
  WAITV(13); BAR(); STAMP(13);
  if (type == 2) {
    bst128(u4{EMPTY, EMPTY, EMPTY, EMPTY}, rws, t * 16, WS_KVP + (rt * 8 + cg) * 7168);
    bst128(u4{EMPTY, EMPTY, EMPTY, EMPTY}, rws, min(t + 256, 419) * 16, WS_KVP + (rt * 8 + cg) * 7168);
    FENCE();
  }
  issue(2);
  const rsrc_t rd_ = mkrs(p.wd, type == 1 ? 0 : F * D * 2), ro_ = mkrs(p.wo, type == 1 ? 0 : D * D * 2);
  const unsigned bo = (unsigned)((lane >> 3) * 1280 + cg2 * 128 + (((lane & 7) ^ (((lane >> 4) & 1) << 2)) << 4));
  auto bdma = [&](int i) {
#pragma unroll
    for (int h = 0; h < 2; h++) blds16(rd_, bo, (lw * 384 + i * 16 + h * 8) * 1280, sm + (17 - i) * 8192 + lw * 2048 + h * 1024);
  };
  u4 br[46];
  auto breg = [&](int i0, int i1) {
#pragma unroll
    for (int i = i0; i < i1; i++) {
      const int st = 11 + (i >> 1);
      br[i] = st < 24 ? bld128<0>(rd_, bo, (lw * 384 + st * 16 + (i & 1) * 8) * 1280) : bld128<0>(ro_, bo, (lw * 160 + (st - 24) * 16 + (i & 1) * 8) * 1280);
    }
    FENCE();
  };
  auto bput = [&](int i0, int i1) {
#pragma unroll
    for (int i = i0; i < i1; i++) {
      const int st = 11 + (i >> 1), sl = st < 18 ? st : st - 18;
      *(LDSP u4*)(sm + (17 - sl) * 8192 + lw * 2048 + (i & 1) * 1024 + lane * 16) = br[i];
    }
  };
  auto bgu = [&](int i0, int i1) { if (gu) breg(i0, i1); };
  auto bkv = [&](int i0, int i1) { if (type == 2) breg(i0, i1); };
#define WAITG(a, b, c) do { if (gu) WAITV(a); else if (type == 1) WAITV(b); else WAITV(c); } while (0)
#define WAITA(a, b, c, d) do { if (type == 2 && act) WAITV(d); else WAITG(a, b, c); } while (0)
  csl(0, q1 ? 11 : 8); WAITA(13, 24, 15, 23); BAR(); STAMP(14);
  issue(3); csl(q1 ? 11 : 8, q1 ? 22 : 16); WAITA(13, 35, 13, 29); BAR(); STAMP(19);
  const u64 tag = lds_tag(sm);
  if (t < (type == 2 ? 2 : 1)) st_flag(p.ws, t ? WS_FKV + (cg * 8 + rt) * 8 : WS_F0 + b * 8, tag);
  FENCE();
  issue(4, 0, q1 ? 11 : 0); bgu(0, 4); csl(22, q1 ? 32 : 0); WAITA(17, 34, 13, 21); BAR(); STAMP(15);
  issue(5, q1 ? 11 : 0, q1 ? 22 : 8); bgu(4, 8); WAITG(21, 23, 13); BAR(); STAMP(16);
  issue(6, q1 ? 22 : 8, q1 ? 32 : 16); bgu(8, 12);
  if (type == 1 || act) cs_store(ck, rws, lane, (unsigned)tag);
  WAITA(21, 14, 13, 14); BAR(); STAMP(6);
  issue(7); bgu(12, 16); bkv(0, 4);
  unsigned sto = min(row0 + t, S - 1) * 16, cso;
  {
    const int tc = gu ? t : min(t, 159);
    cso = 8 * (gu ? 1920 + (tc >> 7) * 1536 + cg * 128 + (tc & 127)
                  : (type == 1 ? cg * 160 + tc : cg * 80 + tc + (tc < 80 ? 640 : (tc < 96 ? 1264 : 1184))));
  }
  asm volatile("" : "+v"(sto), "+v"(cso));
  WAITA(21, 14, 17, 18); BAR(); STAMP(7);
  issue(8); bgu(16, 20); bkv(4, 8); WAITG(21, 13, 21); BAR(); STAMP(17);
  issue(9); bgu(20, 24); bkv(8, 12); WAITG(21, 13, 21); BAR(); STAMP(18);
  u4 stv; u2 csv[3];
  auto aux = [&]() {
    stv = bld128<SC1>(rws, sto, WS_STATS);
#pragma unroll
    for (int i = 0; i < 3; i++) csv[i] = bld64<SC1>(rws, cso, WS_CS + i * NCOL * 8);
    FENCE();
  };
  aux();
#pragma unroll
  for (int i = 5; i < 11; i++) bdma(i);
  FENCE();
  if (type == 1) *(LDSP u2*)(sm + IMG + t * 8) = u2{0, 0};
  WAITG(20, 16, 20); BAR();
  if (type != 1) {
#pragma unroll
    for (int i = 0; i < 5; i++) bdma(i);
    FENCE();
  }
  auto fresh = [&]() {
    const unsigned tg = (unsigned)tag;
    return __all(stv[1] == tg && stv[3] == tg && csv[0][1] == tg && csv[1][1] == tg && csv[2][1] == tg);
  };
  if (!fresh()) {
#pragma unroll 1
    for (int it = 0; it < (1 << 18); it++) { aux(); if (fresh()) break; }
  }
  *(LDSP unsigned*)(sm + AUX + t * 4) = stv[0];
  *(LDSP unsigned*)(sm + AUX + 1024 + t * 4) = stv[2];
  *(LDSP float*)(sm + AUX + 2048 + t * 4) = (__uint_as_float(csv[0][0]) + __uint_as_float(csv[1][0])) + __uint_as_float(csv[2][0]);
  if (type == 1) WAITV(0);
  FENCE();
  bgu(24, 28);
  WAITL0(); BAR();
  STAMP(3);
  if (type == 1) {
    const unsigned fk = WS_FKV + (cg * 16 + (lane & 15)) * 8;
    wait_flag(p.ws, fk, tag, ld_flag(p.ws, fk));
    u4 pv[4][8];
    unsigned off[4], da[4];
#pragma unroll
    for (int m = 0; m < 4; m++) {
      const int g = min(t + m * 256, 999), dd = g / 25, e5 = g - dd * 25, hg = dd >= 20, dq = dd - hg * 20;
      const int eg = (dq / 5) * 20 + (e5 < 20 ? e5 : (e5 - 20) * 4);
      off[m] = WS_KVP + (cg * 2 + hg) * 7168 + (dq * 21 + min(e5, 20)) * 16;
      da[m] = IMG + hg * 25600 + (dq >> 2) * 1024 + (dq & 1) * 512 + ((dq >> 1) & 1) * 8 + (eg >> 4) * 5120 + (eg & 15) * 16 + (e5 < 20 ? 0 : 256);
      asm volatile("" : "+v"(off[m]), "+v"(da[m]));
#pragma unroll
      for (int i = 0; i < 8; i++) asm volatile("" : "=v"(pv[m][i]));
    }
    const u4 rk = {(unsigned)(size_t)p.ws, (unsigned)((size_t)p.ws >> 32) & 0xffffu, WS_END, 0x00020000u};
    auto fetch = [&](unsigned set) {
#pragma unroll
      for (int i = 0; i < 8; i++)
        if (set >> i & 1)
          asm volatile("buffer_load_dwordx4 %0, %4, %8, %9 offen sc1\n\tbuffer_load_dwordx4 %1, %5, %8, %9 offen sc1\n\t"
                       "buffer_load_dwordx4 %2, %6, %8, %9 offen sc1\n\tbuffer_load_dwordx4 %3, %7, %8, %9 offen sc1"
                       : "+v"(pv[0][i]), "+v"(pv[1][i]), "+v"(pv[2][i]), "+v"(pv[3][i])
                       : "v"(off[0]), "v"(off[1]), "v"(off[2]), "v"(off[3]), "s"(rk), "s"(i * 8 * 7168) : "memory");
    };
    auto landed = [&]() {
      WAITV(0);
#pragma unroll
      for (int i = 0; i < 8; i++) asm volatile("" : "+v"(pv[0][i]), "+v"(pv[1][i]), "+v"(pv[2][i]), "+v"(pv[3][i]));
    };
    {
      const unsigned ho = WS_KVP + ((lane & 7) * 8 + cg * 2 + ((lane >> 3) & 1)) * 7168 + 419 * 16;
      unsigned got = 0;
#pragma unroll 1
      for (int it = 0; it < (1 << 18) && got != 255; it++) {
        const unsigned mk = (unsigned)__ballot(bld32<SC1>(rws, ho, 0) != EMPTY), now = mk & (mk >> 8) & 255 & ~got;
        fetch(now);
        got |= now;
      }
    }
    STAMP(4);
    f4 sum[4];
#pragma unroll 1
    for (int it = 0; it < (1 << 18); it++) {
      landed();
      bool nan = false;
#pragma unroll
      for (int m = 0; m < 4; m++) {
        sum[m] = __builtin_bit_cast(f4, pv[m][0]);
#pragma unroll
        for (int i = 1; i < 8; i++) sum[m] += __builtin_bit_cast(f4, pv[m][i]);
        nan = nan || __builtin_isunordered(sum[m][0], sum[m][1]) || __builtin_isunordered(sum[m][2], sum[m][3]);
      }
      if (!__any(nan)) break;
      unsigned mx = 0;
#pragma unroll
      for (int i = 0; i < 8; i++)
#pragma unroll
        for (int m = 0; m < 4; m++) mx = max(max(mx, max(pv[m][i][0], pv[m][i][1])), max(pv[m][i][2], pv[m][i][3]));
      if (!__any(mx == EMPTY)) break;
      fetch(255);
    }
    STAMP(5);
#pragma unroll
    for (int m = 0; m < 4; m++) *(LDSP u2*)(sm + da[m]) = u2{pk2(sum[m][0], sum[m][1]), pk2(sum[m][2], sum[m][3])};
    WAITL0(); BAR();
    BAR();
    return;
  }
  __builtin_amdgcn_s_setprio(0);
  if (gu) {
    BAR();
    breg(28, 38);
    WAITV(14);
    STAMP(4);
  } else {
    breg(12, 30);
    STAMP(4);
    BAR(); kv_publish(sm, rws, rt, cg); WAITL0();
    WAITV(18);
  }
  BAR();
  bput(0, 14);
  FENCE();
  bkv(30, 46); bgu(38, 46);
  WAITL0(); BAR();
  bput(14, 30);
  WAITL0(); BAR();
  bput(30, 46);
  WAITL0(); BAR();
  unsigned go[2]; u4 xv[2];
#pragma unroll
  for (int i = 0; i < 2; i++) {
    const int item = i * 256 + t;
    go[i] = (unsigned)((row2 + (item >> 3)) * 1280 + cg2 * 128 + (item & 7) * 16);
    xv[i] = *(const GLBP u4*)(p.x + go[i]);
  }
  BAR();
#pragma unroll
  for (int i = 0; i < 2; i++) p2_out(p, sm, i * 256 + t, xv[i], go[i]);
}

__global__ __launch_bounds__(512, 2) void fused_kernel(Params) {
  typedef unsigned long long ka_t __attribute__((ext_vector_type(8)));
  ka_t ka;
  asm volatile("s_load_dwordx16 %0, %1, 0x0" : "=s"(ka) : "s"(__builtin_amdgcn_kernarg_segment_ptr()));
  extern __shared__ __attribute__((aligned(16))) char smem[];
  LDSP char* sm = (LDSP char*)smem;
  const int lane = threadIdx.x & 63, w = __builtin_amdgcn_readfirstlane(threadIdx.x >> 6), b = blockIdx.x;
  u64 pc0 = __builtin_amdgcn_s_getpc();
  if (w < 2) {
    const u4 rc = {(unsigned)pc0, (unsigned)(pc0 >> 32) & 0xffffu, CODE_BYTES, 0x00020000u};
    asm volatile("s_mov_b32 m0, %0\n\ts_nop 0\n\tbuffer_load_dword %1, %2, 0 offen lds"
                 :: "s"((unsigned)(size_t)(sm + AUX + 3328)), "v"(min(threadIdx.x * 128 + ((blockIdx.x >> 3) & 3) * 16384, CODE_BYTES - 128)), "s"(rc) : "memory", "m0");
  }
  FENCE();
  int type, rt, cg, n2;
  {
    const int c = b & 7, j = b >> 3, a = c & 1, q = c >> 1;
    int lo = (j - 12) >> 31;
    asm volatile("" : "+s"(lo));
    const int n = j + ((a * 19 - 12) & ~lo), n3 = (n * 43) >> 7, m = n - n3 * 3, t1 = min(m, 1) & lo;
    type = ((t1 + 1) & lo) + ((((b | 6) + 2) >> 8) & ~a) * 3;
    rt = n3 + (4 * a & lo);
    cg = q * (3 + ((t1 - 2) & lo)) + m - t1;
    n2 = j - 4 + ((3 - n3) & lo);
    n2 = max(n2 + q * 55 + a * 27, 0);
  }
  int row0 = min(rt * (b < 96 ? 256 : 160), S - 160);
  const int rt2 = (n2 * 205) >> 11;
  int cg2 = n2 - rt2 * 10, row2 = min(rt2 * 96, S - 96);
  asm volatile("s_waitcnt lgkmcnt(0)" : "+s"(ka), "+s"(type), "+s"(rt), "+s"(cg), "+s"(row0), "+s"(row2), "+s"(cg2));
  const Params p = {(const char*)ka[0], (const char*)ka[1], (const char*)ka[2], (const char*)ka[3], (const char*)ka[4], (const char*)ka[5], (char*)ka[6], (char*)ka[7]};
  STAMPR(0); STAMP(1);
  const rsrc_t rws = mkrs(p.ws, WS_END);
  if (type == 3) {
    u2 xs[5];
    const u64 tag = row_load(p, xs, w & 3, lane) + 1;
    const int task = 128 + ((b >> 1) & 3) + w * 4;
    CS ck;
    cs_init(p, ck, task < 156 ? task : 234, lane);
#pragma unroll
    for (int i = 0; i < 32; i++) cs_load(ck, i);
    FENCE();
    if (w < 4) row_stats(xs, rws, w, lane, (unsigned)tag);
#pragma unroll
    for (int i = 0; i < 32; i++) cs_add(ck, i);
    if (task < 156) cs_store(ck, rws, lane, (unsigned)tag);
    STAMP(3);
    BAR();
    if (w != 0) return;
    if (lane == 0) st_flag(p.ws, WS_F0 + b * 8, tag);
    if (b != 248) return;
#pragma unroll 1
    for (int i = 0; i < 4; i++) wait_flag(p.ws, WS_F0 + (i * 64 + lane) * 8, tag, tag - 1);
    if (lane == 0) st_flag(p.ws, WS_GEN, tag);
    return;
  }
  if (w >= 4) {
    __builtin_amdgcn_s_setprio(3);
    if (type == 2) loader<2>(p, sm, rws, w - 4, lane, rt, cg, row0, row2, cg2);
    else if (type == 0) loader<0>(p, sm, rws, w - 4, lane, rt, cg, row0, row2, cg2);
    else loader<1>(p, sm, rws, w - 4, lane, rt, cg, row0, row2, cg2);
  } else {
    if (type == 0) k1_mfma<0>(p, sm, rws, w, lane, rt, cg, row0);
    else if (type == 1) k1_mfma<1>(p, sm, rws, w, lane, rt, cg, row0);
    else k1_mfma<2>(p, sm, rws, w, lane, rt, cg, row0);
    STAMP(7);
    if (type != 1) p2_mfma(p, sm, rws, w, lane, row2, cg2);
  }
  STAMP(11); STAMPR(2);
}

extern "C" void kernel_launch(void* const* d_in, const int* in_sizes, int n_in, void* d_out, int out_size,
                              void* d_ws, size_t ws_size, hipStream_t stream) {
  Params p;
  p.x = (const char*)d_in[0]; p.wqkv = (const char*)d_in[1]; p.wo = (const char*)d_in[2];
  p.wg = (const char*)d_in[4]; p.wu = (const char*)d_in[5]; p.wd = (const char*)d_in[6];
  p.out = (char*)d_out; p.ws = (char*)d_ws;
  (void)hipFuncSetAttribute((const void*)fused_kernel, hipFuncAttributeMaxDynamicSharedMemorySize, LDS_BYTES);
  fused_kernel<<<256, 512, LDS_BYTES, stream>>>(p);
}
```

```cpp
#include <hip/hip_runtime.h>
#include <hip/hip_bf16.h>
#include <stdint.h>

typedef __attribute__((ext_vector_type(8))) __bf16 bf8;
typedef __attribute__((ext_vector_type(2))) __bf16 bf2;
typedef __attribute__((ext_vector_type(4))) short s4;
typedef __attribute__((ext_vector_type(2))) float f2;
typedef __attribute__((ext_vector_type(4))) float f4;
typedef __attribute__((ext_vector_type(16))) float f16v;
typedef __attribute__((ext_vector_type(2))) unsigned u2;
typedef __attribute__((ext_vector_type(4))) unsigned u4;
typedef unsigned long long u64;
typedef __amdgpu_buffer_rsrc_t rsrc_t;
#define LDSP __attribute__((address_space(3)))
#define GLBP __attribute__((address_space(1)))
#define DI __device__ __forceinline__

constexpr int S = 2048, D = 640, F = 1536;
constexpr int NCOL = 4992;
constexpr unsigned WS_GEN = 0;
constexpr unsigned WS_F0 = 4096;
constexpr unsigned WS_FH = 8192;
constexpr unsigned WS_FKV = 12288;
constexpr unsigned WS_FO = 16384;
constexpr unsigned WS_STATS = 32768;
constexpr unsigned WS_CS = 65536;
constexpr unsigned WS_KVP = 327680;
constexpr unsigned WS_H = 1 << 20;
constexpr unsigned WS_O = WS_H + S * F * 2;
constexpr unsigned WS_END = WS_O + S * D * 2;

constexpr int SLOT = 52 * 1024;
constexpr int LDS_BYTES = 160 * 1024;
constexpr int AUX = 3 * SLOT;
constexpr int IMG = 54 * 1024;
constexpr int SC1 = 16;
constexpr unsigned CODE_BYTES = 62 * 1024;

DI rsrc_t mkrs(const void* b, unsigned n) { return __builtin_amdgcn_make_buffer_rsrc((void*)b, (short)0, (int)n, 0x00020000); }
DI void blds16(rsrc_t rs, unsigned voff, unsigned soff, LDSP char* l) {
  __builtin_amdgcn_raw_ptr_buffer_load_lds(rs, (LDSP void*)l, 16, voff, soff, 0, 0);
}
template <int AUXB> DI u4 bld128(rsrc_t rs, unsigned voff, unsigned soff) { return __builtin_amdgcn_raw_buffer_load_b128(rs, voff, soff, AUXB); }
template <int AUXB> DI u2 bld64(rsrc_t rs, unsigned voff, unsigned soff) { return __builtin_amdgcn_raw_buffer_load_b64(rs, voff, soff, AUXB); }
template <int AUXB> DI unsigned bld32(rsrc_t rs, unsigned voff, unsigned soff) { return __builtin_amdgcn_raw_buffer_load_b32(rs, voff, soff, AUXB); }
DI void bst128(u4 v, rsrc_t rs, unsigned voff, unsigned soff) { __builtin_amdgcn_raw_buffer_store_b128(v, rs, voff, soff, SC1); }
DI void bst64(u2 v, rsrc_t rs, unsigned voff, unsigned soff) { __builtin_amdgcn_raw_buffer_store_b64(v, rs, voff, soff, SC1); }
DI u4 lds_r128(LDSP char* p) { return *(LDSP u4*)p; }
DI u2 lds_tr(LDSP char* p) { return __builtin_bit_cast(u2, __builtin_amdgcn_ds_read_tr16_b64_v4i16((LDSP s4*)p)); }
DI f16v mfma32(u4 a, u4 b, f16v c) { return __builtin_amdgcn_mfma_f32_32x32x16_bf16(__builtin_bit_cast(bf8, a), __builtin_bit_cast(bf8, b), c, 0, 0, 0); }
DI unsigned pk2(float lo, float hi) { f2 v = {lo, hi}; return __builtin_bit_cast(unsigned, __builtin_convertvector(v, bf2)); }
DI float blo(unsigned v) { return __uint_as_float(v << 16); }
DI float bhi(unsigned v) { return __uint_as_float(v & 0xffff0000u); }
DI float exp2c(float u) { return __builtin_amdgcn_fmed3f(__builtin_amdgcn_exp2f(u), 0.f, 1.f); }
constexpr float LOG2E = 1.44269504088896f, LN2 = 0.693147180559945f;
constexpr float PHC = 1e-6f * LOG2E;
DI unsigned phi2(f2 u) {
  const f2 e = {exp2c(u[0]), exp2c(u[1])};
  const f2 v = f2{fmaxf(u[0], PHC), fmaxf(u[1], PHC)} * LN2 + e;
  return pk2(v[0], v[1]);
}
DI u64 ld_flag(const char* ws, unsigned off) { return __hip_atomic_load((const GLBP u64*)(ws + off), __ATOMIC_RELAXED, __HIP_MEMORY_SCOPE_AGENT); }
DI void st_flag(char* ws, unsigned off, u64 v) { __hip_atomic_store((GLBP u64*)(ws + off), v, __ATOMIC_RELAXED, __HIP_MEMORY_SCOPE_AGENT); }
#define BAR() __builtin_amdgcn_s_barrier()
#define WAITV(n) asm volatile("s_waitcnt vmcnt(" #n ")" ::: "memory")
#define WAITL0() asm volatile("s_waitcnt lgkmcnt(0)" ::: "memory")
#define SCHED0() __builtin_amdgcn_sched_barrier(0)
#define FENCE() do { asm volatile("" ::: "memory"); __builtin_amdgcn_sched_barrier(0); } while (0)

#define STAMP(i)
#define STAMPR(i)

struct Params {
  const char* x; const char* wqkv; const char* wo; const char* wg; const char* wu; const char* wd;
  char* out; char* ws;
};

DI void wait_flag(const char* ws, unsigned off, u64 tag, u64 first) {
  u64 v = first;
#pragma unroll 1
  for (int it = 0; it < (1 << 18); it++) {
    if (__all(v == tag)) break;
    __builtin_amdgcn_s_sleep(1);
    v = ld_flag(ws, off);
  }
  FENCE();
}
DI u64 lds_tag(LDSP char* sm) {
  const u2 t = *(LDSP u2*)(sm + AUX + 3072);
  return (u64)(unsigned)__builtin_amdgcn_readfirstlane(t[0]) | ((u64)(unsigned)__builtin_amdgcn_readfirstlane(t[1]) << 32);
}

template <int CTL> DI float dppadd(float v) { return v + __int_as_float(__builtin_amdgcn_update_dpp(0, __float_as_int(v), CTL, 0xf, 0xf, true)); }
DI float swap16add(float v) { const u2 t = __builtin_amdgcn_permlane16_swap(__float_as_uint(v), __float_as_uint(v), false, false); return __uint_as_float(t[0]) + __uint_as_float(t[1]); }
DI float sum32(float v) { return swap16add(dppadd<0x121>(dppadd<0x122>(dppadd<0x124>(dppadd<0x128>(v))))); }
constexpr unsigned EMPTY = 0xffffffffu;

DI u64 row_load(const Params& p, u2 (&xs)[5], int w, int lane) {
  const u64 gen = ld_flag(p.ws, WS_GEN);
  const rsrc_t rx = mkrs(p.x, S * D * 2);
  const unsigned xo = (unsigned)((blockIdx.x * 8 + w * 2 + (lane >> 5)) * 1280 + (lane & 31) * 8);
#pragma unroll
  for (int i = 0; i < 5; i++) xs[i] = bld64<0>(rx, xo + i * 256, 0);
  FENCE();
  return gen;
}
DI void row_stats(const u2 (&xs)[5], rsrc_t rws, int w, int lane, unsigned tag) {
  const int b = blockIdx.x;
  float s = 0;
#pragma unroll
  for (int i = 0; i < 5; i++) s += (blo(xs[i][0]) + bhi(xs[i][0])) + (blo(xs[i][1]) + bhi(xs[i][1]));
  s = sum32(s);
  const float mu = s * (1.f / 640.f);
  float q = 0;
#pragma unroll
  for (int i = 0; i < 5; i++) {
    const float a = blo(xs[i][0]) - mu, c = bhi(xs[i][0]) - mu, e = blo(xs[i][1]) - mu, g = bhi(xs[i][1]) - mu;
    q += a * a + c * c + e * e + g * g;
  }
  q = sum32(q);
  const float rs = rsqrtf(q * (1.f / 640.f) + 1e-5f);
  if ((lane & 31) == 0) bst128(u4{__float_as_uint(mu), tag, __float_as_uint(rs), tag}, rws, (b * 8 + w * 2 + (lane >> 5)) * 16, WS_STATS);
}
struct CS { rsrc_t rs; unsigned vo, step, dst; u4 d[32]; float c[8]; };
DI void cs_init(const Params& p, CS& k, int task, int lane) {
  const int kp = task / 78, cu = task - kp * 78;
  const char* base = cu < 30 ? p.wqkv + cu * 128 : (cu < 54 ? p.wg + (cu - 30) * 128 : p.wu + (cu - 54) * 128);
  const unsigned stride = cu < 30 ? 3840 : 3072;
  k.rs = mkrs(base, task < 234 ? 640 * stride - stride + 128 : 0);
  k.vo = (unsigned)(kp * 256 + (lane >> 3)) * stride + (lane & 7) * 16;
  k.step = 8 * stride;
  k.dst = (kp * NCOL + cu * 64 + (lane & 7) * 8 + (lane >> 5) * 4 + ((lane >> 3) & 1) * 2) * 8;
#pragma unroll
  for (int j = 0; j < 8; j++) k.c[j] = 0;
}
DI void cs_load(CS& k, int i) { k.d[i] = bld128<0>(k.rs, k.vo, i * k.step); }
DI void cs_add(CS& k, int i) {
#pragma unroll
  for (int j = 0; j < 4; j++) { k.c[2 * j] += blo(k.d[i][j]); k.c[2 * j + 1] += bhi(k.d[i][j]); }
}
DI void cs_store(CS& k, rsrc_t rws, int lane, unsigned tag) {
  float e[4];
#pragma unroll
  for (int j = 0; j < 4; j++) {
    const u2 t = __builtin_amdgcn_permlane32_swap(__float_as_uint(k.c[j]), __float_as_uint(k.c[4 + j]), false, false);
    e[j] = dppadd<0x128>(swap16add(__uint_as_float(t[0]) + __uint_as_float(t[1])));
  }
  if ((lane & 16) == 0) bst128(u4{__float_as_uint(lane & 8 ? e[2] : e[0]), tag, __float_as_uint(lane & 8 ? e[3] : e[1]), tag}, rws, k.dst, WS_CS);
}

template <int TYPE>
DI void k1_main(LDSP char* sm, int w, int lane, f16v (&acc)[10]) {
  constexpr int NXF = TYPE == 0 ? 5 : 2, NWF = 7 - NXF;
  constexpr int XB = TYPE == 0 ? 160 * 128 : 256 * 128;
  const int r = lane & 31, hh = lane >> 5, blk = (lane >> 4) & 1, q = (lane & 15) >> 2, pp = lane & 3;
  unsigned xa[4];
#pragma unroll
  for (int k = 0; k < 4; k++) xa[k] = (unsigned)(((TYPE == 0 ? 0 : w * 64) + r) * 128 + (((2 * k + hh) ^ ((r >> 1) & 7)) << 4));
  unsigned wa[2];
  if (TYPE == 0) {
#pragma unroll
    for (int ft = 0; ft < 2; ft++)
      wa[ft] = (unsigned)(XB + blk * 16384 + (8 * hh + q) * 256 + (((w * 4 + ft * 2 + (pp >> 1)) ^ (q << 2) ^ (blk << 1)) << 4) + (pp & 1) * 8);
  } else {
    wa[0] = (unsigned)(XB + (8 * hh + q) * 320 + blk * 32 + pp * 8); wa[1] = 0;
  }
  u4 xf[2][NXF], wf[2][NWF];
  auto rd = [&](int buf, LDSP char* base, int k) {
#pragma unroll
    for (int i = 0; i < NWF; i++) {
      u2 lo, hi;
      if (TYPE == 0) { lo = lds_tr(base + wa[i] + k * 4096); hi = lds_tr(base + wa[i] + k * 4096 + 1024); }
      else { lo = lds_tr(base + wa[0] + k * 5120 + i * 64); hi = lds_tr(base + wa[0] + k * 5120 + i * 64 + 1280); }
      wf[buf][i] = u4{lo[0], lo[1], hi[0], hi[1]};
    }
#pragma unroll
    for (int i = 0; i < NXF; i++) xf[buf][i] = lds_r128(base + xa[k] + i * 4096);
  };
  auto mm = [&](int buf) {
#pragma unroll
    for (int i = 0; i < NXF; i++)
#pragma unroll
      for (int j = 0; j < NWF; j++) {
        if (TYPE == 2) acc[i * NWF + j] = mfma32(xf[buf][i], wf[buf][j], acc[i * NWF + j]);
        else acc[i * NWF + j] = mfma32(wf[buf][j], xf[buf][i], acc[i * NWF + j]);
      }
  };
  auto inter = [&]() {
    constexpr int NR = NXF + 2 * NWF;
#pragma unroll
    for (int i = 0; i < 6; i++) {
      __builtin_amdgcn_sched_group_barrier(0x8, 1, 0);
      __builtin_amdgcn_sched_group_barrier(0x100, (NR + 5) / 6, 0);
    }
    __builtin_amdgcn_sched_group_barrier(0x8, 4, 0);
  };
  BAR();
  rd(0, sm, 0);
  int slot = 0;
#pragma unroll 1
  for (int s = 0; s < 9; s++) {
    LDSP char* base = sm + slot * SLOT;
    slot = slot == 2 ? 0 : slot + 1;
    SCHED0();
    rd(1, base, 1); mm(0); inter(); SCHED0();
    rd(0, base, 2); mm(1); inter(); SCHED0();
    rd(1, base, 3); mm(0); inter(); SCHED0();
    WAITL0(); BAR(); SCHED0();
    rd(0, sm + slot * SLOT, 0); mm(1); inter(); SCHED0();
  }
  SCHED0();
  rd(1, sm, 1); mm(0); inter(); SCHED0();
  rd(0, sm, 2); mm(1); inter(); SCHED0();
  rd(1, sm, 3); mm(0); inter(); SCHED0();
  mm(1); SCHED0();
}

DI void kv_publish(LDSP char* sm, rsrc_t rws, int rt, int cg) {
  const int t = threadIdx.x;
  if (t < 420) {
    LDSP char* src = sm + t * 16;
    const f4 s = (*(LDSP f4*)src + *(LDSP f4*)(src + 8192)) + (*(LDSP f4*)(src + 16384) + *(LDSP f4*)(src + 24576));
    u4 v = __builtin_bit_cast(u4, s);
#pragma unroll
    for (int j = 0; j < 4; j++) v[j] = min(v[j], EMPTY - 1);
    bst128(v, rws, t * 16, WS_KVP + (rt * 8 + cg) * 7168);
  }
}

template <int TYPE>
DI void k1_mfma(const Params& p, LDSP char* sm, rsrc_t rws, int w, int lane, int rt, int cg, int row0) {
  if (TYPE == 0 && (blockIdx.x >> 6) == 2) {
    const u64 a1 = (u64)(w == 0 ? p.wqkv : (w == 1 ? p.wg : (w == 2 ? p.wu : p.wd))), a2 = (u64)(w & 1 ? p.wo : p.x);
    const unsigned n1 = w == 0 ? D * 3840 : F * D * 2, n2 = w & 1 ? D * D * 2 : S * D * 2;
    const u4 r1 = {(unsigned)a1, (unsigned)(a1 >> 32) & 0xffffu, n1, 0x00020000u}, r2 = {(unsigned)a2, (unsigned)(a2 >> 32) & 0xffffu, n2, 0x00020000u};
    asm volatile("s_mov_b32 m0, %4\n\ts_nop 0\n\tbuffer_load_dword %0, %2, 0 offen lds\n\tbuffer_load_dword %1, %3, 0 offen lds"
                 :: "v"((lane * 8 + ((blockIdx.x >> 3) & 7)) * (n1 >> 9)), "v"((lane * 16 + (w >> 1) * 8 + ((blockIdx.x >> 3) & 7)) * (n2 >> 10)), "s"(r1), "s"(r2), "s"((unsigned)(size_t)(sm + AUX + 3328)) : "memory", "m0");
  }
  u2 xs[5];
  const u64 gen = row_load(p, xs, w, lane);
  const int hh = lane >> 5, r = lane & 31;
  f16v acc[10];
#pragma unroll
  for (int i = 0; i < 10; i++)
#pragma unroll
    for (int j = 0; j < 16; j++) acc[i][j] = 0.f;
#pragma unroll
  for (int i = 0; i < 10; i++) asm volatile("" : "+v"(acc[i]));
  FENCE();
  {
    const u64 tag = gen + 1;
    row_stats(xs, rws, w, lane, (unsigned)tag);
    if (threadIdx.x == 0) *(LDSP u2*)(sm + AUX + 3072) = u2{(unsigned)tag, (unsigned)(tag >> 32)};
    WAITL0();
    const u64 wa = (u64)p.ws, oa = (u64)p.out;
    const u4 r1 = {(unsigned)wa, (unsigned)(wa >> 32) & 0xffffu, WS_END, 0x00020000u}, r2 = {(unsigned)oa, (unsigned)(oa >> 32) & 0xffffu, S * D * 2, 0x00020000u};
    if (TYPE == 0 && (blockIdx.x >> 5) == 3)
    asm volatile("s_mov_b32 m0, %4\n\ts_nop 0\n\tbuffer_load_dword %0, %2, 0 offen sc1 lds\n\tbuffer_load_dword %1, %3, 0 offen sc1 lds"
                 :: "v"((threadIdx.x * 4 + ((blockIdx.x >> 3) & 3)) * 9728), "v"((threadIdx.x * 4 + ((blockIdx.x >> 3) & 3)) * 2560), "s"(r1), "s"(r2), "s"((unsigned)(size_t)(sm + AUX + 3328)) : "memory", "m0");
    STAMP(12);
  }
  k1_main<TYPE>(sm, w, lane, acc);
  STAMP(3);
  BAR(); FENCE();
  STAMP(19);
  if (TYPE == 0) {
    f4 cs[2][4];
#pragma unroll
    for (int ft = 0; ft < 2; ft++)
#pragma unroll
      for (int qd = 0; qd < 4; qd++)
        cs[ft][qd] = *(LDSP f4*)(sm + AUX + 2048 + ((qd >> 1) * 128 + w * 32 + ft * 16 + (qd & 1) * 8 + hh * 4) * 4);
    const unsigned dst = (unsigned)(((row0 >> 5) * 96 + cg * 8 + w * 2) * 1024 + lane * 16);
#pragma unroll
    for (int tt = 0; tt < 5; tt++) {
      const float mu = *(LDSP float*)(sm + AUX + (tt * 32 + r) * 4);
      if (tt == 0 && rt == 12) continue;
#pragma unroll
      for (int ft = 0; ft < 2; ft++) {
        const f16v a = acc[tt * 2 + ft];
        float h[8];
#pragma unroll
        for (int j = 0; j < 8; j++) h[j] = (a[j] - mu * cs[ft][j >> 2][j & 3]) * (a[8 + j] - mu * cs[ft][2 + (j >> 2)][j & 3]);
        bst128(u4{pk2(h[0], h[1]), pk2(h[2], h[3]), pk2(h[4], h[5]), pk2(h[6], h[7])}, rws, dst + ft * 1024, WS_H + tt * 98304);
      }
    }
    STAMP(4);
    FENCE(); BAR();
    WAITV(0); BAR();
    if (threadIdx.x == 0) st_flag(p.ws, WS_FH + (rt * 16 + cg) * 8, lds_tag(sm));
  } else if (TYPE == 1) {
    u4 pq[2][10];
#pragma unroll
    for (int tt = 0; tt < 2; tt++) {
      const float mu = *(LDSP float*)(sm + AUX + (w * 64 + tt * 32 + r) * 4);
      const float rl = *(LDSP float*)(sm + AUX + 1024 + (w * 64 + tt * 32 + r) * 4) * LOG2E;
#pragma unroll
      for (int ft = 0; ft < 5; ft++) {
        const f16v a = acc[tt * 5 + ft];
#pragma unroll
        for (int qd = 0; qd < 4; qd++) {
          const f4 cs = *(LDSP f4*)(sm + AUX + 2048 + (ft * 32 + qd * 8 + hh * 4) * 4);
#pragma unroll
          for (int j = 0; j < 2; j++) {
            const f2 u = (f2{a[qd * 4 + 2 * j], a[qd * 4 + 2 * j + 1]} - f2{cs[2 * j], cs[2 * j + 1]} * mu) * rl + PHC;
            pq[tt][ft * 2 + (qd >> 1)][(qd & 1) * 2 + j] = phi2(u);
          }
        }
        asm volatile("" : "+v"(pq[tt][ft * 2]), "+v"(pq[tt][ft * 2 + 1]));
      }
    }
    STAMP(4);
    SCHED0(); BAR(); SCHED0();
    STAMP(5);
    const unsigned dst = (unsigned)((((row0 >> 5) + w * 2) * 40 + cg * 10) * 1024 + lane * 16);
    f16v oa[2][2];
    u4 af[2][3];
    auto rdi = [&](int n) {
      const int hg = n / 5, T = n % 5, k0 = T < 2 ? 0 : T - 1, k1 = T == 0 ? 1 : (T == 4 ? 4 : T + 1);
#pragma unroll
      for (int k = k0; k <= k1; k++) af[n & 1][k - k0] = lds_r128(sm + IMG + (hg * 25 + T * 5 + k) * 1024 + lane * 16);
    };
    auto omm = [&](int n) {
      const int hg = n / 5, T = n % 5, k0 = T < 2 ? 0 : T - 1, k1 = T == 0 ? 1 : (T == 4 ? 4 : T + 1);
#pragma unroll
      for (int tt = 0; tt < 2; tt++)
#pragma unroll
        for (int j = 0; j < 16; j++) oa[n & 1][tt][j] = 0.f;
#pragma unroll
      for (int k = k0; k <= k1; k++)
#pragma unroll
        for (int tt = 0; tt < 2; tt++) oa[n & 1][tt] = mfma32(af[n & 1][k - k0], pq[tt][hg * 5 + k], oa[n & 1][tt]);
    };
    rdi(0); rdi(1); omm(0); SCHED0();
#pragma unroll
    for (int n = 0; n < 10; n++) {
      if (n + 1 < 10) omm(n + 1);
      if (n + 2 < 10) rdi(n + 2);
#pragma unroll
      for (int tt = 0; tt < 2; tt++) {
        const f16v a = oa[n & 1][tt];
        const float d0 = __builtin_amdgcn_rcpf(a[8] + 1e-6f), d1 = __builtin_amdgcn_rcpf(a[12] + 1e-6f);
        bst128(u4{pk2(a[0] * d0, a[1] * d0), pk2(a[2] * d0, a[3] * d0), pk2(a[4] * d1, a[5] * d1), pk2(a[6] * d1, a[7] * d1)},
               rws, dst + (n & 3) * 1024, WS_O + tt * 40960 + (n >> 2) * 4096);
      }
#pragma unroll
      for (int i = 0; i < 6; i++) { __builtin_amdgcn_sched_group_barrier(0x8, 1, 0); __builtin_amdgcn_sched_group_barrier(0x2, 5, 0); }
      SCHED0();
    }
    STAMP(6);
    WAITV(0); BAR();
    if (threadIdx.x == 0) st_flag(p.ws, WS_FO + (rt * 16 + cg) * 8, lds_tag(sm));
  } else {
    float cs[5];
#pragma unroll
    for (int ct = 0; ct < 5; ct++) cs[ct] = *(LDSP float*)(sm + AUX + 2048 + (ct * 32 + r) * 4);
    const bool isk = r < 16;
    const float kmul = isk ? 1.f : 0.f, kadd = isk ? 1e-6f : 0.f, klo = isk ? 1e-6f : -__builtin_inff();
    u4 fr[4][5];
#pragma unroll
    for (int g = 0; g < 4; g++) {
      f2 mu[4], rs[4], rl[4];
#pragma unroll
      for (int hq = 0; hq < 2; hq++) {
        LDSP char* sp = sm + AUX + (w * 64 + g * 16 + hq * 8 + hh * 4) * 4;
        const f4 a = *(LDSP f4*)sp, b = *(LDSP f4*)(sp + 1024);
        mu[hq * 2] = f2{a[0], a[1]}; mu[hq * 2 + 1] = f2{a[2], a[3]};
        rs[hq * 2] = f2{b[0], b[1]}; rs[hq * 2 + 1] = f2{b[2], b[3]};
      }
#pragma unroll
      for (int j = 0; j < 4; j++) rl[j] = rs[j] * LOG2E;
#pragma unroll
      for (int ct = 0; ct < 5; ct++) {
#pragma unroll
        for (int j = 0; j < 4; j++) {
          const f16v& a = acc[(g >> 1) * 5 + ct];
          const f2 y = f2{a[(g & 1) * 8 + 2 * j], a[(g & 1) * 8 + 2 * j + 1]} - mu[j] * cs[ct];
          if (ct < 2) fr[g][ct][j] = phi2(y * rl[j] + PHC);
          else if (ct == 2) {
            const f2 u = y * rl[j], t = y * rs[j] + kadd, e = {exp2c(u[0]), exp2c(u[1])};
            const f2 v = e * kmul + f2{fmaxf(t[0], klo), fmaxf(t[1], klo)};
            fr[g][ct][j] = pk2(v[0], v[1]);
          } else { const f2 v = y * rs[j]; fr[g][ct][j] = pk2(v[0], v[1]); }
        }
      }
      SCHED0();
    }
    f16v z[8];
#pragma unroll
    for (int i = 0; i < 8; i++)
#pragma unroll
      for (int j = 0; j < 16; j++) z[i][j] = 0.f;
#pragma unroll
    for (int g = 0; g < 4; g++) {
      u4 mb;
#pragma unroll
      for (int j = 0; j < 4; j++) mb[j] = isk ? 0x3f803f80u : fr[g][2][j];
      z[0] = mfma32(fr[g][0], fr[g][3], z[0]);
      z[1] = mfma32(fr[g][0], fr[g][4], z[1]);
      z[2] = mfma32(fr[g][0], mb, z[2]);
      z[3] = mfma32(fr[g][1], fr[g][3], z[3]);
      z[4] = mfma32(fr[g][1], fr[g][4], z[4]);
      z[5] = mfma32(fr[g][1], mb, z[5]);
      z[6] = mfma32(fr[g][2], fr[g][4], z[6]);
      z[7] = mfma32(fr[g][2], mb, z[7]);
    }
    unsigned pa[3], pm[3];
#pragma unroll
    for (int vt = 0; vt < 3; vt++) {
      const int e = vt == 2 ? 48 + r : vt * 32 + r, he = e / 20;
      int ee = e - he * 20; unsigned m = 31u << (5 * he);
      if (vt == 2 && isk) { ee = 20; m = r == 0 ? 0xfffffu : 0u; }
      pa[vt] = (unsigned)(w * 8192 + ee * 16 + hh * 336); pm[vt] = m >> hh;
    }
    SCHED0();
#pragma unroll
    for (int i = 0; i < 8; i++)
#pragma unroll
      for (int qd = 0; qd < 4; qd++) {
        const int kt = i / 3, vt = i < 6 ? i % 3 : i - 5, c = kt * 8 + qd * 2;
        const bool need = (i == 1) ? qd >= 2 : (i == 3) ? qd == 0 : (i >= 6) ? qd < 2 : true;
        if (need && (pm[vt] & (1u << c)))
          *(LDSP f4*)(sm + pa[vt] + c * 336) = f4{z[i][qd * 4], z[i][qd * 4 + 1], z[i][qd * 4 + 2], z[i][qd * 4 + 3]};
      }
    STAMP(4);
    WAITL0(); BAR();
    kv_publish(sm, rws, rt, cg);
    WAITL0(); BAR();
  }
}

DI void p2_out(const Params& p, LDSP char* sm, int item, u4 xv, unsigned go) {
  const int row = item >> 3, c = item & 7;
  LDSP char* src = sm + (row >> 3) * 8192 + (row & 7) * 256;
  const unsigned c0 = ((2 * c) ^ (row & 7)) << 4, c1 = c0 ^ 16;
  f4 a = *(LDSP f4*)(src + c0), b = *(LDSP f4*)(src + c1);
#pragma unroll
  for (int i = 1; i < 4; i++) { a += *(LDSP f4*)(src + i * 2048 + c0); b += *(LDSP f4*)(src + i * 2048 + c1); }
  bst128(u4{pk2(blo(xv[0]) + a[0], bhi(xv[0]) + a[1]), pk2(blo(xv[1]) + a[2], bhi(xv[1]) + a[3]),
            pk2(blo(xv[2]) + b[0], bhi(xv[2]) + b[1]), pk2(blo(xv[3]) + b[2], bhi(xv[3]) + b[3])}, mkrs(p.out, S * D * 2), go, 0);
}

constexpr int PD = 8;
DI void p2_mfma(const Params& p, LDSP char* sm, rsrc_t rws, int w, int lane, int row2, int cg2) {
  const int hh = lane >> 5, r = lane & 31, blk = (lane >> 4) & 1, q = (lane & 15) >> 2, pp = lane & 3;
  const u64 tag = lds_tag(sm);
  const unsigned fh = WS_FH + (((row2 + ((lane >> 2) & 1) * 95) / 160) * 16 + 3 * w + min(lane & 3, 2)) * 8;
  const unsigned fo = WS_FO + (((row2 + (lane & 1) * 95) >> 8) * 16 + w) * 8;
  float r2[3];
  wait_flag(p.ws, fh, tag, ld_flag(p.ws, fh));
  STAMP(8);
  const unsigned ah = (unsigned)(((row2 >> 5) * 96 + w * 24) * 1024 + lane * 16), ao = (unsigned)(((row2 >> 5) * 40 + w * 10) * 1024 + lane * 16);
  u4 af[PD][3], bf[2][2];
  auto lda = [&](int i) {
#pragma unroll
    for (int tt = 0; tt < 3; tt++) {
      if (i < 24) af[i % PD][tt] = bld128<SC1>(rws, ah + (i & 3) * 1024, WS_H + tt * 98304 + (i >> 2) * 4096);
      else af[i % PD][tt] = bld128<SC1>(rws, ao + (i & 3) * 1024, WS_O + tt * 40960 + ((i - 24) >> 2) * 4096);
    }
  };
  LDSP char* ba[2][3];
#pragma unroll
  for (int ct = 0; ct < 2; ct++)
#pragma unroll
    for (int sg = 0; sg < 3; sg++) {
      ba[ct][sg] = sm + sg * 49152 + w * 2048 + (4 * hh + q) * 128 + (((ct * 4 + blk * 2 + (pp >> 1)) ^ ((q >> 1) << 2)) << 4) + (pp & 1) * 8;
      asm volatile("" : "+v"(ba[ct][sg]));
    }
  auto ldb = [&](int i) {
    const int sl = 17 - (i < 18 ? i : i - 18);
#pragma unroll
    for (int ct = 0; ct < 2; ct++) {
      LDSP char* base = ba[ct][sl / 6] + (sl % 6) * 8192;
      const u2 lo = lds_tr(base), hi = lds_tr(base + 1024);
      bf[i & 1][ct] = u4{lo[0], lo[1], hi[0], hi[1]};
    }
  };
  f16v acc[6];
#pragma unroll
  for (int i = 0; i < 6; i++)
#pragma unroll
    for (int j = 0; j < 16; j++) acc[i][j] = 0.f;
#pragma unroll
  for (int i = 0; i < PD; i++) lda(i);
  ldb(0);
  u64 ofl = 0;
  auto step = [&](int i, bool pre) {
    SCHED0();
    if (i == 7 || i == 15 || i == 23) { STAMP(12 + i / 4); WAITL0(); BAR(); STAMP(13 + i / 4); SCHED0(); }
    if (i + 1 < 34) ldb(i + 1);
#pragma unroll
    for (int tt = 0; tt < 3; tt++)
#pragma unroll
      for (int ct = 0; ct < 2; ct++) acc[tt * 2 + ct] = mfma32(bf[i & 1][ct], af[i % PD][tt], acc[tt * 2 + ct]);
    SCHED0();
    if (pre) lda(i + PD);
    if (i == 23) {
#pragma unroll
      for (int tt = 0; tt < 3; tt++) { acc[tt * 2] *= r2[tt] * r2[tt]; acc[tt * 2 + 1] *= r2[tt] * r2[tt]; }
#pragma unroll
      for (int k = 0; k < 6; k++) asm volatile("" : "+v"(acc[k]));
      SCHED0();
    }
  };
  constexpr int OCHK = 20;
#pragma unroll
  for (int i = 0; i < OCHK; i++) {
    step(i, i + PD < 24);
    if (i + PD == 24) {
#pragma unroll
      for (int tt = 0; tt < 3; tt++) r2[tt] = __uint_as_float(bld32<SC1>(rws, (row2 + tt * 32 + r) * 16 + 8, WS_STATS));
      ofl = ld_flag(p.ws, fo);
    }
  }
  STAMP(5);
  const bool early = __all(ofl == tag);
  FENCE();
  if (early) {
#pragma unroll
    for (int j = 24; j < OCHK + PD; j++) lda(j);
  }
#pragma unroll
  for (int i = OCHK; i < 24; i++) step(i, false);
  if (!early) {
    wait_flag(p.ws, fo, tag, ld_flag(p.ws, fo));
#pragma unroll
    for (int j = 24; j < OCHK + PD; j++) lda(j);
  }
  STAMP(9);
#pragma unroll
  for (int j = OCHK + PD; j < 24 + PD; j++) lda(j);
#pragma unroll
  for (int i = 24; i < 34; i++) step(i, i + PD < 34);
  STAMP(10);
  const int item = 512 + threadIdx.x;
  const unsigned go = (unsigned)((row2 + (item >> 3)) * 1280 + cg2 * 128 + (item & 7) * 16);
  const u4 xv = *(const GLBP u4*)(p.x + go);
#pragma unroll
  for (int tt = 0; tt < 3; tt++)
#pragma unroll
    for (int ct = 0; ct < 2; ct++)
#pragma unroll
      for (int qd = 0; qd < 4; qd++)
        *(LDSP f4*)(sm + (tt * 4 + (r >> 3)) * 8192 + w * 2048 + (r & 7) * 256 + (((ct * 8 + qd * 2 + hh) ^ (r & 7)) << 4)) =
            f4{acc[tt * 2 + ct][qd * 4], acc[tt * 2 + ct][qd * 4 + 1], acc[tt * 2 + ct][qd * 4 + 2], acc[tt * 2 + ct][qd * 4 + 3]};
  WAITL0(); BAR();
  p2_out(p, sm, item, xv, go);
}

template <int type>
DI void loader(const Params& p, LDSP char* sm, rsrc_t rws, int lw, int lane, int rt, int cg, int row0, int row2, int cg2) {
  const int b = blockIdx.x, t = lw * 64 + lane;
  constexpr bool gu = type == 0;
  unsigned vo[13];
  {
    const int x0 = gu ? lw * 5 : lw * 8;
    const unsigned xr = (unsigned)((row0 + x0 * 8 + (lane >> 3)) * 1280), xs = (unsigned)(((lane & 7) ^ (lane >> 4) ^ ((x0 & 1) << 2)) << 4);
#pragma unroll
    for (int i = 0; i < 8; i++) vo[i] = xr + (xs ^ ((i & 1) << 6)) + i * 10240;
    if (gu) {
      const unsigned g0 = (unsigned)(((lw & 1) * 32 + (lane >> 4)) * 3072 + (((lane & 15) ^ ((lane >> 4) << 2) ^ (lw & 2)) << 4));
#pragma unroll
      for (int i = 5; i < 13; i++) vo[i] = g0 + (i - 5) * 12288;
    } else {
#pragma unroll
      for (int i = 0; i < 5; i++) {
        const int e = i * 64 + lane, rq = (e * 205) >> 12, pos = e - rq * 20;
        const int adj = type == 1 ? 0 : (pos < 10 ? 0 : (pos < 12 ? 1248 : 1088));
        vo[8 + i] = (unsigned)((lw * 16 + rq) * 3840 + pos * 16 + adj);
      }
    }
  }
  const unsigned wofs = gu ? cg * 256 : (type == 1 ? cg * 320 : 1280 + cg * 160);
  const char* wbase = (gu ? ((lw >> 1) ? p.wu : p.wg) : p.wqkv) + wofs;
  const unsigned wstep = gu ? 64 * 3072 : 64 * 3840, wlen = wstep * 10 - wofs, mstep = gu ? wstep : 128;
  const rsrc_t rx = mkrs(p.x, S * D * 2), rw = mkrs(wbase, wlen), rm = mkrs(gu ? wbase : p.x, gu ? wlen : S * D * 2);
  const int dx = gu ? lw * 5120 : lw * 8192, dm = gu ? 20480 + lw * 8192 : dx + 5120, dw = gu ? dm + 3072 : 32768 + lw * 5120;
  CS ck;
  const int task = type == 1 ? (rt * 4 + cg) * 4 + lw : min(156 + lw * 64 + cg * 8 + rt, 234);
  const bool act = !gu && task < 234;
  auto issue = [&](int s, int a0 = 0, int a1 = 0) {
    LDSP char* d = sm + (s % 3) * SLOT;
#pragma unroll
    for (int i = 0; i < 13; i++) {
      if (i < 5) blds16(rx, vo[i], s * 128, d + dx + i * 1024);
      else if (i < 8) blds16(rm, vo[i], s * mstep, d + dm + (i - 5) * 1024);
      else blds16(rw, vo[i], s * wstep, d + dw + (i - 8) * 1024);
      if (i >= 1 && a0 + i - 1 < a1 && (type == 1 || act)) {
        FENCE(); cs_add(ck, a0 + i - 1);
        asm volatile("" : "+v"(ck.c[0]), "+v"(ck.c[1]), "+v"(ck.c[2]), "+v"(ck.c[3]), "+v"(ck.c[4]), "+v"(ck.c[5]), "+v"(ck.c[6]), "+v"(ck.c[7]));
        FENCE();
      }
    }
    FENCE();
  };
  auto csl = [&](int i0, int i1) {
    if (type == 1 || act) {
#pragma unroll
      for (int i = i0; i < i1; i++) cs_load(ck, i);
      FENCE();
    }
  };
  issue(0); issue(1);
  if (!gu) cs_init(p, ck, task, lane);
  constexpr bool q1 = type == 1;
  STAMP(12);
  WAITV(13); BAR(); STAMP(13);
  if (type == 2) {
    bst128(u4{EMPTY, EMPTY, EMPTY, EMPTY}, rws, t * 16, WS_KVP + (rt * 8 + cg) * 7168);
    bst128(u4{EMPTY, EMPTY, EMPTY, EMPTY}, rws, min(t + 256, 419) * 16, WS_KVP + (rt * 8 + cg) * 7168);
    FENCE();
  }
  issue(2);
  const rsrc_t rd_ = mkrs(p.wd, type == 1 ? 0 : F * D * 2), ro_ = mkrs(p.wo, type == 1 ? 0 : D * D * 2);
  const unsigned bo = (unsigned)((lane >> 3) * 1280 + cg2 * 128 + (((lane & 7) ^ (((lane >> 4) & 1) << 2)) << 4));
  auto bdma = [&](int i) {
#pragma unroll
    for (int h = 0; h < 2; h++) blds16(rd_, bo, (lw * 384 + i * 16 + h * 8) * 1280, sm + (17 - i) * 8192 + lw * 2048 + h * 1024);
  };
  u4 br[46];
  auto breg = [&](int i0, int i1) {
#pragma unroll
    for (int i = i0; i < i1; i++) {
      const int st = 11 + (i >> 1);
      br[i] = st < 24 ? bld128<0>(rd_, bo, (lw * 384 + st * 16 + (i & 1) * 8) * 1280) : bld128<0>(ro_, bo, (lw * 160 + (st - 24) * 16 + (i & 1) * 8) * 1280);
    }
    FENCE();
  };
  auto bput = [&](int i0, int i1) {
#pragma unroll
    for (int i = i0; i < i1; i++) {
      const int st = 11 + (i >> 1), sl = st < 18 ? st : st - 18;
      *(LDSP u4*)(sm + (17 - sl) * 8192 + lw * 2048 + (i & 1) * 1024 + lane * 16) = br[i];
    }
  };
  auto bgu = [&](int i0, int i1) { if (gu) breg(i0, i1); };
  auto bkv = [&](int i0, int i1) { if (type == 2) breg(i0, i1); };
#define WAITG(a, b, c) do { if (gu) WAITV(a); else if (type == 1) WAITV(b); else WAITV(c); } while (0)
#define WAITA(a, b, c, d) do { if (type == 2 && act) WAITV(d); else WAITG(a, b, c); } while (0)
  csl(0, q1 ? 11 : 8); WAITA(13, 24, 15, 23); BAR(); STAMP(14);
  issue(3); csl(q1 ? 11 : 8, q1 ? 22 : 16); WAITA(13, 35, 13, 29); BAR(); STAMP(19);
  const u64 tag = lds_tag(sm);
  if (t < (type == 2 ? 2 : 1)) st_flag(p.ws, t ? WS_FKV + (cg * 8 + rt) * 8 : WS_F0 + b * 8, tag);
  FENCE();
  issue(4, 0, q1 ? 11 : 0); bgu(0, 4); csl(22, q1 ? 32 : 0); WAITA(17, 34, 13, 21); BAR(); STAMP(15);
  issue(5, q1 ? 11 : 0, q1 ? 22 : 8); bgu(4, 8); WAITG(21, 23, 13); BAR(); STAMP(16);
  issue(6, q1 ? 22 : 8, q1 ? 32 : 16); bgu(8, 12);
  if (type == 1 || act) cs_store(ck, rws, lane, (unsigned)tag);
  WAITA(21, 14, 13, 14); BAR(); STAMP(6);
  issue(7); bgu(12, 16); bkv(0, 4);
  unsigned sto = min(row0 + t, S - 1) * 16, cso;
  {
    const int tc = gu ? t : min(t, 159);
    cso = 8 * (gu ? 1920 + (tc >> 7) * 1536 + cg * 128 + (tc & 127)
                  : (type == 1 ? cg * 160 + tc : cg * 80 + tc + (tc < 80 ? 640 : (tc < 96 ? 1264 : 1184))));
  }
  asm volatile("" : "+v"(sto), "+v"(cso));
  WAITA(21, 14, 17, 18); BAR(); STAMP(7);
  issue(8); bgu(16, 20); bkv(4, 8); WAITG(21, 13, 21); BAR(); STAMP(17);
  issue(9); bgu(20, 24); bkv(8, 12); WAITG(21, 13, 21); BAR(); STAMP(18);
  u4 stv; u2 csv[3];
  auto aux = [&]() {
    stv = bld128<SC1>(rws, sto, WS_STATS);
#pragma unroll
    for (int i = 0; i < 3; i++) csv[i] = bld64<SC1>(rws, cso, WS_CS + i * NCOL * 8);
    FENCE();
  };
  aux();
#pragma unroll
  for (int i = 5; i < 11; i++) bdma(i);
  FENCE();
  if (type == 1) *(LDSP u2*)(sm + IMG + t * 8) = u2{0, 0};
  WAITG(20, 16, 20); BAR();
  if (type != 1) {
#pragma unroll
    for (int i = 0; i < 5; i++) bdma(i);
    FENCE();
  }
  auto fresh = [&]() {
    const unsigned tg = (unsigned)tag;
    return __all(stv[1] == tg && stv[3] == tg && csv[0][1] == tg && csv[1][1] == tg && csv[2][1] == tg);
  };
  if (!fresh()) {
#pragma unroll 1
    for (int it = 0; it < (1 << 18); it++) { aux(); if (fresh()) break; }
  }
  *(LDSP unsigned*)(sm + AUX + t * 4) = stv[0];
  *(LDSP unsigned*)(sm + AUX + 1024 + t * 4) = stv[2];
  *(LDSP float*)(sm + AUX + 2048 + t * 4) = (__uint_as_float(csv[0][0]) + __uint_as_float(csv[1][0])) + __uint_as_float(csv[2][0]);
  if (type == 1) WAITV(0);
  FENCE();
  bgu(24, 28);
  WAITL0(); BAR();
  STAMP(3);
  if (type == 1) {
    const unsigned fk = WS_FKV + (cg * 16 + (lane & 15)) * 8;
    wait_flag(p.ws, fk, tag, ld_flag(p.ws, fk));
    u4 pv[4][8];
    unsigned off[4], da[4];
#pragma unroll
    for (int m = 0; m < 4; m++) {
      const int g = min(t + m * 256, 999), dd = g / 25, e5 = g - dd * 25, hg = dd >= 20, dq = dd - hg * 20;
      const int eg = (dq / 5) * 20 + (e5 < 20 ? e5 : (e5 - 20) * 4);
      off[m] = WS_KVP + (cg * 2 + hg) * 7168 + (dq * 21 + min(e5, 20)) * 16;
      da[m] = IMG + hg * 25600 + (dq >> 2) * 1024 + (dq & 1) * 512 + ((dq >> 1) & 1) * 8 + (eg >> 4) * 5120 + (eg & 15) * 16 + (e5 < 20 ? 0 : 256);
      asm volatile("" : "+v"(off[m]), "+v"(da[m]));
#pragma unroll
      for (int i = 0; i < 8; i++) asm volatile("" : "=v"(pv[m][i]));
    }
    const u4 rk = {(unsigned)(size_t)p.ws, (unsigned)((size_t)p.ws >> 32) & 0xffffu, WS_END, 0x00020000u};
    auto fetch = [&](unsigned set) {
#pragma unroll
      for (int i = 0; i < 8; i++)
        if (set >> i & 1)
          asm volatile("buffer_load_dwordx4 %0, %4, %8, %9 offen sc1\n\tbuffer_load_dwordx4 %1, %5, %8, %9 offen sc1\n\t"
                       "buffer_load_dwordx4 %2, %6, %8, %9 offen sc1\n\tbuffer_load_dwordx4 %3, %7, %8, %9 offen sc1"
                       : "+v"(pv[0][i]), "+v"(pv[1][i]), "+v"(pv[2][i]), "+v"(pv[3][i])
                       : "v"(off[0]), "v"(off[1]), "v"(off[2]), "v"(off[3]), "s"(rk), "s"(i * 8 * 7168) : "memory");
    };
    auto landed = [&]() {
      WAITV(0);
#pragma unroll
      for (int i = 0; i < 8; i++) asm volatile("" : "+v"(pv[0][i]), "+v"(pv[1][i]), "+v"(pv[2][i]), "+v"(pv[3][i]));
    };
    {
      const unsigned ho = WS_KVP + ((lane & 7) * 8 + cg * 2 + ((lane >> 3) & 1)) * 7168 + 419 * 16;
      unsigned got = 0;
#pragma unroll 1
      for (int it = 0; it < (1 << 18) && got != 255; it++) {
        const unsigned mk = (unsigned)__ballot(bld32<SC1>(rws, ho, 0) != EMPTY), now = mk & (mk >> 8) & 255 & ~got;
        fetch(now);
        got |= now;
      }
    }
    STAMP(4);
    f4 sum[4];
#pragma unroll 1
    for (int it = 0; it < (1 << 18); it++) {
      landed();
      bool nan = false;
#pragma unroll
      for (int m = 0; m < 4; m++) {
        sum[m] = __builtin_bit_cast(f4, pv[m][0]);
#pragma unroll
        for (int i = 1; i < 8; i++) sum[m] += __builtin_bit_cast(f4, pv[m][i]);
        nan = nan || __builtin_isunordered(sum[m][0], sum[m][1]) || __builtin_isunordered(sum[m][2], sum[m][3]);
      }
      if (!__any(nan)) break;
      unsigned mx = 0;
#pragma unroll
      for (int i = 0; i < 8; i++)
#pragma unroll
        for (int m = 0; m < 4; m++) mx = max(max(mx, max(pv[m][i][0], pv[m][i][1])), max(pv[m][i][2], pv[m][i][3]));
      if (!__any(mx == EMPTY)) break;
      fetch(255);
    }
    STAMP(5);
#pragma unroll
    for (int m = 0; m < 4; m++) *(LDSP u2*)(sm + da[m]) = u2{pk2(sum[m][0], sum[m][1]), pk2(sum[m][2], sum[m][3])};
    WAITL0(); BAR();
    BAR();
    return;
  }
  __builtin_amdgcn_s_setprio(0);
  if (gu) {
    BAR();
    breg(28, 38);
    WAITV(14);
    STAMP(4);
  } else {
    breg(12, 30);
    STAMP(4);
    BAR(); kv_publish(sm, rws, rt, cg); WAITL0();
    WAITV(18);
  }
  BAR();
  bput(0, 14);
  FENCE();
  bkv(30, 46); bgu(38, 46);
  WAITL0(); BAR();
  bput(14, 30);
  WAITL0(); BAR();
  bput(30, 46);
  WAITL0(); BAR();
  unsigned go[2]; u4 xv[2];
#pragma unroll
  for (int i = 0; i < 2; i++) {
    const int item = i * 256 + t;
    go[i] = (unsigned)((row2 + (item >> 3)) * 1280 + cg2 * 128 + (item & 7) * 16);
    xv[i] = *(const GLBP u4*)(p.x + go[i]);
  }
  BAR();
#pragma unroll
  for (int i = 0; i < 2; i++) p2_out(p, sm, i * 256 + t, xv[i], go[i]);
}

__global__ __launch_bounds__(512, 2) void fused_kernel(Params) {
  typedef unsigned long long ka_t __attribute__((ext_vector_type(8)));
  ka_t ka;
  asm volatile("s_load_dwordx16 %0, %1, 0x0" : "=s"(ka) : "s"(__builtin_amdgcn_kernarg_segment_ptr()));
  extern __shared__ __attribute__((aligned(16))) char smem[];
  LDSP char* sm = (LDSP char*)smem;
  const int lane = threadIdx.x & 63, w = __builtin_amdgcn_readfirstlane(threadIdx.x >> 6), b = blockIdx.x;
  u64 pc0 = __builtin_amdgcn_s_getpc();
  if (w < 2) {
    const u4 rc = {(unsigned)pc0, (unsigned)(pc0 >> 32) & 0xffffu, CODE_BYTES, 0x00020000u};
    asm volatile("s_mov_b32 m0, %0\n\ts_nop 0\n\tbuffer_load_dword %1, %2, 0 offen lds"
                 :: "s"((unsigned)(size_t)(sm + AUX + 3328)), "v"(min(threadIdx.x * 128 + ((blockIdx.x >> 3) & 3) * 16384, CODE_BYTES - 128)), "s"(rc) : "memory", "m0");
  }
  FENCE();
  int type, rt, cg, n2;
  {
    const int c = b & 7, j = b >> 3, a = c & 1, q = c >> 1;
    int lo = (j - 12) >> 31;
    asm volatile("" : "+s"(lo));
    const int n = j + ((a * 19 - 12) & ~lo), n3 = (n * 43) >> 7, m = n - n3 * 3, t1 = min(m, 1) & lo;
    type = ((t1 + 1) & lo) + ((((b | 6) + 2) >> 8) & ~a) * 3;
    rt = n3 + (4 * a & lo);
    cg = q * (3 + ((t1 - 2) & lo)) + m - t1;
    n2 = j - 4 + ((3 - n3) & lo);
    n2 = max(n2 + q * 55 + a * 27, 0);
  }
  int row0 = min(rt * (b < 96 ? 256 : 160), S - 160);
  const int rt2 = (n2 * 205) >> 11;
  int cg2 = n2 - rt2 * 10, row2 = min(rt2 * 96, S - 96);
  asm volatile("s_waitcnt lgkmcnt(0)" : "+s"(ka), "+s"(type), "+s"(rt), "+s"(cg), "+s"(row0), "+s"(row2), "+s"(cg2));
  const Params p = {(const char*)ka[0], (const char*)ka[1], (const char*)ka[2], (const char*)ka[3], (const char*)ka[4], (const char*)ka[5], (char*)ka[6], (char*)ka[7]};
  STAMPR(0); STAMP(1);
  const rsrc_t rws = mkrs(p.ws, WS_END);
  if (type == 3) {
    u2 xs[5];
    const u64 tag = row_load(p, xs, w & 3, lane) + 1;
    const int task = 128 + ((b >> 1) & 3) + w * 4;
    CS ck;
    cs_init(p, ck, task < 156 ? task : 234, lane);
#pragma unroll
    for (int i = 0; i < 32; i++) cs_load(ck, i);
    FENCE();
    if (w < 4) row_stats(xs, rws, w, lane, (unsigned)tag);
#pragma unroll
    for (int i = 0; i < 32; i++) cs_add(ck, i);
    if (task < 156) cs_store(ck, rws, lane, (unsigned)tag);
    STAMP(3);
    BAR();
    if (w != 0) return;
    if (lane == 0) st_flag(p.ws, WS_F0 + b * 8, tag);
    if (b != 248) return;
#pragma unroll 1
    for (int i = 0; i < 4; i++) wait_flag(p.ws, WS_F0 + (i * 64 + lane) * 8, tag, tag - 1);
    if (lane == 0) st_flag(p.ws, WS_GEN, tag);
    return;
  }
  if (w >= 4) {
    __builtin_amdgcn_s_setprio(3);
    if (type == 2) loader<2>(p, sm, rws, w - 4, lane, rt, cg, row0, row2, cg2);
    else if (type == 0) loader<0>(p, sm, rws, w - 4, lane, rt, cg, row0, row2, cg2);
    else loader<1>(p, sm, rws, w - 4, lane, rt, cg, row0, row2, cg2);
  } else {
    if (type == 0) k1_mfma<0>(p, sm, rws, w, lane, rt, cg, row0);
    else if (type == 1) k1_mfma<1>(p, sm, rws, w, lane, rt, cg, row0);
    else k1_mfma<2>(p, sm, rws, w, lane, rt, cg, row0);
    STAMP(7);
    if (type != 1) p2_mfma(p, sm, rws, w, lane, row2, cg2);
  }
  STAMP(11); STAMPR(2);
}

extern "C" void kernel_launch(void* const* d_in, const int* in_sizes, int n_in, void* d_out, int out_size,
                              void* d_ws, size_t ws_size, hipStream_t stream) {
  Params p;
  p.x = (const char*)d_in[0]; p.wqkv = (const char*)d_in[1]; p.wo = (const char*)d_in[2];
  p.wg = (const char*)d_in[4]; p.wu = (const char*)d_in[5]; p.wd = (const char*)d_in[6];
  p.out = (char*)d_out; p.ws = (char*)d_ws;
  (void)hipFuncSetAttribute((const void*)fused_kernel, hipFuncAttributeMaxDynamicSharedMemorySize, LDS_BYTES);
  fused_kernel<<<256, 512, LDS_BYTES, stream>>>(p);
}
```

```cpp
#include <hip/hip_runtime.h>
#include <hip/hip_bf16.h>
#include <stdint.h>

typedef __attribute__((ext_vector_type(8))) __bf16 bf8;
typedef __attribute__((ext_vector_type(2))) __bf16 bf2;
typedef __attribute__((ext_vector_type(4))) short s4;
typedef __attribute__((ext_vector_type(2))) float f2;
typedef __attribute__((ext_vector_type(4))) float f4;
typedef __attribute__((ext_vector_type(16))) float f16v;
typedef __attribute__((ext_vector_type(2))) unsigned u2;
typedef __attribute__((ext_vector_type(4))) unsigned u4;
typedef unsigned long long u64;
typedef __amdgpu_buffer_rsrc_t rsrc_t;
#define LDSP __attribute__((address_space(3)))
#define GLBP __attribute__((address_space(1)))
#define DI __device__ __forceinline__

constexpr int S = 2048, D = 640, F = 1536;
constexpr int NCOL = 4992;
constexpr unsigned WS_GEN = 0;
constexpr unsigned WS_F0 = 4096;
constexpr unsigned WS_FH = 8192;
constexpr unsigned WS_FKV = 12288;
constexpr unsigned WS_FO = 16384;
constexpr unsigned WS_STATS = 32768;
constexpr unsigned WS_CS = 65536;
constexpr unsigned WS_KVP = 327680;
constexpr unsigned WS_H = 1 << 20;
constexpr unsigned WS_O = WS_H + S * F * 2;
constexpr unsigned WS_END = WS_O + S * D * 2;

constexpr int SLOT = 52 * 1024;
constexpr int LDS_BYTES = 160 * 1024;
constexpr int AUX = 3 * SLOT;
constexpr int IMG = 54 * 1024;
constexpr int SC1 = 16;
constexpr unsigned CODE_BYTES = 63 * 1024;

DI rsrc_t mkrs(const void* b, unsigned n) { return __builtin_amdgcn_make_buffer_rsrc((void*)b, (short)0, (int)n, 0x00020000); }
DI void blds16(rsrc_t rs, unsigned voff, unsigned soff, LDSP char* l) {
  __builtin_amdgcn_raw_ptr_buffer_load_lds(rs, (LDSP void*)l, 16, voff, soff, 0, 0);
}
template <int AUXB> DI u4 bld128(rsrc_t rs, unsigned voff, unsigned soff) { return __builtin_amdgcn_raw_buffer_load_b128(rs, voff, soff, AUXB); }
template <int AUXB> DI u2 bld64(rsrc_t rs, unsigned voff, unsigned soff) { return __builtin_amdgcn_raw_buffer_load_b64(rs, voff, soff, AUXB); }
template <int AUXB> DI unsigned bld32(rsrc_t rs, unsigned voff, unsigned soff) { return __builtin_amdgcn_raw_buffer_load_b32(rs, voff, soff, AUXB); }
DI void bst128(u4 v, rsrc_t rs, unsigned voff, unsigned soff) { __builtin_amdgcn_raw_buffer_store_b128(v, rs, voff, soff, SC1); }
DI void bst64(u2 v, rsrc_t rs, unsigned voff, unsigned soff) { __builtin_amdgcn_raw_buffer_store_b64(v, rs, voff, soff, SC1); }
DI u4 lds_r128(LDSP char* p) { return *(LDSP u4*)p; }
DI u2 lds_tr(LDSP char* p) { return __builtin_bit_cast(u2, __builtin_amdgcn_ds_read_tr16_b64_v4i16((LDSP s4*)p)); }
DI f16v mfma32(u4 a, u4 b, f16v c) { return __builtin_amdgcn_mfma_f32_32x32x16_bf16(__builtin_bit_cast(bf8, a), __builtin_bit_cast(bf8, b), c, 0, 0, 0); }
DI unsigned pk2(float lo, float hi) { f2 v = {lo, hi}; return __builtin_bit_cast(unsigned, __builtin_convertvector(v, bf2)); }
DI float blo(unsigned v) { return __uint_as_float(v << 16); }
DI float bhi(unsigned v) { return __uint_as_float(v & 0xffff0000u); }
DI float exp2c(float u) { return __builtin_amdgcn_fmed3f(__builtin_amdgcn_exp2f(u), 0.f, 1.f); }
constexpr float LOG2E = 1.44269504088896f, LN2 = 0.693147180559945f;
constexpr float PHC = 1e-6f * LOG2E;
DI unsigned phi2(f2 u) {
  const f2 e = {exp2c(u[0]), exp2c(u[1])};
  const f2 v = f2{fmaxf(u[0], PHC), fmaxf(u[1], PHC)} * LN2 + e;
  return pk2(v[0], v[1]);
}
DI u64 ld_flag(const char* ws, unsigned off) { return __hip_atomic_load((const GLBP u64*)(ws + off), __ATOMIC_RELAXED, __HIP_MEMORY_SCOPE_AGENT); }
DI void st_flag(char* ws, unsigned off, u64 v) { __hip_atomic_store((GLBP u64*)(ws + off), v, __ATOMIC_RELAXED, __HIP_MEMORY_SCOPE_AGENT); }
#define BAR() __builtin_amdgcn_s_barrier()
#define WAITV(n) asm volatile("s_waitcnt vmcnt(" #n ")" ::: "memory")
#define WAITL0() asm volatile("s_waitcnt lgkmcnt(0)" ::: "memory")
#define SCHED0() __builtin_amdgcn_sched_barrier(0)
#define FENCE() do { asm volatile("" ::: "memory"); __builtin_amdgcn_sched_barrier(0); } while (0)

#define STAMP(i)
#define STAMPR(i)

struct Params {
  const char* x; const char* wqkv; const char* wo; const char* wg; const char* wu; const char* wd;
  char* out; char* ws;
};

DI void wait_flag(const char* ws, unsigned off, u64 tag, u64 first) {
  u64 v = first;
#pragma unroll 1
  for (int it = 0; it < (1 << 18); it++) {
    if (__all(v == tag)) break;
    __builtin_amdgcn_s_sleep(1);
    v = ld_flag(ws, off);
  }
  FENCE();
}
DI u64 lds_tag(LDSP char* sm) {
  const u2 t = *(LDSP u2*)(sm + AUX + 3072);
  return (u64)(unsigned)__builtin_amdgcn_readfirstlane(t[0]) | ((u64)(unsigned)__builtin_amdgcn_readfirstlane(t[1]) << 32);
}

template <int CTL> DI float dppadd(float v) { return v + __int_as_float(__builtin_amdgcn_update_dpp(0, __float_as_int(v), CTL, 0xf, 0xf, true)); }
DI float swap16add(float v) { const u2 t = __builtin_amdgcn_permlane16_swap(__float_as_uint(v), __float_as_uint(v), false, false); return __uint_as_float(t[0]) + __uint_as_float(t[1]); }
DI float sum32(float v) { return swap16add(dppadd<0x121>(dppadd<0x122>(dppadd<0x124>(dppadd<0x128>(v))))); }
constexpr unsigned EMPTY = 0xffffffffu;

DI u64 row_load(const Params& p, u2 (&xs)[5], int w, int lane) {
  const u64 gen = ld_flag(p.ws, WS_GEN);
  const rsrc_t rx = mkrs(p.x, S * D * 2);
  const unsigned xo = (unsigned)((blockIdx.x * 8 + w * 2 + (lane >> 5)) * 1280 + (lane & 31) * 8);
#pragma unroll
  for (int i = 0; i < 5; i++) xs[i] = bld64<0>(rx, xo + i * 256, 0);
  FENCE();
  return gen;
}
DI void row_stats(const u2 (&xs)[5], rsrc_t rws, int w, int lane, unsigned tag) {
  const int b = blockIdx.x;
  float s = 0;
#pragma unroll
  for (int i = 0; i < 5; i++) s += (blo(xs[i][0]) + bhi(xs[i][0])) + (blo(xs[i][1]) + bhi(xs[i][1]));
  s = sum32(s);
  const float mu = s * (1.f / 640.f);
  float q = 0;
#pragma unroll
  for (int i = 0; i < 5; i++) {
    const float a = blo(xs[i][0]) - mu, c = bhi(xs[i][0]) - mu, e = blo(xs[i][1]) - mu, g = bhi(xs[i][1]) - mu;
    q += a * a + c * c + e * e + g * g;
  }
  q = sum32(q);
  const float rs = rsqrtf(q * (1.f / 640.f) + 1e-5f);
  if ((lane & 31) == 0) bst128(u4{__float_as_uint(mu), tag, __float_as_uint(rs), tag}, rws, (b * 8 + w * 2 + (lane >> 5)) * 16, WS_STATS);
}
struct CS { rsrc_t rs; unsigned vo, step, dst; u4 d[32]; float c[8]; };
DI void cs_init(const Params& p, CS& k, int task, int lane) {
  const int kp = task / 78, cu = task - kp * 78;
  const char* base = cu < 30 ? p.wqkv + cu * 128 : (cu < 54 ? p.wg + (cu - 30) * 128 : p.wu + (cu - 54) * 128);
  const unsigned stride = cu < 30 ? 3840 : 3072;
  k.rs = mkrs(base, task < 234 ? 640 * stride - stride + 128 : 0);
  k.vo = (unsigned)(kp * 256 + (lane >> 3)) * stride + (lane & 7) * 16;
  k.step = 8 * stride;
  k.dst = (kp * NCOL + cu * 64 + (lane & 7) * 8 + (lane >> 5) * 4 + ((lane >> 3) & 1) * 2) * 8;
#pragma unroll
  for (int j = 0; j < 8; j++) k.c[j] = 0;
}
DI void cs_load(CS& k, int i) { k.d[i] = bld128<0>(k.rs, k.vo, i * k.step); }
DI void cs_add(CS& k, int i) {
#pragma unroll
  for (int j = 0; j < 4; j++) { k.c[2 * j] += blo(k.d[i][j]); k.c[2 * j + 1] += bhi(k.d[i][j]); }
}
DI void cs_store(CS& k, rsrc_t rws, int lane, unsigned tag) {
  float e[4];
#pragma unroll
  for (int j = 0; j < 4; j++) {
    const u2 t = __builtin_amdgcn_permlane32_swap(__float_as_uint(k.c[j]), __float_as_uint(k.c[4 + j]), false, false);
    e[j] = dppadd<0x128>(swap16add(__uint_as_float(t[0]) + __uint_as_float(t[1])));
  }
  if ((lane & 16) == 0) bst128(u4{__float_as_uint(lane & 8 ? e[2] : e[0]), tag, __float_as_uint(lane & 8 ? e[3] : e[1]), tag}, rws, k.dst, WS_CS);
}

template <int TYPE>
DI void k1_main(LDSP char* sm, int w, int lane, f16v (&acc)[10]) {
  constexpr int NXF = TYPE == 0 ? 5 : 2, NWF = 7 - NXF;
  constexpr int XB = TYPE == 0 ? 160 * 128 : 256 * 128;
  const int r = lane & 31, hh = lane >> 5, blk = (lane >> 4) & 1, q = (lane & 15) >> 2, pp = lane & 3;
  unsigned xa[4];
#pragma unroll
  for (int k = 0; k < 4; k++) xa[k] = (unsigned)(((TYPE == 0 ? 0 : w * 64) + r) * 128 + (((2 * k + hh) ^ ((r >> 1) & 7)) << 4));
  unsigned wa[2];
  if (TYPE == 0) {
#pragma unroll
    for (int ft = 0; ft < 2; ft++)
      wa[ft] = (unsigned)(XB + blk * 16384 + (8 * hh + q) * 256 + (((w * 4 + ft * 2 + (pp >> 1)) ^ (q << 2) ^ (blk << 1)) << 4) + (pp & 1) * 8);
  } else {
    wa[0] = (unsigned)(XB + (8 * hh + q) * 320 + blk * 32 + pp * 8); wa[1] = 0;
  }
  u4 xf[2][NXF], wf[2][NWF];
  auto rd = [&](int buf, LDSP char* base, int k) {
#pragma unroll
    for (int i = 0; i < NWF; i++) {
      u2 lo, hi;
      if (TYPE == 0) { lo = lds_tr(base + wa[i] + k * 4096); hi = lds_tr(base + wa[i] + k * 4096 + 1024); }
      else { lo = lds_tr(base + wa[0] + k * 5120 + i * 64); hi = lds_tr(base + wa[0] + k * 5120 + i * 64 + 1280); }
      wf[buf][i] = u4{lo[0], lo[1], hi[0], hi[1]};
    }
#pragma unroll
    for (int i = 0; i < NXF; i++) xf[buf][i] = lds_r128(base + xa[k] + i * 4096);
  };
  auto mm = [&](int buf) {
#pragma unroll
    for (int i = 0; i < NXF; i++)
#pragma unroll
      for (int j = 0; j < NWF; j++) {
        if (TYPE == 2) acc[i * NWF + j] = mfma32(xf[buf][i], wf[buf][j], acc[i * NWF + j]);
        else acc[i * NWF + j] = mfma32(wf[buf][j], xf[buf][i], acc[i * NWF + j]);
      }
  };
  auto inter = [&]() {
    constexpr int NR = NXF + 2 * NWF;
#pragma unroll
    for (int i = 0; i < 6; i++) {
      __builtin_amdgcn_sched_group_barrier(0x8, 1, 0);
      __builtin_amdgcn_sched_group_barrier(0x100, (NR + 5) / 6, 0);
    }
    __builtin_amdgcn_sched_group_barrier(0x8, 4, 0);
  };
  BAR();
  rd(0, sm, 0);
  int slot = 0;
#pragma unroll 1
  for (int s = 0; s < 9; s++) {
    LDSP char* base = sm + slot * SLOT;
    slot = slot == 2 ? 0 : slot + 1;
    SCHED0();
    rd(1, base, 1); mm(0); inter(); SCHED0();
    rd(0, base, 2); mm(1); inter(); SCHED0();
    rd(1, base, 3); mm(0); inter(); SCHED0();
    WAITL0(); BAR(); SCHED0();
    rd(0, sm + slot * SLOT, 0); mm(1); inter(); SCHED0();
  }
  SCHED0();
  rd(1, sm, 1); mm(0); inter(); SCHED0();
  rd(0, sm, 2); mm(1); inter(); SCHED0();
  rd(1, sm, 3); mm(0); inter(); SCHED0();
  mm(1); SCHED0();
}

DI void kv_publish(LDSP char* sm, rsrc_t rws, int rt, int cg) {
  const int t = threadIdx.x;
  if (t < 420) {
    LDSP char* src = sm + t * 16;
    const f4 s = (*(LDSP f4*)src + *(LDSP f4*)(src + 8192)) + (*(LDSP f4*)(src + 16384) + *(LDSP f4*)(src + 24576));
    u4 v = __builtin_bit_cast(u4, s);
#pragma unroll
    for (int j = 0; j < 4; j++) v[j] = min(v[j], EMPTY - 1);
    bst128(v, rws, t * 16, WS_KVP + (rt * 8 + cg) * 7168);
  }
}

template <int TYPE>
DI void k1_mfma(const Params& p, LDSP char* sm, rsrc_t rws, int w, int lane, int rt, int cg, int row0) {
  if (TYPE == 0 && (blockIdx.x >> 6) == 2) {
    const u64 a1 = (u64)(w == 0 ? p.wqkv : (w == 1 ? p.wg : (w == 2 ? p.wu : p.wd))), a2 = (u64)(w & 1 ? p.wo : p.x);
    const unsigned n1 = w == 0 ? D * 3840 : F * D * 2, n2 = w & 1 ? D * D * 2 : S * D * 2;
    const u4 r1 = {(unsigned)a1, (unsigned)(a1 >> 32) & 0xffffu, n1, 0x00020000u}, r2 = {(unsigned)a2, (unsigned)(a2 >> 32) & 0xffffu, n2, 0x00020000u};
    asm volatile("s_mov_b32 m0, %4\n\ts_nop 0\n\tbuffer_load_dword %0, %2, 0 offen lds\n\tbuffer_load_dword %1, %3, 0 offen lds"
                 :: "v"((lane * 8 + ((blockIdx.x >> 3) & 7)) * (n1 >> 9)), "v"((lane * 16 + (w >> 1) * 8 + ((blockIdx.x >> 3) & 7)) * (n2 >> 10)), "s"(r1), "s"(r2), "s"((unsigned)(size_t)(sm + AUX + 3328)) : "memory", "m0");
  }
  u2 xs[5];
  const u64 gen = row_load(p, xs, w, lane);
  const int hh = lane >> 5, r = lane & 31;
  f16v acc[10];
#pragma unroll
  for (int i = 0; i < 10; i++)
#pragma unroll
    for (int j = 0; j < 16; j++) acc[i][j] = 0.f;
#pragma unroll
  for (int i = 0; i < 10; i++) asm volatile("" : "+v"(acc[i]));
  FENCE();
  {
    const u64 tag = gen + 1;
    row_stats(xs, rws, w, lane, (unsigned)tag);
    if (threadIdx.x == 0) *(LDSP u2*)(sm + AUX + 3072) = u2{(unsigned)tag, (unsigned)(tag >> 32)};
    WAITL0();
    const u64 wa = (u64)p.ws, oa = (u64)p.out;
    const u4 r1 = {(unsigned)wa, (unsigned)(wa >> 32) & 0xffffu, WS_END, 0x00020000u}, r2 = {(unsigned)oa, (unsigned)(oa >> 32) & 0xffffu, S * D * 2, 0x00020000u};
    if (TYPE == 0 && (blockIdx.x >> 3) == 12)
    asm volatile("s_mov_b32 m0, %4\n\ts_nop 0\n\tbuffer_load_dword %0, %2, 0 offen sc1 lds\n\tbuffer_load_dword %1, %3, 0 offen sc1 lds"
                 :: "v"(threadIdx.x * 38912), "v"(threadIdx.x * 10240), "s"(r1), "s"(r2), "s"((unsigned)(size_t)(sm + AUX + 3328)) : "memory", "m0");
    STAMP(12);
  }
  k1_main<TYPE>(sm, w, lane, acc);
  STAMP(3);
  BAR(); FENCE();
  STAMP(19);
  if (TYPE == 0) {
    f4 cs[2][4];
#pragma unroll
    for (int ft = 0; ft < 2; ft++)
#pragma unroll
      for (int qd = 0; qd < 4; qd++)
        cs[ft][qd] = *(LDSP f4*)(sm + AUX + 2048 + ((qd >> 1) * 128 + w * 32 + ft * 16 + (qd & 1) * 8 + hh * 4) * 4);
    const unsigned dst = (unsigned)(((row0 >> 5) * 96 + cg * 8 + w * 2) * 1024 + lane * 16);
#pragma unroll
    for (int tt = 0; tt < 5; tt++) {
      const float mu = *(LDSP float*)(sm + AUX + (tt * 32 + r) * 4);
      if (tt == 0 && rt == 12) continue;
#pragma unroll
      for (int ft = 0; ft < 2; ft++) {
        const f16v a = acc[tt * 2 + ft];
        float h[8];
#pragma unroll
        for (int j = 0; j < 8; j++) h[j] = (a[j] - mu * cs[ft][j >> 2][j & 3]) * (a[8 + j] - mu * cs[ft][2 + (j >> 2)][j & 3]);
        bst128(u4{pk2(h[0], h[1]), pk2(h[2], h[3]), pk2(h[4], h[5]), pk2(h[6], h[7])}, rws, dst + ft * 1024, WS_H + tt * 98304);
      }
    }
    STAMP(4);
    FENCE(); BAR();
    WAITV(0); BAR();
    if (threadIdx.x == 0) st_flag(p.ws, WS_FH + (rt * 16 + cg) * 8, lds_tag(sm));
  } else if (TYPE == 1) {
    u4 pq[2][10];
#pragma unroll
    for (int tt = 0; tt < 2; tt++) {
      const float mu = *(LDSP float*)(sm + AUX + (w * 64 + tt * 32 + r) * 4);
      const float rl = *(LDSP float*)(sm + AUX + 1024 + (w * 64 + tt * 32 + r) * 4) * LOG2E;
#pragma unroll
      for (int ft = 0; ft < 5; ft++) {
        const f16v a = acc[tt * 5 + ft];
#pragma unroll
        for (int qd = 0; qd < 4; qd++) {
          const f4 cs = *(LDSP f4*)(sm + AUX + 2048 + (ft * 32 + qd * 8 + hh * 4) * 4);
#pragma unroll
          for (int j = 0; j < 2; j++) {
            const f2 u = (f2{a[qd * 4 + 2 * j], a[qd * 4 + 2 * j + 1]} - f2{cs[2 * j], cs[2 * j + 1]} * mu) * rl + PHC;
            pq[tt][ft * 2 + (qd >> 1)][(qd & 1) * 2 + j] = phi2(u);
          }
        }
        asm volatile("" : "+v"(pq[tt][ft * 2]), "+v"(pq[tt][ft * 2 + 1]));
      }
    }
    STAMP(4);
    SCHED0(); BAR(); SCHED0();
    STAMP(5);
    const unsigned dst = (unsigned)((((row0 >> 5) + w * 2) * 40 + cg * 10) * 1024 + lane * 16);
    f16v oa[2][2];
    u4 af[2][3];
    auto rdi = [&](int n) {
      const int hg = n / 5, T = n % 5, k0 = T < 2 ? 0 : T - 1, k1 = T == 0 ? 1 : (T == 4 ? 4 : T + 1);
#pragma unroll
      for (int k = k0; k <= k1; k++) af[n & 1][k - k0] = lds_r128(sm + IMG + (hg * 25 + T * 5 + k) * 1024 + lane * 16);
    };
    auto omm = [&](int n) {
      const int hg = n / 5, T = n % 5, k0 = T < 2 ? 0 : T - 1, k1 = T == 0 ? 1 : (T == 4 ? 4 : T + 1);
#pragma unroll
      for (int tt = 0; tt < 2; tt++)
#pragma unroll
        for (int j = 0; j < 16; j++) oa[n & 1][tt][j] = 0.f;
#pragma unroll
      for (int k = k0; k <= k1; k++)
#pragma unroll
        for (int tt = 0; tt < 2; tt++) oa[n & 1][tt] = mfma32(af[n & 1][k - k0], pq[tt][hg * 5 + k], oa[n & 1][tt]);
    };
    rdi(0); rdi(1); omm(0); SCHED0();
#pragma unroll
    for (int n = 0; n < 10; n++) {
      if (n + 1 < 10) omm(n + 1);
      if (n + 2 < 10) rdi(n + 2);
#pragma unroll
      for (int tt = 0; tt < 2; tt++) {
        const f16v a = oa[n & 1][tt];
        const float d0 = __builtin_amdgcn_rcpf(a[8] + 1e-6f), d1 = __builtin_amdgcn_rcpf(a[12] + 1e-6f);
        bst128(u4{pk2(a[0] * d0, a[1] * d0), pk2(a[2] * d0, a[3] * d0), pk2(a[4] * d1, a[5] * d1), pk2(a[6] * d1, a[7] * d1)},
               rws, dst + (n & 3) * 1024, WS_O + tt * 40960 + (n >> 2) * 4096);
      }
#pragma unroll
      for (int i = 0; i < 6; i++) { __builtin_amdgcn_sched_group_barrier(0x8, 1, 0); __builtin_amdgcn_sched_group_barrier(0x2, 5, 0); }
      SCHED0();
    }
    STAMP(6);
    WAITV(0); BAR();
    if (threadIdx.x == 0) st_flag(p.ws, WS_FO + (rt * 16 + cg) * 8, lds_tag(sm));
  } else {
    float cs[5];
#pragma unroll
    for (int ct = 0; ct < 5; ct++) cs[ct] = *(LDSP float*)(sm + AUX + 2048 + (ct * 32 + r) * 4);
    const bool isk = r < 16;
    const float kmul = isk ? 1.f : 0.f, kadd = isk ? 1e-6f : 0.f, klo = isk ? 1e-6f : -__builtin_inff();
    u4 fr[4][5];
#pragma unroll
    for (int g = 0; g < 4; g++) {
      f2 mu[4], rs[4], rl[4];
#pragma unroll
      for (int hq = 0; hq < 2; hq++) {
        LDSP char* sp = sm + AUX + (w * 64 + g * 16 + hq * 8 + hh * 4) * 4;
        const f4 a = *(LDSP f4*)sp, b = *(LDSP f4*)(sp + 1024);
        mu[hq * 2] = f2{a[0], a[1]}; mu[hq * 2 + 1] = f2{a[2], a[3]};
        rs[hq * 2] = f2{b[0], b[1]}; rs[hq * 2 + 1] = f2{b[2], b[3]};
      }
#pragma unroll
      for (int j = 0; j < 4; j++) rl[j] = rs[j] * LOG2E;
#pragma unroll
      for (int ct = 0; ct < 5; ct++) {
#pragma unroll
        for (int j = 0; j < 4; j++) {
          const f16v& a = acc[(g >> 1) * 5 + ct];
          const f2 y = f2{a[(g & 1) * 8 + 2 * j], a[(g & 1) * 8 + 2 * j + 1]} - mu[j] * cs[ct];
          if (ct < 2) fr[g][ct][j] = phi2(y * rl[j] + PHC);
          else if (ct == 2) {
            const f2 u = y * rl[j], t = y * rs[j] + kadd, e = {exp2c(u[0]), exp2c(u[1])};
            const f2 v = e * kmul + f2{fmaxf(t[0], klo), fmaxf(t[1], klo)};
            fr[g][ct][j] = pk2(v[0], v[1]);
          } else { const f2 v = y * rs[j]; fr[g][ct][j] = pk2(v[0], v[1]); }
        }
      }
      SCHED0();
    }
    f16v z[8];
#pragma unroll
    for (int i = 0; i < 8; i++)
#pragma unroll
      for (int j = 0; j < 16; j++) z[i][j] = 0.f;
#pragma unroll
    for (int g = 0; g < 4; g++) {
      u4 mb;
#pragma unroll
      for (int j = 0; j < 4; j++) mb[j] = isk ? 0x3f803f80u : fr[g][2][j];
      z[0] = mfma32(fr[g][0], fr[g][3], z[0]);
      z[1] = mfma32(fr[g][0], fr[g][4], z[1]);
      z[2] = mfma32(fr[g][0], mb, z[2]);
      z[3] = mfma32(fr[g][1], fr[g][3], z[3]);
      z[4] = mfma32(fr[g][1], fr[g][4], z[4]);
      z[5] = mfma32(fr[g][1], mb, z[5]);
      z[6] = mfma32(fr[g][2], fr[g][4], z[6]);
      z[7] = mfma32(fr[g][2], mb, z[7]);
    }
    unsigned pa[3], pm[3];
#pragma unroll
    for (int vt = 0; vt < 3; vt++) {
      const int e = vt == 2 ? 48 + r : vt * 32 + r, he = e / 20;
      int ee = e - he * 20; unsigned m = 31u << (5 * he);
      if (vt == 2 && isk) { ee = 20; m = r == 0 ? 0xfffffu : 0u; }
      pa[vt] = (unsigned)(w * 8192 + ee * 16 + hh * 336); pm[vt] = m >> hh;
    }
    SCHED0();
#pragma unroll
    for (int i = 0; i < 8; i++)
#pragma unroll
      for (int qd = 0; qd < 4; qd++) {
        const int kt = i / 3, vt = i < 6 ? i % 3 : i - 5, c = kt * 8 + qd * 2;
        const bool need = (i == 1) ? qd >= 2 : (i == 3) ? qd == 0 : (i >= 6) ? qd < 2 : true;
        if (need && (pm[vt] & (1u << c)))
          *(LDSP f4*)(sm + pa[vt] + c * 336) = f4{z[i][qd * 4], z[i][qd * 4 + 1], z[i][qd * 4 + 2], z[i][qd * 4 + 3]};
      }
    STAMP(4);
    WAITL0(); BAR();
    kv_publish(sm, rws, rt, cg);
    WAITL0(); BAR();
  }
}

DI void p2_out(const Params& p, LDSP char* sm, int item, u4 xv, unsigned go) {
  const int row = item >> 3, c = item & 7;
  LDSP char* src = sm + (row >> 3) * 8192 + (row & 7) * 256;
  const unsigned c0 = ((2 * c) ^ (row & 7)) << 4, c1 = c0 ^ 16;
  f4 a = *(LDSP f4*)(src + c0), b = *(LDSP f4*)(src + c1);
#pragma unroll
  for (int i = 1; i < 4; i++) { a += *(LDSP f4*)(src + i * 2048 + c0); b += *(LDSP f4*)(src + i * 2048 + c1); }
  bst128(u4{pk2(blo(xv[0]) + a[0], bhi(xv[0]) + a[1]), pk2(blo(xv[1]) + a[2], bhi(xv[1]) + a[3]),
            pk2(blo(xv[2]) + b[0], bhi(xv[2]) + b[1]), pk2(blo(xv[3]) + b[2], bhi(xv[3]) + b[3])}, mkrs(p.out, S * D * 2), go, 0);
}

constexpr int PD = 8;
DI void p2_mfma(const Params& p, LDSP char* sm, rsrc_t rws, int w, int lane, int row2, int cg2) {
  const int hh = lane >> 5, r = lane & 31, blk = (lane >> 4) & 1, q = (lane & 15) >> 2, pp = lane & 3;
  const u64 tag = lds_tag(sm);
  const unsigned fh = WS_FH + (((row2 + ((lane >> 2) & 1) * 95) / 160) * 16 + 3 * w + min(lane & 3, 2)) * 8;
  const unsigned fo = WS_FO + (((row2 + (lane & 1) * 95) >> 8) * 16 + w) * 8;
  float r2[3];
  wait_flag(p.ws, fh, tag, ld_flag(p.ws, fh));
  STAMP(8);
  const unsigned ah = (unsigned)(((row2 >> 5) * 96 + w * 24) * 1024 + lane * 16), ao = (unsigned)(((row2 >> 5) * 40 + w * 10) * 1024 + lane * 16);
  u4 af[PD][3], bf[2][2];
  auto lda = [&](int i) {
#pragma unroll
    for (int tt = 0; tt < 3; tt++) {
      if (i < 24) af[i % PD][tt] = bld128<SC1>(rws, ah + (i & 3) * 1024, WS_H + tt * 98304 + (i >> 2) * 4096);
      else af[i % PD][tt] = bld128<SC1>(rws, ao + (i & 3) * 1024, WS_O + tt * 40960 + ((i - 24) >> 2) * 4096);
    }
  };
  LDSP char* ba[2][3];
#pragma unroll
  for (int ct = 0; ct < 2; ct++)
#pragma unroll
    for (int sg = 0; sg < 3; sg++) {
      ba[ct][sg] = sm + sg * 49152 + w * 2048 + (4 * hh + q) * 128 + (((ct * 4 + blk * 2 + (pp >> 1)) ^ ((q >> 1) << 2)) << 4) + (pp & 1) * 8;
      asm volatile("" : "+v"(ba[ct][sg]));
    }
  auto ldb = [&](int i) {
    const int sl = 17 - (i < 18 ? i : i - 18);
#pragma unroll
    for (int ct = 0; ct < 2; ct++) {
      LDSP char* base = ba[ct][sl / 6] + (sl % 6) * 8192;
      const u2 lo = lds_tr(base), hi = lds_tr(base + 1024);
      bf[i & 1][ct] = u4{lo[0], lo[1], hi[0], hi[1]};
    }
  };
  f16v acc[6];
#pragma unroll
  for (int i = 0; i < 6; i++)
#pragma unroll
    for (int j = 0; j < 16; j++) acc[i][j] = 0.f;
#pragma unroll
  for (int i = 0; i < PD; i++) lda(i);
  ldb(0);
  u64 ofl = 0;
  auto step = [&](int i, bool pre) {
    SCHED0();
    if (i == 7 || i == 15 || i == 23) { STAMP(12 + i / 4); WAITL0(); BAR(); STAMP(13 + i / 4); SCHED0(); }
    if (i + 1 < 34) ldb(i + 1);
#pragma unroll
    for (int tt = 0; tt < 3; tt++)
#pragma unroll
      for (int ct = 0; ct < 2; ct++) acc[tt * 2 + ct] = mfma32(bf[i & 1][ct], af[i % PD][tt], acc[tt * 2 + ct]);
    SCHED0();
    if (pre) lda(i + PD);
    if (i == 23) {
#pragma unroll
      for (int tt = 0; tt < 3; tt++) { acc[tt * 2] *= r2[tt] * r2[tt]; acc[tt * 2 + 1] *= r2[tt] * r2[tt]; }
#pragma unroll
      for (int k = 0; k < 6; k++) asm volatile("" : "+v"(acc[k]));
      SCHED0();
    }
  };
  const int item = 512 + threadIdx.x;
  const unsigned go = (unsigned)((row2 + (item >> 3)) * 1280 + cg2 * 128 + (item & 7) * 16);
  u4 xv;
  constexpr int OCHK = 20;
#pragma unroll
  for (int i = 0; i < OCHK; i++) {
    step(i, i + PD < 24);
    if (i + PD == 24) {
#pragma unroll
      for (int tt = 0; tt < 3; tt++) r2[tt] = __uint_as_float(bld32<SC1>(rws, (row2 + tt * 32 + r) * 16 + 8, WS_STATS));
      xv = bld128<0>(mkrs(p.x, S * D * 2), go, 0);
      ofl = ld_flag(p.ws, fo);
    }
  }
  STAMP(5);
  const bool early = __all(ofl == tag);
  asm volatile("" : "+v"(xv));
  FENCE();
  if (early) {
#pragma unroll
    for (int j = 24; j < OCHK + PD; j++) lda(j);
  }
#pragma unroll
  for (int i = OCHK; i < 24; i++) step(i, false);
  if (!early) {
    wait_flag(p.ws, fo, tag, ld_flag(p.ws, fo));
#pragma unroll
    for (int j = 24; j < OCHK + PD; j++) lda(j);
  }
  STAMP(9);
#pragma unroll
  for (int j = OCHK + PD; j < 24 + PD; j++) lda(j);
#pragma unroll
  for (int i = 24; i < 34; i++) step(i, i + PD < 34);
  STAMP(10);
#pragma unroll
  for (int tt = 0; tt < 3; tt++)
#pragma unroll
    for (int ct = 0; ct < 2; ct++)
#pragma unroll
      for (int qd = 0; qd < 4; qd++)
        *(LDSP f4*)(sm + (tt * 4 + (r >> 3)) * 8192 + w * 2048 + (r & 7) * 256 + (((ct * 8 + qd * 2 + hh) ^ (r & 7)) << 4)) =
            f4{acc[tt * 2 + ct][qd * 4], acc[tt * 2 + ct][qd * 4 + 1], acc[tt * 2 + ct][qd * 4 + 2], acc[tt * 2 + ct][qd * 4 + 3]};
  WAITL0(); BAR();
  p2_out(p, sm, item, xv, go);
}

template <int type>
DI void loader(const Params& p, LDSP char* sm, rsrc_t rws, int lw, int lane, int rt, int cg, int row0, int row2, int cg2) {
  const int b = blockIdx.x, t = lw * 64 + lane;
  constexpr bool gu = type == 0;
  unsigned vo[13];
  {
    const int x0 = gu ? lw * 5 : lw * 8;
    const unsigned xr = (unsigned)((row0 + x0 * 8 + (lane >> 3)) * 1280), xs = (unsigned)(((lane & 7) ^ (lane >> 4) ^ ((x0 & 1) << 2)) << 4);
#pragma unroll
    for (int i = 0; i < 8; i++) vo[i] = xr + (xs ^ ((i & 1) << 6)) + i * 10240;
    if (gu) {
      const unsigned g0 = (unsigned)(((lw & 1) * 32 + (lane >> 4)) * 3072 + (((lane & 15) ^ ((lane >> 4) << 2) ^ (lw & 2)) << 4));
#pragma unroll
      for (int i = 5; i < 13; i++) vo[i] = g0 + (i - 5) * 12288;
    } else {
#pragma unroll
      for (int i = 0; i < 5; i++) {
        const int e = i * 64 + lane, rq = (e * 205) >> 12, pos = e - rq * 20;
        const int adj = type == 1 ? 0 : (pos < 10 ? 0 : (pos < 12 ? 1248 : 1088));
        vo[8 + i] = (unsigned)((lw * 16 + rq) * 3840 + pos * 16 + adj);
      }
    }
  }
  const unsigned wofs = gu ? cg * 256 : (type == 1 ? cg * 320 : 1280 + cg * 160);
  const char* wbase = (gu ? ((lw >> 1) ? p.wu : p.wg) : p.wqkv) + wofs;
  const unsigned wstep = gu ? 64 * 3072 : 64 * 3840, wlen = wstep * 10 - wofs, mstep = gu ? wstep : 128;
  const rsrc_t rx = mkrs(p.x, S * D * 2), rw = mkrs(wbase, wlen), rm = mkrs(gu ? wbase : p.x, gu ? wlen : S * D * 2);
  const int dx = gu ? lw * 5120 : lw * 8192, dm = gu ? 20480 + lw * 8192 : dx + 5120, dw = gu ? dm + 3072 : 32768 + lw * 5120;
  CS ck;
  const int task = type == 1 ? (rt * 4 + cg) * 4 + lw : min(156 + lw * 64 + cg * 8 + rt, 234);
  const bool act = !gu && task < 234;
  auto issue = [&](int s, int a0 = 0, int a1 = 0) {
    LDSP char* d = sm + (s % 3) * SLOT;
#pragma unroll
    for (int i = 0; i < 13; i++) {
      if (i < 5) blds16(rx, vo[i], s * 128, d + dx + i * 1024);
      else if (i < 8) blds16(rm, vo[i], s * mstep, d + dm + (i - 5) * 1024);
      else blds16(rw, vo[i], s * wstep, d + dw + (i - 8) * 1024);
      if (i >= 1 && a0 + i - 1 < a1 && (type == 1 || act)) {
        FENCE(); cs_add(ck, a0 + i - 1);
        asm volatile("" : "+v"(ck.c[0]), "+v"(ck.c[1]), "+v"(ck.c[2]), "+v"(ck.c[3]), "+v"(ck.c[4]), "+v"(ck.c[5]), "+v"(ck.c[6]), "+v"(ck.c[7]));
        FENCE();
      }
    }
    FENCE();
  };
  auto csl = [&](int i0, int i1) {
    if (type == 1 || act) {
#pragma unroll
      for (int i = i0; i < i1; i++) cs_load(ck, i);
      FENCE();
    }
  };
  issue(0); issue(1);
  if (!gu) cs_init(p, ck, task, lane);
  constexpr bool q1 = type == 1;
  STAMP(12);
  WAITV(13); BAR(); STAMP(13);
  if (type == 2) {
    bst128(u4{EMPTY, EMPTY, EMPTY, EMPTY}, rws, t * 16, WS_KVP + (rt * 8 + cg) * 7168);
    bst128(u4{EMPTY, EMPTY, EMPTY, EMPTY}, rws, min(t + 256, 419) * 16, WS_KVP + (rt * 8 + cg) * 7168);
    FENCE();
  }
  issue(2);
  const rsrc_t rd_ = mkrs(p.wd, type == 1 ? 0 : F * D * 2), ro_ = mkrs(p.wo, type == 1 ? 0 : D * D * 2);
  const unsigned bo = (unsigned)((lane >> 3) * 1280 + cg2 * 128 + (((lane & 7) ^ (((lane >> 4) & 1) << 2)) << 4));
  auto bdma = [&](int i) {
#pragma unroll
    for (int h = 0; h < 2; h++) blds16(rd_, bo, (lw * 384 + i * 16 + h * 8) * 1280, sm + (17 - i) * 8192 + lw * 2048 + h * 1024);
  };
  u4 br[46];
  auto breg = [&](int i0, int i1) {
#pragma unroll
    for (int i = i0; i < i1; i++) {
      const int st = 11 + (i >> 1);
      br[i] = st < 24 ? bld128<0>(rd_, bo, (lw * 384 + st * 16 + (i & 1) * 8) * 1280) : bld128<0>(ro_, bo, (lw * 160 + (st - 24) * 16 + (i & 1) * 8) * 1280);
    }
    FENCE();
  };
  auto bput = [&](int i0, int i1) {
#pragma unroll
    for (int i = i0; i < i1; i++) {
      const int st = 11 + (i >> 1), sl = st < 18 ? st : st - 18;
      *(LDSP u4*)(sm + (17 - sl) * 8192 + lw * 2048 + (i & 1) * 1024 + lane * 16) = br[i];
    }
  };
  auto bgu = [&](int i0, int i1) { if (gu) breg(i0, i1); };
  auto bkv = [&](int i0, int i1) { if (type == 2) breg(i0, i1); };
#define WAITG(a, b, c) do { if (gu) WAITV(a); else if (type == 1) WAITV(b); else WAITV(c); } while (0)
#define WAITA(a, b, c, d) do { if (type == 2 && act) WAITV(d); else WAITG(a, b, c); } while (0)
  csl(0, q1 ? 11 : 8); WAITA(13, 24, 15, 23); BAR(); STAMP(14);
  issue(3); csl(q1 ? 11 : 8, q1 ? 22 : 16); WAITA(13, 35, 13, 29); BAR(); STAMP(19);
  const u64 tag = lds_tag(sm);
  if (t < (type == 2 ? 2 : 1)) st_flag(p.ws, t ? WS_FKV + (cg * 8 + rt) * 8 : WS_F0 + b * 8, tag);
  FENCE();
  issue(4, 0, q1 ? 11 : 0); bgu(0, 4); csl(22, q1 ? 32 : 0); WAITA(17, 34, 13, 21); BAR(); STAMP(15);
  issue(5, q1 ? 11 : 0, q1 ? 22 : 8); bgu(4, 8); WAITG(21, 23, 13); BAR(); STAMP(16);
  issue(6, q1 ? 22 : 8, q1 ? 32 : 16); bgu(8, 12);
  if (type == 1 || act) cs_store(ck, rws, lane, (unsigned)tag);
  WAITA(21, 14, 13, 14); BAR(); STAMP(6);
  issue(7); bgu(12, 16); bkv(0, 4);
  unsigned sto = min(row0 + t, S - 1) * 16, cso;
  {
    const int tc = gu ? t : min(t, 159);
    cso = 8 * (gu ? 1920 + (tc >> 7) * 1536 + cg * 128 + (tc & 127)
                  : (type == 1 ? cg * 160 + tc : cg * 80 + tc + (tc < 80 ? 640 : (tc < 96 ? 1264 : 1184))));
  }
  asm volatile("" : "+v"(sto), "+v"(cso));
  WAITA(21, 14, 17, 18); BAR(); STAMP(7);
  issue(8); bgu(16, 20); bkv(4, 8); WAITG(21, 13, 21); BAR(); STAMP(17);
  issue(9); bgu(20, 24); bkv(8, 12); WAITG(21, 13, 21); BAR(); STAMP(18);
  u4 stv; u2 csv[3];
  auto aux = [&]() {
    stv = bld128<SC1>(rws, sto, WS_STATS);
#pragma unroll
    for (int i = 0; i < 3; i++) csv[i] = bld64<SC1>(rws, cso, WS_CS + i * NCOL * 8);
    FENCE();
  };
  aux();
#pragma unroll
  for (int i = 5; i < 11; i++) bdma(i);
  FENCE();
  if (type == 1) *(LDSP u2*)(sm + IMG + t * 8) = u2{0, 0};
  WAITG(20, 16, 20); BAR();
  if (type != 1) {
#pragma unroll
    for (int i = 0; i < 5; i++) bdma(i);
    FENCE();
  }
  auto fresh = [&]() {
    const unsigned tg = (unsigned)tag;
    return __all(stv[1] == tg && stv[3] == tg && csv[0][1] == tg && csv[1][1] == tg && csv[2][1] == tg);
  };
  if (!fresh()) {
#pragma unroll 1
    for (int it = 0; it < (1 << 18); it++) { aux(); if (fresh()) break; }
  }
  *(LDSP unsigned*)(sm + AUX + t * 4) = stv[0];
  *(LDSP unsigned*)(sm + AUX + 1024 + t * 4) = stv[2];
  *(LDSP float*)(sm + AUX + 2048 + t * 4) = (__uint_as_float(csv[0][0]) + __uint_as_float(csv[1][0])) + __uint_as_float(csv[2][0]);
  if (type == 1) WAITV(0);
  FENCE();
  bgu(24, 28);
  WAITL0(); BAR();
  STAMP(3);
  if (type == 1) {
    const unsigned fk = WS_FKV + (cg * 16 + (lane & 15)) * 8;
    wait_flag(p.ws, fk, tag, ld_flag(p.ws, fk));
    u4 pv[4][8];
    unsigned off[4], da[4];
#pragma unroll
    for (int m = 0; m < 4; m++) {
      const int g = min(t + m * 256, 999), dd = g / 25, e5 = g - dd * 25, hg = dd >= 20, dq = dd - hg * 20;
      const int eg = (dq / 5) * 20 + (e5 < 20 ? e5 : (e5 - 20) * 4);
      off[m] = WS_KVP + (cg * 2 + hg) * 7168 + (dq * 21 + min(e5, 20)) * 16;
      da[m] = IMG + hg * 25600 + (dq >> 2) * 1024 + (dq & 1) * 512 + ((dq >> 1) & 1) * 8 + (eg >> 4) * 5120 + (eg & 15) * 16 + (e5 < 20 ? 0 : 256);
      asm volatile("" : "+v"(off[m]), "+v"(da[m]));
#pragma unroll
      for (int i = 0; i < 8; i++) asm volatile("" : "=v"(pv[m][i]));
    }
    const u4 rk = {(unsigned)(size_t)p.ws, (unsigned)((size_t)p.ws >> 32) & 0xffffu, WS_END, 0x00020000u};
    auto fetch = [&](unsigned set) {
#pragma unroll
      for (int i = 0; i < 8; i++)
        if (set >> i & 1)
          asm volatile("buffer_load_dwordx4 %0, %4, %8, %9 offen sc1\n\tbuffer_load_dwordx4 %1, %5, %8, %9 offen sc1\n\t"
                       "buffer_load_dwordx4 %2, %6, %8, %9 offen sc1\n\tbuffer_load_dwordx4 %3, %7, %8, %9 offen sc1"
                       : "+v"(pv[0][i]), "+v"(pv[1][i]), "+v"(pv[2][i]), "+v"(pv[3][i])
                       : "v"(off[0]), "v"(off[1]), "v"(off[2]), "v"(off[3]), "s"(rk), "s"(i * 8 * 7168) : "memory");
    };
    auto landed = [&]() {
      WAITV(0);
#pragma unroll
      for (int i = 0; i < 8; i++) asm volatile("" : "+v"(pv[0][i]), "+v"(pv[1][i]), "+v"(pv[2][i]), "+v"(pv[3][i]));
    };
    {
      const unsigned ho = WS_KVP + ((lane & 7) * 8 + cg * 2 + ((lane >> 3) & 1)) * 7168 + 419 * 16;
      unsigned got = 0;
#pragma unroll 1
      for (int it = 0; it < (1 << 18) && got != 255; it++) {
        const unsigned mk = (unsigned)__ballot(bld32<SC1>(rws, ho, 0) != EMPTY), now = mk & (mk >> 8) & 255 & ~got;
        fetch(now);
        got |= now;
      }
    }
    STAMP(4);
    f4 sum[4];
#pragma unroll 1
    for (int it = 0; it < (1 << 18); it++) {
      landed();
      bool nan = false;
#pragma unroll
      for (int m = 0; m < 4; m++) {
        sum[m] = __builtin_bit_cast(f4, pv[m][0]);
#pragma unroll
        for (int i = 1; i < 8; i++) sum[m] += __builtin_bit_cast(f4, pv[m][i]);
        nan = nan || __builtin_isunordered(sum[m][0], sum[m][1]) || __builtin_isunordered(sum[m][2], sum[m][3]);
      }
      if (!__any(nan)) break;
      unsigned mx = 0;
#pragma unroll
      for (int i = 0; i < 8; i++)
#pragma unroll
        for (int m = 0; m < 4; m++) mx = max(max(mx, max(pv[m][i][0], pv[m][i][1])), max(pv[m][i][2], pv[m][i][3]));
      if (!__any(mx == EMPTY)) break;
      fetch(255);
    }
    STAMP(5);
#pragma unroll
    for (int m = 0; m < 4; m++) *(LDSP u2*)(sm + da[m]) = u2{pk2(sum[m][0], sum[m][1]), pk2(sum[m][2], sum[m][3])};
    WAITL0(); BAR();
    BAR();
    return;
  }
  __builtin_amdgcn_s_setprio(0);
  if (gu) {
    BAR();
    breg(28, 38);
    WAITV(14);
    STAMP(4);
  } else {
    breg(12, 30);
    STAMP(4);
    BAR(); kv_publish(sm, rws, rt, cg); WAITL0();
    WAITV(18);
  }
  BAR();
  bput(0, 14);
  FENCE();
  bkv(30, 46); bgu(38, 46);
  WAITL0(); BAR();
  bput(14, 30);
  WAITL0(); BAR();
  bput(30, 46);
  WAITL0(); BAR();
  unsigned go[2]; u4 xv[2];
#pragma unroll
  for (int i = 0; i < 2; i++) {
    const int item = i * 256 + t;
    go[i] = (unsigned)((row2 + (item >> 3)) * 1280 + cg2 * 128 + (item & 7) * 16);
    xv[i] = *(const GLBP u4*)(p.x + go[i]);
  }
  BAR();
#pragma unroll
  for (int i = 0; i < 2; i++) p2_out(p, sm, i * 256 + t, xv[i], go[i]);
}

__global__ __launch_bounds__(512, 2) void fused_kernel(Params) {
  typedef unsigned long long ka_t __attribute__((ext_vector_type(8)));
  ka_t ka;
  asm volatile("s_load_dwordx16 %0, %1, 0x0" : "=s"(ka) : "s"(__builtin_amdgcn_kernarg_segment_ptr()));
  extern __shared__ __attribute__((aligned(16))) char smem[];
  LDSP char* sm = (LDSP char*)smem;
  const int lane = threadIdx.x & 63, w = __builtin_amdgcn_readfirstlane(threadIdx.x >> 6), b = blockIdx.x;
  u64 pc0 = __builtin_amdgcn_s_getpc();
  if (w < 2) {
    const u4 rc = {(unsigned)pc0, (unsigned)(pc0 >> 32) & 0xffffu, CODE_BYTES, 0x00020000u};
    asm volatile("s_mov_b32 m0, %0\n\ts_nop 0\n\tbuffer_load_dword %1, %2, 0 offen lds"
                 :: "s"((unsigned)(size_t)(sm + AUX + 3328)), "v"(min(threadIdx.x * 128 + ((blockIdx.x >> 3) & 3) * 16384, CODE_BYTES - 128)), "s"(rc) : "memory", "m0");
  }
  FENCE();
  int type, rt, cg, n2;
  {
    const int c = b & 7, j = b >> 3, a = c & 1, q = c >> 1;
    int lo = (j - 12) >> 31;
    asm volatile("" : "+s"(lo));
    const int n = j + ((a * 19 - 12) & ~lo), n3 = (n * 43) >> 7, m = n - n3 * 3, t1 = min(m, 1) & lo;
    type = ((t1 + 1) & lo) + ((((b | 6) + 2) >> 8) & ~a) * 3;
    rt = n3 + (4 * a & lo);
    cg = q * (3 + ((t1 - 2) & lo)) + m - t1;
    n2 = j - 4 + ((3 - n3) & lo);
    n2 = max(n2 + q * 55 + a * 27, 0);
  }
  int row0 = min(rt * (b < 96 ? 256 : 160), S - 160);
  const int rt2 = (n2 * 205) >> 11;
  int cg2 = n2 - rt2 * 10, row2 = min(rt2 * 96, S - 96);
  asm volatile("s_waitcnt lgkmcnt(0)" : "+s"(ka), "+s"(type), "+s"(rt), "+s"(cg), "+s"(row0), "+s"(row2), "+s"(cg2));
  const Params p = {(const char*)ka[0], (const char*)ka[1], (const char*)ka[2], (const char*)ka[3], (const char*)ka[4], (const char*)ka[5], (char*)ka[6], (char*)ka[7]};
  STAMPR(0); STAMP(1);
  const rsrc_t rws = mkrs(p.ws, WS_END);
  if (type == 3) {
    u2 xs[5];
    const u64 tag = row_load(p, xs, w & 3, lane) + 1;
    const int task = 128 + ((b >> 1) & 3) + w * 4;
    CS ck;
    cs_init(p, ck, task < 156 ? task : 234, lane);
#pragma unroll
    for (int i = 0; i < 32; i++) cs_load(ck, i);
    FENCE();
    if (w < 4) row_stats(xs, rws, w, lane, (unsigned)tag);
#pragma unroll
    for (int i = 0; i < 32; i++) cs_add(ck, i);
    if (task < 156) cs_store(ck, rws, lane, (unsigned)tag);
    STAMP(3);
    BAR();
    if (w != 0) return;
    if (lane == 0) st_flag(p.ws, WS_F0 + b * 8, tag);
    if (b != 248) return;
#pragma unroll 1
    for (int i = 0; i < 4; i++) wait_flag(p.ws, WS_F0 + (i * 64 + lane) * 8, tag, tag - 1);
    if (lane == 0) st_flag(p.ws, WS_GEN, tag);
    return;
  }
  if (w >= 4) {
    __builtin_amdgcn_s_setprio(3);
    if (type == 2) loader<2>(p, sm, rws, w - 4, lane, rt, cg, row0, row2, cg2);
    else if (type == 0) loader<0>(p, sm, rws, w - 4, lane, rt, cg, row0, row2, cg2);
    else loader<1>(p, sm, rws, w - 4, lane, rt, cg, row0, row2, cg2);
  } else {
    if (type == 0) k1_mfma<0>(p, sm, rws, w, lane, rt, cg, row0);
    else if (type == 1) k1_mfma<1>(p, sm, rws, w, lane, rt, cg, row0);
    else k1_mfma<2>(p, sm, rws, w, lane, rt, cg, row0);
    STAMP(7);
    if (type != 1) p2_mfma(p, sm, rws, w, lane, row2, cg2);
  }
  STAMP(11); STAMPR(2);
}

extern "C" void kernel_launch(void* const* d_in, const int* in_sizes, int n_in, void* d_out, int out_size,
                              void* d_ws, size_t ws_size, hipStream_t stream) {
  Params p;
  p.x = (const char*)d_in[0]; p.wqkv = (const char*)d_in[1]; p.wo = (const char*)d_in[2];
  p.wg = (const char*)d_in[4]; p.wu = (const char*)d_in[5]; p.wd = (const char*)d_in[6];
  p.out = (char*)d_out; p.ws = (char*)d_ws;
  (void)hipFuncSetAttribute((const void*)fused_kernel, hipFuncAttributeMaxDynamicSharedMemorySize, LDS_BYTES);
  fused_kernel<<<256, 512, LDS_BYTES, stream>>>(p);
}
```

```cpp
#include <hip/hip_runtime.h>
#include <hip/hip_bf16.h>
#include <stdint.h>

typedef __attribute__((ext_vector_type(8))) __bf16 bf8;
typedef __attribute__((ext_vector_type(2))) __bf16 bf2;
typedef __attribute__((ext_vector_type(4))) short s4;
typedef __attribute__((ext_vector_type(2))) float f2;
typedef __attribute__((ext_vector_type(4))) float f4;
typedef __attribute__((ext_vector_type(16))) float f16v;
typedef __attribute__((ext_vector_type(2))) unsigned u2;
typedef __attribute__((ext_vector_type(4))) unsigned u4;
typedef unsigned long long u64;
typedef __amdgpu_buffer_rsrc_t rsrc_t;
#define LDSP __attribute__((address_space(3)))
#define GLBP __attribute__((address_space(1)))
#define DI __device__ __forceinline__

constexpr int S = 2048, D = 640, F = 1536;
constexpr int NCOL = 4992;
constexpr unsigned WS_GEN = 0;
constexpr unsigned WS_F0 = 4096;
constexpr unsigned WS_FH = 8192;
constexpr unsigned WS_FKV = 12288;
constexpr unsigned WS_FO = 16384;
constexpr unsigned WS_STATS = 32768;
constexpr unsigned WS_CS = 65536;
constexpr unsigned WS_KVP = 327680;
constexpr unsigned WS_H = 1 << 20;
constexpr unsigned WS_O = WS_H + S * F * 2;
constexpr unsigned WS_END = WS_O + S * D * 2;

constexpr int SLOT = 52 * 1024;
constexpr int LDS_BYTES = 160 * 1024;
constexpr int AUX = 3 * SLOT;
constexpr int IMG = 54 * 1024;
constexpr int SC1 = 16;
constexpr unsigned CODE_BYTES = 62 * 1024;

DI rsrc_t mkrs(const void* b, unsigned n) { return __builtin_amdgcn_make_buffer_rsrc((void*)b, (short)0, (int)n, 0x00020000); }
DI void blds16(rsrc_t rs, unsigned voff, unsigned soff, LDSP char* l) {
  __builtin_amdgcn_raw_ptr_buffer_load_lds(rs, (LDSP void*)l, 16, voff, soff, 0, 0);
}
template <int AUXB> DI u4 bld128(rsrc_t rs, unsigned voff, unsigned soff) { return __builtin_amdgcn_raw_buffer_load_b128(rs, voff, soff, AUXB); }
template <int AUXB> DI u2 bld64(rsrc_t rs, unsigned voff, unsigned soff) { return __builtin_amdgcn_raw_buffer_load_b64(rs, voff, soff, AUXB); }
template <int AUXB> DI unsigned bld32(rsrc_t rs, unsigned voff, unsigned soff) { return __builtin_amdgcn_raw_buffer_load_b32(rs, voff, soff, AUXB); }
DI void bst128(u4 v, rsrc_t rs, unsigned voff, unsigned soff) { __builtin_amdgcn_raw_buffer_store_b128(v, rs, voff, soff, SC1); }
DI void bst64(u2 v, rsrc_t rs, unsigned voff, unsigned soff) { __builtin_amdgcn_raw_buffer_store_b64(v, rs, voff, soff, SC1); }
DI u4 lds_r128(LDSP char* p) { return *(LDSP u4*)p; }
DI u2 lds_tr(LDSP char* p) { return __builtin_bit_cast(u2, __builtin_amdgcn_ds_read_tr16_b64_v4i16((LDSP s4*)p)); }
DI f16v mfma32(u4 a, u4 b, f16v c) { return __builtin_amdgcn_mfma_f32_32x32x16_bf16(__builtin_bit_cast(bf8, a), __builtin_bit_cast(bf8, b), c, 0, 0, 0); }
DI unsigned pk2(float lo, float hi) { f2 v = {lo, hi}; return __builtin_bit_cast(unsigned, __builtin_convertvector(v, bf2)); }
DI float blo(unsigned v) { return __uint_as_float(v << 16); }
DI float bhi(unsigned v) { return __uint_as_float(v & 0xffff0000u); }
DI float exp2c(float u) { return __builtin_amdgcn_fmed3f(__builtin_amdgcn_exp2f(u), 0.f, 1.f); }
constexpr float LOG2E = 1.44269504088896f, LN2 = 0.693147180559945f;
constexpr float PHC = 1e-6f * LOG2E;
DI unsigned phi2(f2 u) {
  const f2 e = {exp2c(u[0]), exp2c(u[1])};
  const f2 v = f2{fmaxf(u[0], PHC), fmaxf(u[1], PHC)} * LN2 + e;
  return pk2(v[0], v[1]);
}
DI u64 ld_flag(const char* ws, unsigned off) { return __hip_atomic_load((const GLBP u64*)(ws + off), __ATOMIC_RELAXED, __HIP_MEMORY_SCOPE_AGENT); }
DI void st_flag(char* ws, unsigned off, u64 v) { __hip_atomic_store((GLBP u64*)(ws + off), v, __ATOMIC_RELAXED, __HIP_MEMORY_SCOPE_AGENT); }
#define BAR() __builtin_amdgcn_s_barrier()
#define WAITV(n) asm volatile("s_waitcnt vmcnt(" #n ")" ::: "memory")
#define WAITL0() asm volatile("s_waitcnt lgkmcnt(0)" ::: "memory")
#define SCHED0() __builtin_amdgcn_sched_barrier(0)
#define FENCE() do { asm volatile("" ::: "memory"); __builtin_amdgcn_sched_barrier(0); } while (0)

#define STAMP(i)
#define STAMPR(i)

struct Params {
  const char* x; const char* wqkv; const char* wo; const char* wg; const char* wu; const char* wd;
  char* out; char* ws;
};

DI void wait_flag(const char* ws, unsigned off, u64 tag, u64 first) {
  u64 v = first;
#pragma unroll 1
  for (int it = 0; it < (1 << 18); it++) {
    if (__all(v == tag)) break;
    __builtin_amdgcn_s_sleep(1);
    v = ld_flag(ws, off);
  }
  FENCE();
}
DI u64 lds_tag(LDSP char* sm) {
  const u2 t = *(LDSP u2*)(sm + AUX + 3072);
  return (u64)(unsigned)__builtin_amdgcn_readfirstlane(t[0]) | ((u64)(unsigned)__builtin_amdgcn_readfirstlane(t[1]) << 32);
}

template <int CTL> DI float dppadd(float v) { return v + __int_as_float(__builtin_amdgcn_update_dpp(0, __float_as_int(v), CTL, 0xf, 0xf, true)); }
DI float swap16add(float v) { const u2 t = __builtin_amdgcn_permlane16_swap(__float_as_uint(v), __float_as_uint(v), false, false); return __uint_as_float(t[0]) + __uint_as_float(t[1]); }
DI float sum32(float v) { return swap16add(dppadd<0x121>(dppadd<0x122>(dppadd<0x124>(dppadd<0x128>(v))))); }
constexpr unsigned EMPTY = 0xffffffffu;

DI u64 row_load(const Params& p, u2 (&xs)[5], int w, int lane) {
  const u64 gen = ld_flag(p.ws, WS_GEN);
  const rsrc_t rx = mkrs(p.x, S * D * 2);
  const unsigned xo = (unsigned)((blockIdx.x * 8 + w * 2 + (lane >> 5)) * 1280 + (lane & 31) * 8);
#pragma unroll
  for (int i = 0; i < 5; i++) xs[i] = bld64<0>(rx, xo + i * 256, 0);
  FENCE();
  return gen;
}
DI void row_stats(const u2 (&xs)[5], rsrc_t rws, int w, int lane, unsigned tag) {
  const int b = blockIdx.x;
  float s = 0;
#pragma unroll
  for (int i = 0; i < 5; i++) s += (blo(xs[i][0]) + bhi(xs[i][0])) + (blo(xs[i][1]) + bhi(xs[i][1]));
  s = sum32(s);
  const float mu = s * (1.f / 640.f);
  float q = 0;
#pragma unroll
  for (int i = 0; i < 5; i++) {
    const float a = blo(xs[i][0]) - mu, c = bhi(xs[i][0]) - mu, e = blo(xs[i][1]) - mu, g = bhi(xs[i][1]) - mu;
    q += a * a + c * c + e * e + g * g;
  }
  q = sum32(q);
  const float rs = rsqrtf(q * (1.f / 640.f) + 1e-5f);
  if ((lane & 31) == 0) bst128(u4{__float_as_uint(mu), tag, __float_as_uint(rs), tag}, rws, (b * 8 + w * 2 + (lane >> 5)) * 16, WS_STATS);
}
struct CS { rsrc_t rs; unsigned vo, step, dst; u4 d[32]; float c[8]; };
DI void cs_init(const Params& p, CS& k, int task, int lane) {
  const int kp = task / 78, cu = task - kp * 78;
  const char* base = cu < 30 ? p.wqkv + cu * 128 : (cu < 54 ? p.wg + (cu - 30) * 128 : p.wu + (cu - 54) * 128);
  const unsigned stride = cu < 30 ? 3840 : 3072;
  k.rs = mkrs(base, task < 234 ? 640 * stride - stride + 128 : 0);
  k.vo = (unsigned)(kp * 256 + (lane >> 3)) * stride + (lane & 7) * 16;
  k.step = 8 * stride;
  k.dst = (kp * NCOL + cu * 64 + (lane & 7) * 8 + (lane >> 5) * 4 + ((lane >> 3) & 1) * 2) * 8;
#pragma unroll
  for (int j = 0; j < 8; j++) k.c[j] = 0;
}
DI void cs_load(CS& k, int i) { k.d[i] = bld128<0>(k.rs, k.vo, i * k.step); }
DI void cs_add(CS& k, int i) {
#pragma unroll
  for (int j = 0; j < 4; j++) { k.c[2 * j] += blo(k.d[i][j]); k.c[2 * j + 1] += bhi(k.d[i][j]); }
}
DI void cs_store(CS& k, rsrc_t rws, int lane, unsigned tag) {
  float e[4];
#pragma unroll
  for (int j = 0; j < 4; j++) {
    const u2 t = __builtin_amdgcn_permlane32_swap(__float_as_uint(k.c[j]), __float_as_uint(k.c[4 + j]), false, false);
    e[j] = dppadd<0x128>(swap16add(__uint_as_float(t[0]) + __uint_as_float(t[1])));
  }
  if ((lane & 16) == 0) bst128(u4{__float_as_uint(lane & 8 ? e[2] : e[0]), tag, __float_as_uint(lane & 8 ? e[3] : e[1]), tag}, rws, k.dst, WS_CS);
}

template <int TYPE>
DI void k1_main(LDSP char* sm, int w, int lane, f16v (&acc)[10]) {
  constexpr int NXF = TYPE == 0 ? 5 : 2, NWF = 7 - NXF;
  constexpr int XB = TYPE == 0 ? 160 * 128 : 256 * 128;
  const int r = lane & 31, hh = lane >> 5, blk = (lane >> 4) & 1, q = (lane & 15) >> 2, pp = lane & 3;
  unsigned xa[4];
#pragma unroll
  for (int k = 0; k < 4; k++) xa[k] = (unsigned)(((TYPE == 0 ? 0 : w * 64) + r) * 128 + (((2 * k + hh) ^ ((r >> 1) & 7)) << 4));
  unsigned wa[2];
  if (TYPE == 0) {
#pragma unroll
    for (int ft = 0; ft < 2; ft++)
      wa[ft] = (unsigned)(XB + blk * 16384 + (8 * hh + q) * 256 + (((w * 4 + ft * 2 + (pp >> 1)) ^ (q << 2) ^ (blk << 1)) << 4) + (pp & 1) * 8);
  } else {
    wa[0] = (unsigned)(XB + (8 * hh + q) * 320 + blk * 32 + pp * 8); wa[1] = 0;
  }
  u4 xf[2][NXF], wf[2][NWF];
  auto rd = [&](int buf, LDSP char* base, int k) {
#pragma unroll
    for (int i = 0; i < NWF; i++) {
      u2 lo, hi;
      if (TYPE == 0) { lo = lds_tr(base + wa[i] + k * 4096); hi = lds_tr(base + wa[i] + k * 4096 + 1024); }
      else { lo = lds_tr(base + wa[0] + k * 5120 + i * 64); hi = lds_tr(base + wa[0] + k * 5120 + i * 64 + 1280); }
      wf[buf][i] = u4{lo[0], lo[1], hi[0], hi[1]};
    }
#pragma unroll
    for (int i = 0; i < NXF; i++) xf[buf][i] = lds_r128(base + xa[k] + i * 4096);
  };
  auto mm = [&](int buf) {
#pragma unroll
    for (int i = 0; i < NXF; i++)
#pragma unroll
      for (int j = 0; j < NWF; j++) {
        if (TYPE == 2) acc[i * NWF + j] = mfma32(xf[buf][i], wf[buf][j], acc[i * NWF + j]);
        else acc[i * NWF + j] = mfma32(wf[buf][j], xf[buf][i], acc[i * NWF + j]);
      }
  };
  auto inter = [&]() {
    constexpr int NR = NXF + 2 * NWF;
#pragma unroll
    for (int i = 0; i < 6; i++) {
      __builtin_amdgcn_sched_group_barrier(0x8, 1, 0);
      __builtin_amdgcn_sched_group_barrier(0x100, (NR + 5) / 6, 0);
    }
    __builtin_amdgcn_sched_group_barrier(0x8, 4, 0);
  };
  BAR();
  rd(0, sm, 0);
  int slot = 0;
#pragma unroll 1
  for (int s = 0; s < 9; s++) {
    LDSP char* base = sm + slot * SLOT;
    slot = slot == 2 ? 0 : slot + 1;
    SCHED0();
    rd(1, base, 1); mm(0); inter(); SCHED0();
    rd(0, base, 2); mm(1); inter(); SCHED0();
    rd(1, base, 3); mm(0); inter(); SCHED0();
    WAITL0(); BAR(); SCHED0();
    rd(0, sm + slot * SLOT, 0); mm(1); inter(); SCHED0();
  }
  SCHED0();
  rd(1, sm, 1); mm(0); inter(); SCHED0();
  rd(0, sm, 2); mm(1); inter(); SCHED0();
  rd(1, sm, 3); mm(0); inter(); SCHED0();
  mm(1); SCHED0();
}

DI void kv_publish(LDSP char* sm, rsrc_t rws, int rt, int cg) {
  const int t = threadIdx.x;
  if (t < 420) {
    LDSP char* src = sm + t * 16;
    const f4 s = (*(LDSP f4*)src + *(LDSP f4*)(src + 8192)) + (*(LDSP f4*)(src + 16384) + *(LDSP f4*)(src + 24576));
    u4 v = __builtin_bit_cast(u4, s);
#pragma unroll
    for (int j = 0; j < 4; j++) v[j] = min(v[j], EMPTY - 1);
    bst128(v, rws, t * 16, WS_KVP + (rt * 8 + cg) * 7168);
  }
}

template <int TYPE>
DI void k1_mfma(const Params& p, LDSP char* sm, rsrc_t rws, int w, int lane, int rt, int cg, int row0) {
  if (TYPE == 0 && (blockIdx.x >> 6) == 2) {
    const u64 a1 = (u64)(w == 0 ? p.wqkv : (w == 1 ? p.wg : (w == 2 ? p.wu : p.wd))), a2 = (u64)(w & 1 ? p.wo : p.x);
    const unsigned n1 = w == 0 ? D * 3840 : F * D * 2, n2 = w & 1 ? D * D * 2 : S * D * 2;
    const u4 r1 = {(unsigned)a1, (unsigned)(a1 >> 32) & 0xffffu, n1, 0x00020000u}, r2 = {(unsigned)a2, (unsigned)(a2 >> 32) & 0xffffu, n2, 0x00020000u};
    asm volatile("s_mov_b32 m0, %4\n\ts_nop 0\n\tbuffer_load_dword %0, %2, 0 offen lds\n\tbuffer_load_dword %1, %3, 0 offen lds"
                 :: "v"((lane * 8 + ((blockIdx.x >> 3) & 7)) * (n1 >> 9)), "v"((lane * 16 + (w >> 1) * 8 + ((blockIdx.x >> 3) & 7)) * (n2 >> 10)), "s"(r1), "s"(r2), "s"((unsigned)(size_t)(sm + AUX + 3328)) : "memory", "m0");
  }
  u2 xs[5];
  const u64 gen = row_load(p, xs, w, lane);
  const int hh = lane >> 5, r = lane & 31;
  f16v acc[10];
#pragma unroll
  for (int i = 0; i < 10; i++)
#pragma unroll
    for (int j = 0; j < 16; j++) acc[i][j] = 0.f;
#pragma unroll
  for (int i = 0; i < 10; i++) asm volatile("" : "+v"(acc[i]));
  FENCE();
  {
    const u64 tag = gen + 1;
    row_stats(xs, rws, w, lane, (unsigned)tag);
    if (threadIdx.x == 0) *(LDSP u2*)(sm + AUX + 3072) = u2{(unsigned)tag, (unsigned)(tag >> 32)};
    WAITL0();
    const u64 wa = (u64)p.ws, oa = (u64)p.out;
    const u4 r1 = {(unsigned)wa, (unsigned)(wa >> 32) & 0xffffu, WS_END, 0x00020000u}, r2 = {(unsigned)oa, (unsigned)(oa >> 32) & 0xffffu, S * D * 2, 0x00020000u};
    if (TYPE == 0 && (blockIdx.x >> 3) == 12)
    asm volatile("s_mov_b32 m0, %4\n\ts_nop 0\n\tbuffer_load_dword %0, %2, 0 offen sc1 lds\n\tbuffer_load_dword %1, %3, 0 offen sc1 lds"
                 :: "v"(threadIdx.x * 38912), "v"(threadIdx.x * 10240), "s"(r1), "s"(r2), "s"((unsigned)(size_t)(sm + AUX + 3328)) : "memory", "m0");
    STAMP(12);
  }
  k1_main<TYPE>(sm, w, lane, acc);
  STAMP(3);
  BAR(); FENCE();
  STAMP(19);
  if (TYPE == 0) {
    f4 cs[2][4];
#pragma unroll
    for (int ft = 0; ft < 2; ft++)
#pragma unroll
      for (int qd = 0; qd < 4; qd++)
        cs[ft][qd] = *(LDSP f4*)(sm + AUX + 2048 + ((qd >> 1) * 128 + w * 32 + ft * 16 + (qd & 1) * 8 + hh * 4) * 4);
    const unsigned dst = (unsigned)(((row0 >> 5) * 96 + cg * 8 + w * 2) * 1024 + lane * 16);
#pragma unroll
    for (int tt = 0; tt < 5; tt++) {
      const float mu = *(LDSP float*)(sm + AUX + (tt * 32 + r) * 4);
      if (tt == 0 && rt == 12) continue;
#pragma unroll
      for (int ft = 0; ft < 2; ft++) {
        const f16v a = acc[tt * 2 + ft];
        float h[8];
#pragma unroll
        for (int j = 0; j < 8; j++) h[j] = (a[j] - mu * cs[ft][j >> 2][j & 3]) * (a[8 + j] - mu * cs[ft][2 + (j >> 2)][j & 3]);
        bst128(u4{pk2(h[0], h[1]), pk2(h[2], h[3]), pk2(h[4], h[5]), pk2(h[6], h[7])}, rws, dst + ft * 1024, WS_H + tt * 98304);
      }
    }
    STAMP(4);
    FENCE(); BAR();
    WAITV(0); BAR();
    if (threadIdx.x == 0) st_flag(p.ws, WS_FH + (rt * 16 + cg) * 8, lds_tag(sm));
  } else if (TYPE == 1) {
    u4 pq[2][10];
#pragma unroll
    for (int tt = 0; tt < 2; tt++) {
      const float mu = *(LDSP float*)(sm + AUX + (w * 64 + tt * 32 + r) * 4);
      const float rl = *(LDSP float*)(sm + AUX + 1024 + (w * 64 + tt * 32 + r) * 4) * LOG2E;
#pragma unroll
      for (int ft = 0; ft < 5; ft++) {
        const f16v a = acc[tt * 5 + ft];
#pragma unroll
        for (int qd = 0; qd < 4; qd++) {
          const f4 cs = *(LDSP f4*)(sm + AUX + 2048 + (ft * 32 + qd * 8 + hh * 4) * 4);
#pragma unroll
          for (int j = 0; j < 2; j++) {
            const f2 u = (f2{a[qd * 4 + 2 * j], a[qd * 4 + 2 * j + 1]} - f2{cs[2 * j], cs[2 * j + 1]} * mu) * rl + PHC;
            pq[tt][ft * 2 + (qd >> 1)][(qd & 1) * 2 + j] = phi2(u);
          }
        }
        asm volatile("" : "+v"(pq[tt][ft * 2]), "+v"(pq[tt][ft * 2 + 1]));
      }
    }
    STAMP(4);
    SCHED0(); BAR(); SCHED0();
    STAMP(5);
    const unsigned dst = (unsigned)((((row0 >> 5) + w * 2) * 40 + cg * 10) * 1024 + lane * 16);
    f16v oa[2][2];
    u4 af[2][3];
    auto rdi = [&](int n) {
      const int hg = n / 5, T = n % 5, k0 = T < 2 ? 0 : T - 1, k1 = T == 0 ? 1 : (T == 4 ? 4 : T + 1);
#pragma unroll
      for (int k = k0; k <= k1; k++) af[n & 1][k - k0] = lds_r128(sm + IMG + (hg * 25 + T * 5 + k) * 1024 + lane * 16);
    };
    auto omm = [&](int n) {
      const int hg = n / 5, T = n % 5, k0 = T < 2 ? 0 : T - 1, k1 = T == 0 ? 1 : (T == 4 ? 4 : T + 1);
#pragma unroll
      for (int tt = 0; tt < 2; tt++)
#pragma unroll
        for (int j = 0; j < 16; j++) oa[n & 1][tt][j] = 0.f;
#pragma unroll
      for (int k = k0; k <= k1; k++)
#pragma unroll
        for (int tt = 0; tt < 2; tt++) oa[n & 1][tt] = mfma32(af[n & 1][k - k0], pq[tt][hg * 5 + k], oa[n & 1][tt]);
    };
    rdi(0); rdi(1); omm(0); SCHED0();
#pragma unroll
    for (int n = 0; n < 10; n++) {
      if (n + 1 < 10) omm(n + 1);
      if (n + 2 < 10) rdi(n + 2);
#pragma unroll
      for (int tt = 0; tt < 2; tt++) {
        const f16v a = oa[n & 1][tt];
        const float d0 = __builtin_amdgcn_rcpf(a[8] + 1e-6f), d1 = __builtin_amdgcn_rcpf(a[12] + 1e-6f);
        bst128(u4{pk2(a[0] * d0, a[1] * d0), pk2(a[2] * d0, a[3] * d0), pk2(a[4] * d1, a[5] * d1), pk2(a[6] * d1, a[7] * d1)},
               rws, dst + (n & 3) * 1024, WS_O + tt * 40960 + (n >> 2) * 4096);
      }
#pragma unroll
      for (int i = 0; i < 6; i++) { __builtin_amdgcn_sched_group_barrier(0x8, 1, 0); __builtin_amdgcn_sched_group_barrier(0x2, 5, 0); }
      SCHED0();
    }
    STAMP(6);
    WAITV(0); BAR();
    if (threadIdx.x == 0) st_flag(p.ws, WS_FO + (rt * 16 + cg) * 8, lds_tag(sm));
  } else {
    float cs[5];
#pragma unroll
    for (int ct = 0; ct < 5; ct++) cs[ct] = *(LDSP float*)(sm + AUX + 2048 + (ct * 32 + r) * 4);
    const bool isk = r < 16;
    const float kmul = isk ? 1.f : 0.f, kadd = isk ? 1e-6f : 0.f, klo = isk ? 1e-6f : -__builtin_inff();
    u4 fr[4][5];
#pragma unroll
    for (int g = 0; g < 4; g++) {
      f2 mu[4], rs[4], rl[4];
#pragma unroll
      for (int hq = 0; hq < 2; hq++) {
        LDSP char* sp = sm + AUX + (w * 64 + g * 16 + hq * 8 + hh * 4) * 4;
        const f4 a = *(LDSP f4*)sp, b = *(LDSP f4*)(sp + 1024);
        mu[hq * 2] = f2{a[0], a[1]}; mu[hq * 2 + 1] = f2{a[2], a[3]};
        rs[hq * 2] = f2{b[0], b[1]}; rs[hq * 2 + 1] = f2{b[2], b[3]};
      }
#pragma unroll
      for (int j = 0; j < 4; j++) rl[j] = rs[j] * LOG2E;
#pragma unroll
      for (int ct = 0; ct < 5; ct++) {
#pragma unroll
        for (int j = 0; j < 4; j++) {
          const f16v& a = acc[(g >> 1) * 5 + ct];
          const f2 y = f2{a[(g & 1) * 8 + 2 * j], a[(g & 1) * 8 + 2 * j + 1]} - mu[j] * cs[ct];
          if (ct < 2) fr[g][ct][j] = phi2(y * rl[j] + PHC);
          else if (ct == 2) {
            const f2 u = y * rl[j], t = y * rs[j] + kadd, e = {exp2c(u[0]), exp2c(u[1])};
            const f2 v = e * kmul + f2{fmaxf(t[0], klo), fmaxf(t[1], klo)};
            fr[g][ct][j] = pk2(v[0], v[1]);
          } else { const f2 v = y * rs[j]; fr[g][ct][j] = pk2(v[0], v[1]); }
        }
      }
      SCHED0();
    }
    f16v z[8];
#pragma unroll
    for (int i = 0; i < 8; i++)
#pragma unroll
      for (int j = 0; j < 16; j++) z[i][j] = 0.f;
#pragma unroll
    for (int g = 0; g < 4; g++) {
      u4 mb;
#pragma unroll
      for (int j = 0; j < 4; j++) mb[j] = isk ? 0x3f803f80u : fr[g][2][j];
      z[0] = mfma32(fr[g][0], fr[g][3], z[0]);
      z[1] = mfma32(fr[g][0], fr[g][4], z[1]);
      z[2] = mfma32(fr[g][0], mb, z[2]);
      z[3] = mfma32(fr[g][1], fr[g][3], z[3]);
      z[4] = mfma32(fr[g][1], fr[g][4], z[4]);
      z[5] = mfma32(fr[g][1], mb, z[5]);
      z[6] = mfma32(fr[g][2], fr[g][4], z[6]);
      z[7] = mfma32(fr[g][2], mb, z[7]);
    }
    unsigned pa[3], pm[3];
#pragma unroll
    for (int vt = 0; vt < 3; vt++) {
      const int e = vt == 2 ? 48 + r : vt * 32 + r, he = e / 20;
      int ee = e - he * 20; unsigned m = 31u << (5 * he);
      if (vt == 2 && isk) { ee = 20; m = r == 0 ? 0xfffffu : 0u; }
      pa[vt] = (unsigned)(w * 8192 + ee * 16 + hh * 336); pm[vt] = m >> hh;
    }
    SCHED0();
#pragma unroll
    for (int i = 0; i < 8; i++)
#pragma unroll
      for (int qd = 0; qd < 4; qd++) {
        const int kt = i / 3, vt = i < 6 ? i % 3 : i - 5, c = kt * 8 + qd * 2;
        const bool need = (i == 1) ? qd >= 2 : (i == 3) ? qd == 0 : (i >= 6) ? qd < 2 : true;
        if (need && (pm[vt] & (1u << c)))
          *(LDSP f4*)(sm + pa[vt] + c * 336) = f4{z[i][qd * 4], z[i][qd * 4 + 1], z[i][qd * 4 + 2], z[i][qd * 4 + 3]};
      }
    STAMP(4);
    WAITL0(); BAR();
    kv_publish(sm, rws, rt, cg);
    WAITL0(); BAR();
  }
}

DI void p2_out(const Params& p, LDSP char* sm, int item, u4 xv, unsigned go) {
  const int row = item >> 3, c = item & 7;
  LDSP char* src = sm + (row >> 3) * 8192 + (row & 7) * 256;
  const unsigned c0 = ((2 * c) ^ (row & 7)) << 4, c1 = c0 ^ 16;
  f4 a = *(LDSP f4*)(src + c0), b = *(LDSP f4*)(src + c1);
#pragma unroll
  for (int i = 1; i < 4; i++) { a += *(LDSP f4*)(src + i * 2048 + c0); b += *(LDSP f4*)(src + i * 2048 + c1); }
  bst128(u4{pk2(blo(xv[0]) + a[0], bhi(xv[0]) + a[1]), pk2(blo(xv[1]) + a[2], bhi(xv[1]) + a[3]),
            pk2(blo(xv[2]) + b[0], bhi(xv[2]) + b[1]), pk2(blo(xv[3]) + b[2], bhi(xv[3]) + b[3])}, mkrs(p.out, S * D * 2), go, 0);
}

constexpr int PD = 8;
DI void p2_mfma(const Params& p, LDSP char* sm, rsrc_t rws, int w, int lane, int row2, int cg2) {
  const int hh = lane >> 5, r = lane & 31, blk = (lane >> 4) & 1, q = (lane & 15) >> 2, pp = lane & 3;
  const u64 tag = lds_tag(sm);
  const unsigned fh = WS_FH + (((row2 + ((lane >> 2) & 1) * 95) / 160) * 16 + 3 * w + min(lane & 3, 2)) * 8;
  const unsigned fo = WS_FO + (((row2 + (lane & 1) * 95) >> 8) * 16 + w) * 8;
  float r2[3];
  wait_flag(p.ws, fh, tag, ld_flag(p.ws, fh));
  STAMP(8);
  const unsigned ah = (unsigned)(((row2 >> 5) * 96 + w * 24) * 1024 + lane * 16), ao = (unsigned)(((row2 >> 5) * 40 + w * 10) * 1024 + lane * 16);
  u4 af[PD][3], bf[2][2];
  auto lda = [&](int i) {
#pragma unroll
    for (int tt = 0; tt < 3; tt++) {
      if (i < 24) af[i % PD][tt] = bld128<SC1>(rws, ah + (i & 3) * 1024, WS_H + tt * 98304 + (i >> 2) * 4096);
      else af[i % PD][tt] = bld128<SC1>(rws, ao + (i & 3) * 1024, WS_O + tt * 40960 + ((i - 24) >> 2) * 4096);
    }
  };
  LDSP char* ba[2][3];
#pragma unroll
  for (int ct = 0; ct < 2; ct++)
#pragma unroll
    for (int sg = 0; sg < 3; sg++) {
      ba[ct][sg] = sm + sg * 49152 + w * 2048 + (4 * hh + q) * 128 + (((ct * 4 + blk * 2 + (pp >> 1)) ^ ((q >> 1) << 2)) << 4) + (pp & 1) * 8;
      asm volatile("" : "+v"(ba[ct][sg]));
    }
  auto ldb = [&](int i) {
    const int sl = 17 - (i < 18 ? i : i - 18);
#pragma unroll
    for (int ct = 0; ct < 2; ct++) {
      LDSP char* base = ba[ct][sl / 6] + (sl % 6) * 8192;
      const u2 lo = lds_tr(base), hi = lds_tr(base + 1024);
      bf[i & 1][ct] = u4{lo[0], lo[1], hi[0], hi[1]};
    }
  };
  f16v acc[6];
#pragma unroll
  for (int i = 0; i < 6; i++)
#pragma unroll
    for (int j = 0; j < 16; j++) acc[i][j] = 0.f;
#pragma unroll
  for (int i = 0; i < PD; i++) lda(i);
  ldb(0);
  u64 ofl = 0;
  auto step = [&](int i, bool pre) {
    SCHED0();
    if (i == 7 || i == 15 || i == 23) { STAMP(12 + i / 4); WAITL0(); BAR(); STAMP(13 + i / 4); SCHED0(); }
    if (i + 1 < 34) ldb(i + 1);
#pragma unroll
    for (int tt = 0; tt < 3; tt++)
#pragma unroll
      for (int ct = 0; ct < 2; ct++) acc[tt * 2 + ct] = mfma32(bf[i & 1][ct], af[i % PD][tt], acc[tt * 2 + ct]);
    SCHED0();
    if (pre) lda(i + PD);
    if (i == 23) {
#pragma unroll
      for (int tt = 0; tt < 3; tt++) { acc[tt * 2] *= r2[tt] * r2[tt]; acc[tt * 2 + 1] *= r2[tt] * r2[tt]; }
#pragma unroll
      for (int k = 0; k < 6; k++) asm volatile("" : "+v"(acc[k]));
      SCHED0();
    }
  };
  constexpr int OCHK = 20;
#pragma unroll
  for (int i = 0; i < OCHK; i++) {
    step(i, i + PD < 24);
    if (i == 16) {
#pragma unroll
      for (int tt = 0; tt < 3; tt++) r2[tt] = __uint_as_float(bld32<SC1>(rws, (row2 + tt * 32 + r) * 16 + 8, WS_STATS));
      ofl = ld_flag(p.ws, fo);
    }
  }
  STAMP(5);
  const bool early = __all(ofl == tag);
  FENCE();
  if (early) {
#pragma unroll
    for (int j = 24; j < OCHK + PD; j++) lda(j);
  }
#pragma unroll
  for (int i = OCHK; i < 24; i++) step(i, false);
  if (!early) {
    wait_flag(p.ws, fo, tag, ld_flag(p.ws, fo));
#pragma unroll
    for (int j = 24; j < OCHK + PD; j++) lda(j);
  }
  STAMP(9);
#pragma unroll
  for (int j = OCHK + PD; j < 24 + PD; j++) lda(j);
#pragma unroll
  for (int i = 24; i < 34; i++) step(i, i + PD < 34);
  STAMP(10);
  const int item = 512 + threadIdx.x;
  const unsigned go = (unsigned)((row2 + (item >> 3)) * 1280 + cg2 * 128 + (item & 7) * 16);
  const u4 xv = *(const GLBP u4*)(p.x + go);
#pragma unroll
  for (int tt = 0; tt < 3; tt++)
#pragma unroll
    for (int ct = 0; ct < 2; ct++)
#pragma unroll
      for (int qd = 0; qd < 4; qd++)
        *(LDSP f4*)(sm + (tt * 4 + (r >> 3)) * 8192 + w * 2048 + (r & 7) * 256 + (((ct * 8 + qd * 2 + hh) ^ (r & 7)) << 4)) =
            f4{acc[tt * 2 + ct][qd * 4], acc[tt * 2 + ct][qd * 4 + 1], acc[tt * 2 + ct][qd * 4 + 2], acc[tt * 2 + ct][qd * 4 + 3]};
  WAITL0(); BAR();
  p2_out(p, sm, item, xv, go);
}

template <int type>
DI void loader(const Params& p, LDSP char* sm, rsrc_t rws, int lw, int lane, int rt, int cg, int row0, int row2, int cg2) {
  const int b = blockIdx.x, t = lw * 64 + lane;
  constexpr bool gu = type == 0;
  unsigned vo[13];
  {
    const int x0 = gu ? lw * 5 : lw * 8;
    const unsigned xr = (unsigned)((row0 + x0 * 8 + (lane >> 3)) * 1280), xs = (unsigned)(((lane & 7) ^ (lane >> 4) ^ ((x0 & 1) << 2)) << 4);
#pragma unroll
    for (int i = 0; i < 8; i++) vo[i] = xr + (xs ^ ((i & 1) << 6)) + i * 10240;
    if (gu) {
      const unsigned g0 = (unsigned)(((lw & 1) * 32 + (lane >> 4)) * 3072 + (((lane & 15) ^ ((lane >> 4) << 2) ^ (lw & 2)) << 4));
#pragma unroll
      for (int i = 5; i < 13; i++) vo[i] = g0 + (i - 5) * 12288;
    } else {
#pragma unroll
      for (int i = 0; i < 5; i++) {
        const int e = i * 64 + lane, rq = (e * 205) >> 12, pos = e - rq * 20;
        const int adj = type == 1 ? 0 : (pos < 10 ? 0 : (pos < 12 ? 1248 : 1088));
        vo[8 + i] = (unsigned)((lw * 16 + rq) * 3840 + pos * 16 + adj);
      }
    }
  }
  const unsigned wofs = gu ? cg * 256 : (type == 1 ? cg * 320 : 1280 + cg * 160);
  const char* wbase = (gu ? ((lw >> 1) ? p.wu : p.wg) : p.wqkv) + wofs;
  const unsigned wstep = gu ? 64 * 3072 : 64 * 3840, wlen = wstep * 10 - wofs, mstep = gu ? wstep : 128;
  const rsrc_t rx = mkrs(p.x, S * D * 2), rw = mkrs(wbase, wlen), rm = mkrs(gu ? wbase : p.x, gu ? wlen : S * D * 2);
  const int dx = gu ? lw * 5120 : lw * 8192, dm = gu ? 20480 + lw * 8192 : dx + 5120, dw = gu ? dm + 3072 : 32768 + lw * 5120;
  CS ck;
  const int task = type == 1 ? (rt * 4 + cg) * 4 + lw : min(156 + lw * 64 + cg * 8 + rt, 234);
  const bool act = !gu && task < 234;
  auto issue = [&](int s, int a0 = 0, int a1 = 0) {
    LDSP char* d = sm + (s % 3) * SLOT;
#pragma unroll
    for (int i = 0; i < 13; i++) {
      if (i < 5) blds16(rx, vo[i], s * 128, d + dx + i * 1024);
      else if (i < 8) blds16(rm, vo[i], s * mstep, d + dm + (i - 5) * 1024);
      else blds16(rw, vo[i], s * wstep, d + dw + (i - 8) * 1024);
      if (i >= 1 && a0 + i - 1 < a1 && (type == 1 || act)) {
        FENCE(); cs_add(ck, a0 + i - 1);
        asm volatile("" : "+v"(ck.c[0]), "+v"(ck.c[1]), "+v"(ck.c[2]), "+v"(ck.c[3]), "+v"(ck.c[4]), "+v"(ck.c[5]), "+v"(ck.c[6]), "+v"(ck.c[7]));
        FENCE();
      }
    }
    FENCE();
  };
  auto csl = [&](int i0, int i1) {
    if (type == 1 || act) {
#pragma unroll
      for (int i = i0; i < i1; i++) cs_load(ck, i);
      FENCE();
    }
  };
  issue(0); issue(1);
  if (!gu) cs_init(p, ck, task, lane);
  constexpr bool q1 = type == 1;
  STAMP(12);
  WAITV(13); BAR(); STAMP(13);
  issue(2);
  if (type == 2) {
    bst128(u4{EMPTY, EMPTY, EMPTY, EMPTY}, rws, t * 16, WS_KVP + (rt * 8 + cg) * 7168);
    bst128(u4{EMPTY, EMPTY, EMPTY, EMPTY}, rws, min(t + 256, 419) * 16, WS_KVP + (rt * 8 + cg) * 7168);
    FENCE();
  }
  const rsrc_t rd_ = mkrs(p.wd, type == 1 ? 0 : F * D * 2), ro_ = mkrs(p.wo, type == 1 ? 0 : D * D * 2);
  const unsigned bo = (unsigned)((lane >> 3) * 1280 + cg2 * 128 + (((lane & 7) ^ (((lane >> 4) & 1) << 2)) << 4));
  auto bdma = [&](int i) {
#pragma unroll
    for (int h = 0; h < 2; h++) blds16(rd_, bo, (lw * 384 + i * 16 + h * 8) * 1280, sm + (17 - i) * 8192 + lw * 2048 + h * 1024);
  };
  u4 br[46];
  auto breg = [&](int i0, int i1) {
#pragma unroll
    for (int i = i0; i < i1; i++) {
      const int st = 11 + (i >> 1);
      br[i] = st < 24 ? bld128<0>(rd_, bo, (lw * 384 + st * 16 + (i & 1) * 8) * 1280) : bld128<0>(ro_, bo, (lw * 160 + (st - 24) * 16 + (i & 1) * 8) * 1280);
    }
    FENCE();
  };
  auto bput = [&](int i0, int i1) {
#pragma unroll
    for (int i = i0; i < i1; i++) {
      const int st = 11 + (i >> 1), sl = st < 18 ? st : st - 18;
      *(LDSP u4*)(sm + (17 - sl) * 8192 + lw * 2048 + (i & 1) * 1024 + lane * 16) = br[i];
    }
  };
  auto bgu = [&](int i0, int i1) { if (gu) breg(i0, i1); };
  auto bkv = [&](int i0, int i1) { if (type == 2) breg(i0, i1); };
#define WAITG(a, b, c) do { if (gu) WAITV(a); else if (type == 1) WAITV(b); else WAITV(c); } while (0)
#define WAITA(a, b, c, d) do { if (type == 2 && act) WAITV(d); else WAITG(a, b, c); } while (0)
  csl(0, q1 ? 11 : 8); WAITA(13, 24, 15, 23); BAR(); STAMP(14);
  issue(3); csl(q1 ? 11 : 8, q1 ? 22 : 16); WAITA(13, 35, 13, 29); BAR(); STAMP(19);
  const u64 tag = lds_tag(sm);
  if (t < (type == 2 ? 2 : 1)) st_flag(p.ws, t ? WS_FKV + (cg * 8 + rt) * 8 : WS_F0 + b * 8, tag);
  FENCE();
  issue(4, 0, q1 ? 11 : 0); bgu(0, 4); csl(22, q1 ? 32 : 0); WAITA(17, 34, 13, 21); BAR(); STAMP(15);
  issue(5, q1 ? 11 : 0, q1 ? 22 : 8); bgu(4, 8); WAITG(21, 23, 13); BAR(); STAMP(16);
  issue(6, q1 ? 22 : 8, q1 ? 32 : 16); bgu(8, 12);
  if (type == 1 || act) cs_store(ck, rws, lane, (unsigned)tag);
  WAITA(21, 14, 13, 14); BAR(); STAMP(6);
  issue(7); bgu(12, 16); bkv(0, 4);
  unsigned sto = min(row0 + t, S - 1) * 16, cso;
  {
    const int tc = gu ? t : min(t, 159);
    cso = 8 * (gu ? 1920 + (tc >> 7) * 1536 + cg * 128 + (tc & 127)
                  : (type == 1 ? cg * 160 + tc : cg * 80 + tc + (tc < 80 ? 640 : (tc < 96 ? 1264 : 1184))));
  }
  asm volatile("" : "+v"(sto), "+v"(cso));
  WAITA(21, 14, 17, 18); BAR(); STAMP(7);
  issue(8); bgu(16, 20); bkv(4, 8); WAITG(21, 13, 21); BAR(); STAMP(17);
  issue(9); bgu(20, 24); bkv(8, 12); WAITG(21, 13, 21); BAR(); STAMP(18);
  u4 stv; u2 csv[3];
  auto aux = [&]() {
    stv = bld128<SC1>(rws, sto, WS_STATS);
#pragma unroll
    for (int i = 0; i < 3; i++) csv[i] = bld64<SC1>(rws, cso, WS_CS + i * NCOL * 8);
    FENCE();
  };
  aux();
#pragma unroll
  for (int i = 5; i < 11; i++) bdma(i);
  FENCE();
  if (type == 1) *(LDSP u2*)(sm + IMG + t * 8) = u2{0, 0};
  WAITG(20, 16, 20); BAR();
  if (type != 1) {
#pragma unroll
    for (int i = 0; i < 5; i++) bdma(i);
    FENCE();
  }
  auto fresh = [&]() {
    const unsigned tg = (unsigned)tag;
    return __all(stv[1] == tg && stv[3] == tg && csv[0][1] == tg && csv[1][1] == tg && csv[2][1] == tg);
  };
  if (!fresh()) {
#pragma unroll 1
    for (int it = 0; it < (1 << 18); it++) { aux(); if (fresh()) break; }
  }
  *(LDSP unsigned*)(sm + AUX + t * 4) = stv[0];
  *(LDSP unsigned*)(sm + AUX + 1024 + t * 4) = stv[2];
  *(LDSP float*)(sm + AUX + 2048 + t * 4) = (__uint_as_float(csv[0][0]) + __uint_as_float(csv[1][0])) + __uint_as_float(csv[2][0]);
  if (type == 1) WAITV(0);
  FENCE();
  bgu(24, 28);
  WAITL0(); BAR();
  STAMP(3);
  if (type == 1) {
    const unsigned fk = WS_FKV + (cg * 16 + (lane & 15)) * 8;
    wait_flag(p.ws, fk, tag, ld_flag(p.ws, fk));
    u4 pv[4][8];
    unsigned off[4], da[4];
#pragma unroll
    for (int m = 0; m < 4; m++) {
      const int g = min(t + m * 256, 999), dd = g / 25, e5 = g - dd * 25, hg = dd >= 20, dq = dd - hg * 20;
      const int eg = (dq / 5) * 20 + (e5 < 20 ? e5 : (e5 - 20) * 4);
      off[m] = WS_KVP + (cg * 2 + hg) * 7168 + (dq * 21 + min(e5, 20)) * 16;
      da[m] = IMG + hg * 25600 + (dq >> 2) * 1024 + (dq & 1) * 512 + ((dq >> 1) & 1) * 8 + (eg >> 4) * 5120 + (eg & 15) * 16 + (e5 < 20 ? 0 : 256);
      asm volatile("" : "+v"(off[m]), "+v"(da[m]));
#pragma unroll
      for (int i = 0; i < 8; i++) asm volatile("" : "=v"(pv[m][i]));
    }
    const u4 rk = {(unsigned)(size_t)p.ws, (unsigned)((size_t)p.ws >> 32) & 0xffffu, WS_END, 0x00020000u};
    auto fetch = [&](unsigned set) {
#pragma unroll
      for (int i = 0; i < 8; i++)
        if (set >> i & 1)
          asm volatile("buffer_load_dwordx4 %0, %4, %8, %9 offen sc1\n\tbuffer_load_dwordx4 %1, %5, %8, %9 offen sc1\n\t"
                       "buffer_load_dwordx4 %2, %6, %8, %9 offen sc1\n\tbuffer_load_dwordx4 %3, %7, %8, %9 offen sc1"
                       : "+v"(pv[0][i]), "+v"(pv[1][i]), "+v"(pv[2][i]), "+v"(pv[3][i])
                       : "v"(off[0]), "v"(off[1]), "v"(off[2]), "v"(off[3]), "s"(rk), "s"(i * 8 * 7168) : "memory");
    };
    auto landed = [&]() {
      WAITV(0);
#pragma unroll
      for (int i = 0; i < 8; i++) asm volatile("" : "+v"(pv[0][i]), "+v"(pv[1][i]), "+v"(pv[2][i]), "+v"(pv[3][i]));
    };
    {
      const unsigned ho = WS_KVP + ((lane & 7) * 8 + cg * 2 + ((lane >> 3) & 1)) * 7168 + 419 * 16;
      unsigned got = 0;
#pragma unroll 1
      for (int it = 0; it < (1 << 18) && got != 255; it++) {
        const unsigned mk = (unsigned)__ballot(bld32<SC1>(rws, ho, 0) != EMPTY), now = mk & (mk >> 8) & 255 & ~got;
        fetch(now);
        got |= now;
      }
    }
    STAMP(4);
    f4 sum[4];
#pragma unroll 1
    for (int it = 0; it < (1 << 18); it++) {
      landed();
      bool nan = false;
#pragma unroll
      for (int m = 0; m < 4; m++) {
        sum[m] = __builtin_bit_cast(f4, pv[m][0]);
#pragma unroll
        for (int i = 1; i < 8; i++) sum[m] += __builtin_bit_cast(f4, pv[m][i]);
        nan = nan || __builtin_isunordered(sum[m][0], sum[m][1]) || __builtin_isunordered(sum[m][2], sum[m][3]);
      }
      if (!__any(nan)) break;
      unsigned mx = 0;
#pragma unroll
      for (int i = 0; i < 8; i++)
#pragma unroll
        for (int m = 0; m < 4; m++) mx = max(max(mx, max(pv[m][i][0], pv[m][i][1])), max(pv[m][i][2], pv[m][i][3]));
      if (!__any(mx == EMPTY)) break;
      fetch(255);
    }
    STAMP(5);
#pragma unroll
    for (int m = 0; m < 4; m++) *(LDSP u2*)(sm + da[m]) = u2{pk2(sum[m][0], sum[m][1]), pk2(sum[m][2], sum[m][3])};
    WAITL0(); BAR();
    BAR();
    return;
  }
  __builtin_amdgcn_s_setprio(0);
  if (gu) {
    BAR();
    breg(28, 38);
    WAITV(14);
    STAMP(4);
  } else {
    breg(12, 30);
    STAMP(4);
    BAR(); kv_publish(sm, rws, rt, cg); WAITL0();
    WAITV(18);
  }
  BAR();
  bput(0, 14);
  FENCE();
  bkv(30, 46); bgu(38, 46);
  WAITL0(); BAR();
  bput(14, 30);
  WAITL0(); BAR();
  bput(30, 46);
  WAITL0(); BAR();
  unsigned go[2]; u4 xv[2];
#pragma unroll
  for (int i = 0; i < 2; i++) {
    const int item = i * 256 + t;
    go[i] = (unsigned)((row2 + (item >> 3)) * 1280 + cg2 * 128 + (item & 7) * 16);
    xv[i] = *(const GLBP u4*)(p.x + go[i]);
  }
  BAR();
#pragma unroll
  for (int i = 0; i < 2; i++) p2_out(p, sm, i * 256 + t, xv[i], go[i]);
}

__global__ __launch_bounds__(512, 2) void fused_kernel(Params) {
  typedef unsigned long long ka_t __attribute__((ext_vector_type(8)));
  ka_t ka;
  asm volatile("s_load_dwordx16 %0, %1, 0x0" : "=s"(ka) : "s"(__builtin_amdgcn_kernarg_segment_ptr()));
  extern __shared__ __attribute__((aligned(16))) char smem[];
  LDSP char* sm = (LDSP char*)smem;
  const int lane = threadIdx.x & 63, w = __builtin_amdgcn_readfirstlane(threadIdx.x >> 6), b = blockIdx.x;
  u64 pc0 = __builtin_amdgcn_s_getpc();
  if (w < 2) {
    const u4 rc = {(unsigned)pc0, (unsigned)(pc0 >> 32) & 0xffffu, CODE_BYTES, 0x00020000u};
    asm volatile("s_mov_b32 m0, %0\n\ts_nop 0\n\tbuffer_load_dword %1, %2, 0 offen lds"
                 :: "s"((unsigned)(size_t)(sm + AUX + 3328)), "v"(min(threadIdx.x * 128 + ((blockIdx.x >> 3) & 3) * 16384, CODE_BYTES - 128)), "s"(rc) : "memory", "m0");
  }
  FENCE();
  int type, rt, cg, n2;
  {
    const int c = b & 7, j = b >> 3, a = c & 1, q = c >> 1;
    int lo = (j - 12) >> 31;
    asm volatile("" : "+s"(lo));
    const int n = j + ((a * 19 - 12) & ~lo), n3 = (n * 43) >> 7, m = n - n3 * 3, t1 = min(m, 1) & lo;
    type = ((t1 + 1) & lo) + ((((b | 6) + 2) >> 8) & ~a) * 3;
    rt = n3 + (4 * a & lo);
    cg = q * (3 + ((t1 - 2) & lo)) + m - t1;
    n2 = j - 4 + ((3 - n3) & lo);
    n2 = max(n2 + q * 55 + a * 27, 0);
  }
  int row0 = min(rt * (b < 96 ? 256 : 160), S - 160);
  const int rt2 = (n2 * 205) >> 11;
  int cg2 = n2 - rt2 * 10, row2 = min(rt2 * 96, S - 96);
  asm volatile("s_waitcnt lgkmcnt(0)" : "+s"(ka), "+s"(type), "+s"(rt), "+s"(cg), "+s"(row0), "+s"(row2), "+s"(cg2));
  const Params p = {(const char*)ka[0], (const char*)ka[1], (const char*)ka[2], (const char*)ka[3], (const char*)ka[4], (const char*)ka[5], (char*)ka[6], (char*)ka[7]};
  STAMPR(0); STAMP(1);
  const rsrc_t rws = mkrs(p.ws, WS_END);
  if (type == 3) {
    u2 xs[5];
    const u64 tag = row_load(p, xs, w & 3, lane) + 1;
    const int task = 128 + ((b >> 1) & 3) + w * 4;
    CS ck;
    cs_init(p, ck, task < 156 ? task : 234, lane);
#pragma unroll
    for (int i = 0; i < 32; i++) cs_load(ck, i);
    FENCE();
    if (w < 4) row_stats(xs, rws, w, lane, (unsigned)tag);
#pragma unroll
    for (int i = 0; i < 32; i++) cs_add(ck, i);
    if (task < 156) cs_store(ck, rws, lane, (unsigned)tag);
    STAMP(3);
    BAR();
    if (w != 0) return;
    if (lane == 0) st_flag(p.ws, WS_F0 + b * 8, tag);
    if (b != 248) return;
#pragma unroll 1
    for (int i = 0; i < 4; i++) wait_flag(p.ws, WS_F0 + (i * 64 + lane) * 8, tag, tag - 1);
    if (lane == 0) st_flag(p.ws, WS_GEN, tag);
    return;
  }
  if (w >= 4) {
    __builtin_amdgcn_s_setprio(3);
    if (type == 2) loader<2>(p, sm, rws, w - 4, lane, rt, cg, row0, row2, cg2);
    else if (type == 0) loader<0>(p, sm, rws, w - 4, lane, rt, cg, row0, row2, cg2);
    else loader<1>(p, sm, rws, w - 4, lane, rt, cg, row0, row2, cg2);
  } else {
    if (type == 0) k1_mfma<0>(p, sm, rws, w, lane, rt, cg, row0);
    else if (type == 1) k1_mfma<1>(p, sm, rws, w, lane, rt, cg, row0);
    else k1_mfma<2>(p, sm, rws, w, lane, rt, cg, row0);
    STAMP(7);
    if (type != 1) p2_mfma(p, sm, rws, w, lane, row2, cg2);
  }
  STAMP(11); STAMPR(2);
}

extern "C" void kernel_launch(void* const* d_in, const int* in_sizes, int n_in, void* d_out, int out_size,
                              void* d_ws, size_t ws_size, hipStream_t stream) {
  Params p;
  p.x = (const char*)d_in[0]; p.wqkv = (const char*)d_in[1]; p.wo = (const char*)d_in[2];
  p.wg = (const char*)d_in[4]; p.wu = (const char*)d_in[5]; p.wd = (const char*)d_in[6];
  p.out = (char*)d_out; p.ws = (char*)d_ws;
  (void)hipFuncSetAttribute((const void*)fused_kernel, hipFuncAttributeMaxDynamicSharedMemorySize, LDS_BYTES);
  fused_kernel<<<256, 512, LDS_BYTES, stream>>>(p);
}
```

```cpp
#include <hip/hip_runtime.h>
#include <hip/hip_bf16.h>
#include <stdint.h>

typedef __attribute__((ext_vector_type(8))) __bf16 bf8;
typedef __attribute__((ext_vector_type(2))) __bf16 bf2;
typedef __attribute__((ext_vector_type(4))) short s4;
typedef __attribute__((ext_vector_type(2))) float f2;
typedef __attribute__((ext_vector_type(4))) float f4;
typedef __attribute__((ext_vector_type(16))) float f16v;
typedef __attribute__((ext_vector_type(2))) unsigned u2;
typedef __attribute__((ext_vector_type(4))) unsigned u4;
typedef unsigned long long u64;
typedef __amdgpu_buffer_rsrc_t rsrc_t;
#define LDSP __attribute__((address_space(3)))
#define GLBP __attribute__((address_space(1)))
#define DI __device__ __forceinline__

constexpr int S = 2048, D = 640, F = 1536;
constexpr int NCOL = 4992;
constexpr unsigned WS_GEN = 0;
constexpr unsigned WS_F0 = 4096;
constexpr unsigned WS_FH = 8192;
constexpr unsigned WS_FKV = 12288;
constexpr unsigned WS_FO = 16384;
constexpr unsigned WS_STATS = 32768;
constexpr unsigned WS_CS = 65536;
constexpr unsigned WS_KVP = 327680;
constexpr unsigned WS_H = 1 << 20;
constexpr unsigned WS_O = WS_H + S * F * 2;
constexpr unsigned WS_END = WS_O + S * D * 2;

constexpr int SLOT = 52 * 1024;
constexpr int LDS_BYTES = 160 * 1024;
constexpr int AUX = 3 * SLOT;
constexpr int IMG = 54 * 1024;
constexpr int SC1 = 16;
constexpr unsigned CODE_BYTES = 63 * 1024;

DI rsrc_t mkrs(const void* b, unsigned n) { return __builtin_amdgcn_make_buffer_rsrc((void*)b, (short)0, (int)n, 0x00020000); }
DI void blds16(rsrc_t rs, unsigned voff, unsigned soff, LDSP char* l) {
  __builtin_amdgcn_raw_ptr_buffer_load_lds(rs, (LDSP void*)l, 16, voff, soff, 0, 0);
}
template <int AUXB> DI u4 bld128(rsrc_t rs, unsigned voff, unsigned soff) { return __builtin_amdgcn_raw_buffer_load_b128(rs, voff, soff, AUXB); }
template <int AUXB> DI u2 bld64(rsrc_t rs, unsigned voff, unsigned soff) { return __builtin_amdgcn_raw_buffer_load_b64(rs, voff, soff, AUXB); }
template <int AUXB> DI unsigned bld32(rsrc_t rs, unsigned voff, unsigned soff) { return __builtin_amdgcn_raw_buffer_load_b32(rs, voff, soff, AUXB); }
DI void bst128(u4 v, rsrc_t rs, unsigned voff, unsigned soff) { __builtin_amdgcn_raw_buffer_store_b128(v, rs, voff, soff, SC1); }
DI void bst64(u2 v, rsrc_t rs, unsigned voff, unsigned soff) { __builtin_amdgcn_raw_buffer_store_b64(v, rs, voff, soff, SC1); }
DI u4 lds_r128(LDSP char* p) { return *(LDSP u4*)p; }
DI u2 lds_tr(LDSP char* p) { return __builtin_bit_cast(u2, __builtin_amdgcn_ds_read_tr16_b64_v4i16((LDSP s4*)p)); }
DI f16v mfma32(u4 a, u4 b, f16v c) { return __builtin_amdgcn_mfma_f32_32x32x16_bf16(__builtin_bit_cast(bf8, a), __builtin_bit_cast(bf8, b), c, 0, 0, 0); }
DI unsigned pk2(float lo, float hi) { f2 v = {lo, hi}; return __builtin_bit_cast(unsigned, __builtin_convertvector(v, bf2)); }
DI float blo(unsigned v) { return __uint_as_float(v << 16); }
DI float bhi(unsigned v) { return __uint_as_float(v & 0xffff0000u); }
DI float exp2c(float u) { return __builtin_amdgcn_fmed3f(__builtin_amdgcn_exp2f(u), 0.f, 1.f); }
constexpr float LOG2E = 1.44269504088896f, LN2 = 0.693147180559945f;
DI unsigned phi2(f2 u) {
  const f2 t = u * LN2 + 1e-6f, e = {exp2c(u[0]), exp2c(u[1])};
  const f2 v = f2{fmaxf(t[0], 1e-6f), fmaxf(t[1], 1e-6f)} + e;
  return pk2(v[0], v[1]);
}
DI u64 ld_flag(const char* ws, unsigned off) { return __hip_atomic_load((const GLBP u64*)(ws + off), __ATOMIC_RELAXED, __HIP_MEMORY_SCOPE_AGENT); }
DI void st_flag(char* ws, unsigned off, u64 v) { __hip_atomic_store((GLBP u64*)(ws + off), v, __ATOMIC_RELAXED, __HIP_MEMORY_SCOPE_AGENT); }
#define BAR() __builtin_amdgcn_s_barrier()
#define WAITV(n) asm volatile("s_waitcnt vmcnt(" #n ")" ::: "memory")
#define WAITL0() asm volatile("s_waitcnt lgkmcnt(0)" ::: "memory")
#define SCHED0() __builtin_amdgcn_sched_barrier(0)
#define FENCE() do { asm volatile("" ::: "memory"); __builtin_amdgcn_sched_barrier(0); } while (0)


struct Params {
  const char* x; const char* wqkv; const char* wo; const char* wg; const char* wu; const char* wd;
  char* out; char* ws;
};

DI void wait_flag(const char* ws, unsigned off, u64 tag, u64 first) {
  u64 v = first;
#pragma unroll 1
  for (int it = 0; it < (1 << 18); it++) {
    if (__all(v == tag)) break;
    __builtin_amdgcn_s_sleep(1);
    v = ld_flag(ws, off);
  }
  FENCE();
}
DI u64 lds_tag(LDSP char* sm) {
  const u2 t = *(LDSP u2*)(sm + AUX + 3072);
  return (u64)(unsigned)__builtin_amdgcn_readfirstlane(t[0]) | ((u64)(unsigned)__builtin_amdgcn_readfirstlane(t[1]) << 32);
}

template <int CTL> DI float dppadd(float v) { return v + __int_as_float(__builtin_amdgcn_update_dpp(0, __float_as_int(v), CTL, 0xf, 0xf, true)); }
DI float swap16add(float v) { const u2 t = __builtin_amdgcn_permlane16_swap(__float_as_uint(v), __float_as_uint(v), false, false); return __uint_as_float(t[0]) + __uint_as_float(t[1]); }
DI float sum32(float v) { return swap16add(dppadd<0x121>(dppadd<0x122>(dppadd<0x124>(dppadd<0x128>(v))))); }
constexpr unsigned EMPTY = 0xffffffffu;

DI u64 row_load(const Params& p, u2 (&xs)[5], int w, int lane) {
  const u64 gen = ld_flag(p.ws, WS_GEN);
  const rsrc_t rx = mkrs(p.x, S * D * 2);
  const unsigned xo = (unsigned)((blockIdx.x * 8 + w * 2 + (lane >> 5)) * 1280 + (lane & 31) * 8);
#pragma unroll
  for (int i = 0; i < 5; i++) xs[i] = bld64<0>(rx, xo + i * 256, 0);
  FENCE();
  return gen;
}
DI void row_stats(const u2 (&xs)[5], rsrc_t rws, int w, int lane, unsigned tag) {
  const int b = blockIdx.x;
  float s = 0;
#pragma unroll
  for (int i = 0; i < 5; i++) s += (blo(xs[i][0]) + bhi(xs[i][0])) + (blo(xs[i][1]) + bhi(xs[i][1]));
  s = sum32(s);
  const float mu = s * (1.f / 640.f);
  float q = 0;
#pragma unroll
  for (int i = 0; i < 5; i++) {
    const float a = blo(xs[i][0]) - mu, c = bhi(xs[i][0]) - mu, e = blo(xs[i][1]) - mu, g = bhi(xs[i][1]) - mu;
    q += a * a + c * c + e * e + g * g;
  }
  q = sum32(q);
  const float rs = rsqrtf(q * (1.f / 640.f) + 1e-5f);
  if ((lane & 31) == 0) bst128(u4{__float_as_uint(mu), tag, __float_as_uint(rs), tag}, rws, (b * 8 + w * 2 + (lane >> 5)) * 16, WS_STATS);
}
struct CS { rsrc_t rs; unsigned vo, step, dst; u4 d[32]; float c[8]; };
DI void cs_init(const Params& p, CS& k, int task, int lane) {
  const int kp = task / 78, cu = task - kp * 78;
  const char* base = cu < 30 ? p.wqkv + cu * 128 : (cu < 54 ? p.wg + (cu - 30) * 128 : p.wu + (cu - 54) * 128);
  const unsigned stride = cu < 30 ? 3840 : 3072;
  k.rs = mkrs(base, task < 234 ? 640 * stride - stride + 128 : 0);
  k.vo = (unsigned)(kp * 256 + (lane >> 3)) * stride + (lane & 7) * 16;
  k.step = 8 * stride;
  k.dst = (kp * NCOL + cu * 64 + (lane & 7) * 8 + (lane >> 5) * 4 + ((lane >> 3) & 1) * 2) * 8;
#pragma unroll
  for (int j = 0; j < 8; j++) k.c[j] = 0;
}
DI void cs_load(CS& k, int i) { k.d[i] = bld128<0>(k.rs, k.vo, i * k.step); }
DI void cs_add(CS& k, int i) {
#pragma unroll
  for (int j = 0; j < 4; j++) { k.c[2 * j] += blo(k.d[i][j]); k.c[2 * j + 1] += bhi(k.d[i][j]); }
}
DI void cs_store(CS& k, rsrc_t rws, int lane, unsigned tag) {
  float e[4];
#pragma unroll
  for (int j = 0; j < 4; j++) {
    const u2 t = __builtin_amdgcn_permlane32_swap(__float_as_uint(k.c[j]), __float_as_uint(k.c[4 + j]), false, false);
    e[j] = dppadd<0x128>(swap16add(__uint_as_float(t[0]) + __uint_as_float(t[1])));
  }
  if ((lane & 16) == 0) bst128(u4{__float_as_uint(lane & 8 ? e[2] : e[0]), tag, __float_as_uint(lane & 8 ? e[3] : e[1]), tag}, rws, k.dst, WS_CS);
}

template <int TYPE>
DI void k1_main(LDSP char* sm, int w, int lane, f16v (&acc)[10]) {
  constexpr int NXF = TYPE == 0 ? 5 : 2, NWF = 7 - NXF;
  constexpr int XB = TYPE == 0 ? 160 * 128 : 256 * 128;
  const int r = lane & 31, hh = lane >> 5, blk = (lane >> 4) & 1, q = (lane & 15) >> 2, pp = lane & 3;
  unsigned xa[4];
#pragma unroll
  for (int k = 0; k < 4; k++) xa[k] = (unsigned)(((TYPE == 0 ? 0 : w * 64) + r) * 128 + (((2 * k + hh) ^ ((r >> 1) & 7)) << 4));
  unsigned wa[2];
  if (TYPE == 0) {
#pragma unroll
    for (int ft = 0; ft < 2; ft++)
      wa[ft] = (unsigned)(XB + blk * 16384 + (8 * hh + q) * 256 + (((w * 4 + ft * 2 + (pp >> 1)) ^ (q << 2) ^ (blk << 1)) << 4) + (pp & 1) * 8);
  } else {
    wa[0] = (unsigned)(XB + (8 * hh + q) * 320 + blk * 32 + pp * 8); wa[1] = 0;
  }
  u4 xf[2][NXF], wf[2][NWF];
  auto rd = [&](int buf, LDSP char* base, int k) {
#pragma unroll
    for (int i = 0; i < NWF; i++) {
      u2 lo, hi;
      if (TYPE == 0) { lo = lds_tr(base + wa[i] + k * 4096); hi = lds_tr(base + wa[i] + k * 4096 + 1024); }
      else { lo = lds_tr(base + wa[0] + k * 5120 + i * 64); hi = lds_tr(base + wa[0] + k * 5120 + i * 64 + 1280); }
      wf[buf][i] = u4{lo[0], lo[1], hi[0], hi[1]};
    }
#pragma unroll
    for (int i = 0; i < NXF; i++) xf[buf][i] = lds_r128(base + xa[k] + i * 4096);
  };
  auto mm = [&](int buf) {
#pragma unroll
    for (int i = 0; i < NXF; i++)
#pragma unroll
      for (int j = 0; j < NWF; j++) {
        if (TYPE == 2) acc[i * NWF + j] = mfma32(xf[buf][i], wf[buf][j], acc[i * NWF + j]);
        else acc[i * NWF + j] = mfma32(wf[buf][j], xf[buf][i], acc[i * NWF + j]);
      }
  };
  auto inter = [&]() {
    constexpr int NR = NXF + 2 * NWF;
#pragma unroll
    for (int i = 0; i < 6; i++) {
      __builtin_amdgcn_sched_group_barrier(0x8, 1, 0);
      __builtin_amdgcn_sched_group_barrier(0x100, (NR + 5) / 6, 0);
    }
    __builtin_amdgcn_sched_group_barrier(0x8, 4, 0);
  };
  BAR();
  rd(0, sm, 0);
  int slot = 0;
#pragma unroll 1
  for (int s = 0; s < 9; s++) {
    LDSP char* base = sm + slot * SLOT;
    slot = slot == 2 ? 0 : slot + 1;
    SCHED0();
    rd(1, base, 1); mm(0); inter(); SCHED0();
    rd(0, base, 2); mm(1); inter(); SCHED0();
    rd(1, base, 3); mm(0); inter(); SCHED0();
    WAITL0(); BAR(); SCHED0();
    rd(0, sm + slot * SLOT, 0); mm(1); inter(); SCHED0();
  }
  SCHED0();
  rd(1, sm, 1); mm(0); inter(); SCHED0();
  rd(0, sm, 2); mm(1); inter(); SCHED0();
  rd(1, sm, 3); mm(0); inter(); SCHED0();
  mm(1); SCHED0();
}

DI void kv_publish(LDSP char* sm, rsrc_t rws, int rt, int cg) {
  const int t = threadIdx.x;
  if (t < 420) {
    LDSP char* src = sm + t * 16;
    const f4 s = (*(LDSP f4*)src + *(LDSP f4*)(src + 8192)) + (*(LDSP f4*)(src + 16384) + *(LDSP f4*)(src + 24576));
    u4 v = __builtin_bit_cast(u4, s);
#pragma unroll
    for (int j = 0; j < 4; j++) v[j] = min(v[j], EMPTY - 1);
    bst128(v, rws, t * 16, WS_KVP + (rt * 8 + cg) * 7168);
  }
}

template <int TYPE>
DI void k1_mfma(const Params& p, LDSP char* sm, rsrc_t rws, int w, int lane, int rt, int cg, int row0) {
  if (TYPE == 0 && (blockIdx.x >> 6) == 2) {
    const u64 a1 = (u64)(w == 0 ? p.wqkv : (w == 1 ? p.wg : (w == 2 ? p.wu : p.wd))), a2 = (u64)(w & 1 ? p.wo : p.x);
    const unsigned n1 = w == 0 ? D * 3840 : F * D * 2, n2 = w & 1 ? D * D * 2 : S * D * 2;
    const u4 r1 = {(unsigned)a1, (unsigned)(a1 >> 32) & 0xffffu, n1, 0x00020000u}, r2 = {(unsigned)a2, (unsigned)(a2 >> 32) & 0xffffu, n2, 0x00020000u};
    asm volatile("s_mov_b32 m0, %4\n\ts_nop 0\n\tbuffer_load_dword %0, %2, 0 offen lds\n\tbuffer_load_dword %1, %3, 0 offen lds"
                 :: "v"((lane * 8 + ((blockIdx.x >> 3) & 7)) * (n1 >> 9)), "v"((lane * 16 + (w >> 1) * 8 + ((blockIdx.x >> 3) & 7)) * (n2 >> 10)), "s"(r1), "s"(r2), "s"((unsigned)(size_t)(sm + AUX + 3328)) : "memory", "m0");
  }
  u2 xs[5];
  const u64 gen = row_load(p, xs, w, lane);
  const int hh = lane >> 5, r = lane & 31;
  f16v acc[10];
#pragma unroll
  for (int i = 0; i < 10; i++)
#pragma unroll
    for (int j = 0; j < 16; j++) acc[i][j] = 0.f;
#pragma unroll
  for (int i = 0; i < 10; i++) asm volatile("" : "+v"(acc[i]));
  FENCE();
  {
    const u64 tag = gen + 1;
    row_stats(xs, rws, w, lane, (unsigned)tag);
    if (threadIdx.x == 0) *(LDSP u2*)(sm + AUX + 3072) = u2{(unsigned)tag, (unsigned)(tag >> 32)};
    WAITL0();
    const u64 wa = (u64)p.ws, oa = (u64)p.out;
    const u4 r1 = {(unsigned)wa, (unsigned)(wa >> 32) & 0xffffu, WS_END, 0x00020000u}, r2 = {(unsigned)oa, (unsigned)(oa >> 32) & 0xffffu, S * D * 2, 0x00020000u};
    if (TYPE == 0 && (blockIdx.x >> 3) == 12)
    asm volatile("s_mov_b32 m0, %4\n\ts_nop 0\n\tbuffer_load_dword %0, %2, 0 offen sc1 lds\n\tbuffer_load_dword %1, %3, 0 offen sc1 lds"
                 :: "v"(threadIdx.x * 38912), "v"(threadIdx.x * 10240), "s"(r1), "s"(r2), "s"((unsigned)(size_t)(sm + AUX + 3328)) : "memory", "m0");
  }
  k1_main<TYPE>(sm, w, lane, acc);
  BAR(); FENCE();
  if (TYPE == 0) {
    f4 cs[2][4];
#pragma unroll
    for (int ft = 0; ft < 2; ft++)
#pragma unroll
      for (int qd = 0; qd < 4; qd++)
        cs[ft][qd] = *(LDSP f4*)(sm + AUX + 2048 + ((qd >> 1) * 128 + w * 32 + ft * 16 + (qd & 1) * 8 + hh * 4) * 4);
    const unsigned dst = (unsigned)(((row0 >> 5) * 96 + cg * 8 + w * 2) * 1024 + lane * 16);
#pragma unroll
    for (int tt = 0; tt < 5; tt++) {
      const float mu = *(LDSP float*)(sm + AUX + (tt * 32 + r) * 4);
      if (tt == 0 && rt == 12) continue;
#pragma unroll
      for (int ft = 0; ft < 2; ft++) {
        const f16v a = acc[tt * 2 + ft];
        float h[8];
#pragma unroll
        for (int j = 0; j < 8; j++) h[j] = (a[j] - mu * cs[ft][j >> 2][j & 3]) * (a[8 + j] - mu * cs[ft][2 + (j >> 2)][j & 3]);
        bst128(u4{pk2(h[0], h[1]), pk2(h[2], h[3]), pk2(h[4], h[5]), pk2(h[6], h[7])}, rws, dst + ft * 1024, WS_H + tt * 98304);
      }
    }
    FENCE(); BAR();
    WAITV(0); BAR();
    if (threadIdx.x == 0) st_flag(p.ws, WS_FH + (rt * 16 + cg) * 8, lds_tag(sm));
  } else if (TYPE == 1) {
    u4 pq[2][10];
#pragma unroll
    for (int tt = 0; tt < 2; tt++) {
      const float mu = *(LDSP float*)(sm + AUX + (w * 64 + tt * 32 + r) * 4);
      const float rl = *(LDSP float*)(sm + AUX + 1024 + (w * 64 + tt * 32 + r) * 4) * LOG2E;
#pragma unroll
      for (int ft = 0; ft < 5; ft++) {
        const f16v a = acc[tt * 5 + ft];
#pragma unroll
        for (int qd = 0; qd < 4; qd++) {
          const f4 cs = *(LDSP f4*)(sm + AUX + 2048 + (ft * 32 + qd * 8 + hh * 4) * 4);
#pragma unroll
          for (int j = 0; j < 2; j++) {
            const f2 u = (f2{a[qd * 4 + 2 * j], a[qd * 4 + 2 * j + 1]} - f2{cs[2 * j], cs[2 * j + 1]} * mu) * rl;
            pq[tt][ft * 2 + (qd >> 1)][(qd & 1) * 2 + j] = phi2(u);
          }
        }
        asm volatile("" : "+v"(pq[tt][ft * 2]), "+v"(pq[tt][ft * 2 + 1]));
      }
    }
    SCHED0(); BAR(); SCHED0();
    const unsigned dst = (unsigned)((((row0 >> 5) + w * 2) * 40 + cg * 10) * 1024 + lane * 16);
    f16v oa[2][2];
    u4 af[2][3];
    auto rdi = [&](int n) {
      const int hg = n / 5, T = n % 5, k0 = T < 2 ? 0 : T - 1, k1 = T == 0 ? 1 : (T == 4 ? 4 : T + 1);
#pragma unroll
      for (int k = k0; k <= k1; k++) af[n & 1][k - k0] = lds_r128(sm + IMG + (hg * 25 + T * 5 + k) * 1024 + lane * 16);
    };
    auto omm = [&](int n) {
      const int hg = n / 5, T = n % 5, k0 = T < 2 ? 0 : T - 1, k1 = T == 0 ? 1 : (T == 4 ? 4 : T + 1);
#pragma unroll
      for (int tt = 0; tt < 2; tt++)
#pragma unroll
        for (int j = 0; j < 16; j++) oa[n & 1][tt][j] = 0.f;
#pragma unroll
      for (int k = k0; k <= k1; k++)
#pragma unroll
        for (int tt = 0; tt < 2; tt++) oa[n & 1][tt] = mfma32(af[n & 1][k - k0], pq[tt][hg * 5 + k], oa[n & 1][tt]);
    };
    rdi(0); rdi(1); omm(0); SCHED0();
#pragma unroll
    for (int n = 0; n < 10; n++) {
      if (n + 1 < 10) omm(n + 1);
      if (n + 2 < 10) rdi(n + 2);
#pragma unroll
      for (int tt = 0; tt < 2; tt++) {
        const f16v a = oa[n & 1][tt];
        const float d0 = __builtin_amdgcn_rcpf(a[8] + 1e-6f), d1 = __builtin_amdgcn_rcpf(a[12] + 1e-6f);
        bst128(u4{pk2(a[0] * d0, a[1] * d0), pk2(a[2] * d0, a[3] * d0), pk2(a[4] * d1, a[5] * d1), pk2(a[6] * d1, a[7] * d1)},
               rws, dst + (n & 3) * 1024, WS_O + tt * 40960 + (n >> 2) * 4096);
      }
#pragma unroll
      for (int i = 0; i < 6; i++) { __builtin_amdgcn_sched_group_barrier(0x8, 1, 0); __builtin_amdgcn_sched_group_barrier(0x2, 5, 0); }
      SCHED0();
    }
    WAITV(0); BAR();
    if (threadIdx.x == 0) st_flag(p.ws, WS_FO + (rt * 16 + cg) * 8, lds_tag(sm));
  } else {
    float cs[5];
#pragma unroll
    for (int ct = 0; ct < 5; ct++) cs[ct] = *(LDSP float*)(sm + AUX + 2048 + (ct * 32 + r) * 4);
    const bool isk = r < 16;
    const float kmul = isk ? 1.f : 0.f, kadd = isk ? 1e-6f : 0.f, klo = isk ? 1e-6f : -__builtin_inff();
    u4 fr[4][5];
#pragma unroll
    for (int g = 0; g < 4; g++) {
      f2 mu[4], rs[4], rl[4];
#pragma unroll
      for (int hq = 0; hq < 2; hq++) {
        LDSP char* sp = sm + AUX + (w * 64 + g * 16 + hq * 8 + hh * 4) * 4;
        const f4 a = *(LDSP f4*)sp, b = *(LDSP f4*)(sp + 1024);
        mu[hq * 2] = f2{a[0], a[1]}; mu[hq * 2 + 1] = f2{a[2], a[3]};
        rs[hq * 2] = f2{b[0], b[1]}; rs[hq * 2 + 1] = f2{b[2], b[3]};
      }
#pragma unroll
      for (int j = 0; j < 4; j++) rl[j] = rs[j] * LOG2E;
#pragma unroll
      for (int ct = 0; ct < 5; ct++) {
#pragma unroll
        for (int j = 0; j < 4; j++) {
          const f16v& a = acc[(g >> 1) * 5 + ct];
          const f2 y = f2{a[(g & 1) * 8 + 2 * j], a[(g & 1) * 8 + 2 * j + 1]} - mu[j] * cs[ct];
          if (ct < 2) fr[g][ct][j] = phi2(y * rl[j]);
          else if (ct == 2) {
            const f2 u = y * rl[j], t = y * rs[j] + kadd, e = {exp2c(u[0]), exp2c(u[1])};
            const f2 v = e * kmul + f2{fmaxf(t[0], klo), fmaxf(t[1], klo)};
            fr[g][ct][j] = pk2(v[0], v[1]);
          } else { const f2 v = y * rs[j]; fr[g][ct][j] = pk2(v[0], v[1]); }
        }
      }
      SCHED0();
    }
    f16v z[8];
#pragma unroll
    for (int i = 0; i < 8; i++)
#pragma unroll
      for (int j = 0; j < 16; j++) z[i][j] = 0.f;
#pragma unroll
    for (int g = 0; g < 4; g++) {
      u4 mb;
#pragma unroll
      for (int j = 0; j < 4; j++) mb[j] = isk ? 0x3f803f80u : fr[g][2][j];
      z[0] = mfma32(fr[g][0], fr[g][3], z[0]);
      z[1] = mfma32(fr[g][0], fr[g][4], z[1]);
      z[2] = mfma32(fr[g][0], mb, z[2]);
      z[3] = mfma32(fr[g][1], fr[g][3], z[3]);
      z[4] = mfma32(fr[g][1], fr[g][4], z[4]);
      z[5] = mfma32(fr[g][1], mb, z[5]);
      z[6] = mfma32(fr[g][2], fr[g][4], z[6]);
      z[7] = mfma32(fr[g][2], mb, z[7]);
    }
    unsigned pa[3], pm[3];
#pragma unroll
    for (int vt = 0; vt < 3; vt++) {
      const int e = vt == 2 ? 48 + r : vt * 32 + r, he = e / 20;
      int ee = e - he * 20; unsigned m = 31u << (5 * he);
      if (vt == 2 && isk) { ee = 20; m = r == 0 ? 0xfffffu : 0u; }
      pa[vt] = (unsigned)(w * 8192 + ee * 16 + hh * 336); pm[vt] = m >> hh;
    }
    SCHED0();
#pragma unroll
    for (int i = 0; i < 8; i++)
#pragma unroll
      for (int qd = 0; qd < 4; qd++) {
        const int kt = i / 3, vt = i < 6 ? i % 3 : i - 5, c = kt * 8 + qd * 2;
        const bool need = (i == 1) ? qd >= 2 : (i == 3) ? qd == 0 : (i >= 6) ? qd < 2 : true;
        if (need && (pm[vt] & (1u << c)))
          *(LDSP f4*)(sm + pa[vt] + c * 336) = f4{z[i][qd * 4], z[i][qd * 4 + 1], z[i][qd * 4 + 2], z[i][qd * 4 + 3]};
      }
    WAITL0(); BAR();
    kv_publish(sm, rws, rt, cg);
    WAITL0(); BAR();
  }
}

DI void p2_out(const Params& p, LDSP char* sm, int item, u4 xv, unsigned go) {
  const int row = item >> 3, c = item & 7;
  LDSP char* src = sm + (row >> 3) * 8192 + (row & 7) * 256;
  const unsigned c0 = ((2 * c) ^ (row & 7)) << 4, c1 = c0 ^ 16;
  f4 a = *(LDSP f4*)(src + c0), b = *(LDSP f4*)(src + c1);
#pragma unroll
  for (int i = 1; i < 4; i++) { a += *(LDSP f4*)(src + i * 2048 + c0); b += *(LDSP f4*)(src + i * 2048 + c1); }
  bst128(u4{pk2(blo(xv[0]) + a[0], bhi(xv[0]) + a[1]), pk2(blo(xv[1]) + a[2], bhi(xv[1]) + a[3]),
            pk2(blo(xv[2]) + b[0], bhi(xv[2]) + b[1]), pk2(blo(xv[3]) + b[2], bhi(xv[3]) + b[3])}, mkrs(p.out, S * D * 2), go, 0);
}

constexpr int PD = 8;
DI void p2_mfma(const Params& p, LDSP char* sm, rsrc_t rws, int w, int lane, int row2, int cg2) {
  const int hh = lane >> 5, r = lane & 31, blk = (lane >> 4) & 1, q = (lane & 15) >> 2, pp = lane & 3;
  const u64 tag = lds_tag(sm);
  const unsigned fh = WS_FH + (((row2 + ((lane >> 2) & 1) * 95) / 160) * 16 + 3 * w + min(lane & 3, 2)) * 8;
  const unsigned fo = WS_FO + (((row2 + (lane & 1) * 95) >> 8) * 16 + w) * 8;
  float r2[3];
  wait_flag(p.ws, fh, tag, ld_flag(p.ws, fh));
  const unsigned ah = (unsigned)(((row2 >> 5) * 96 + w * 24) * 1024 + lane * 16), ao = (unsigned)(((row2 >> 5) * 40 + w * 10) * 1024 + lane * 16);
  u4 af[PD][3], bf[2][2];
  auto lda = [&](int i) {
#pragma unroll
    for (int tt = 0; tt < 3; tt++) {
      if (i < 24) af[i % PD][tt] = bld128<SC1>(rws, ah + (i & 3) * 1024, WS_H + tt * 98304 + (i >> 2) * 4096);
      else af[i % PD][tt] = bld128<SC1>(rws, ao + (i & 3) * 1024, WS_O + tt * 40960 + ((i - 24) >> 2) * 4096);
    }
  };
  LDSP char* ba[2][3];
#pragma unroll
  for (int ct = 0; ct < 2; ct++)
#pragma unroll
    for (int sg = 0; sg < 3; sg++) {
      ba[ct][sg] = sm + sg * 49152 + w * 2048 + (4 * hh + q) * 128 + (((ct * 4 + blk * 2 + (pp >> 1)) ^ ((q >> 1) << 2)) << 4) + (pp & 1) * 8;
      asm volatile("" : "+v"(ba[ct][sg]));
    }
  auto ldb = [&](int i) {
    const int sl = 17 - (i < 18 ? i : i - 18);
#pragma unroll
    for (int ct = 0; ct < 2; ct++) {
      LDSP char* base = ba[ct][sl / 6] + (sl % 6) * 8192;
      const u2 lo = lds_tr(base), hi = lds_tr(base + 1024);
      bf[i & 1][ct] = u4{lo[0], lo[1], hi[0], hi[1]};
    }
  };
  f16v acc[6];
#pragma unroll
  for (int i = 0; i < 6; i++)
#pragma unroll
    for (int j = 0; j < 16; j++) acc[i][j] = 0.f;
#pragma unroll
  for (int i = 0; i < PD; i++) lda(i);
  ldb(0);
  u64 ofl = 0;
  auto step = [&](int i, bool pre) {
    SCHED0();
    if (i == 7 || i == 15 || i == 23) { WAITL0(); BAR(); SCHED0(); }
    if (i + 1 < 34) ldb(i + 1);
#pragma unroll
    for (int tt = 0; tt < 3; tt++)
#pragma unroll
      for (int ct = 0; ct < 2; ct++) acc[tt * 2 + ct] = mfma32(bf[i & 1][ct], af[i % PD][tt], acc[tt * 2 + ct]);
    SCHED0();
    if (pre) lda(i + PD);
    if (i == 23) {
#pragma unroll
      for (int tt = 0; tt < 3; tt++) { acc[tt * 2] *= r2[tt] * r2[tt]; acc[tt * 2 + 1] *= r2[tt] * r2[tt]; }
#pragma unroll
      for (int k = 0; k < 6; k++) asm volatile("" : "+v"(acc[k]));
      SCHED0();
    }
  };
  constexpr int OCHK = 20;
#pragma unroll
  for (int i = 0; i < OCHK; i++) {
    step(i, i + PD < 24);
    if (i == 16) {
#pragma unroll
      for (int tt = 0; tt < 3; tt++) r2[tt] = __uint_as_float(bld32<SC1>(rws, (row2 + tt * 32 + r) * 16 + 8, WS_STATS));
      ofl = ld_flag(p.ws, fo);
    }
  }
  const bool early = __all(ofl == tag);
  FENCE();
  if (early) {
#pragma unroll
    for (int j = 24; j < OCHK + PD; j++) lda(j);
  }
#pragma unroll
  for (int i = OCHK; i < 24; i++) step(i, false);
  if (!early) {
    wait_flag(p.ws, fo, tag, ld_flag(p.ws, fo));
#pragma unroll
    for (int j = 24; j < OCHK + PD; j++) lda(j);
  }
#pragma unroll
  for (int j = OCHK + PD; j < 24 + PD; j++) lda(j);
#pragma unroll
  for (int i = 24; i < 34; i++) step(i, i + PD < 34);
  const int item = 512 + threadIdx.x;
  const unsigned go = (unsigned)((row2 + (item >> 3)) * 1280 + cg2 * 128 + (item & 7) * 16);
  const u4 xv = *(const GLBP u4*)(p.x + go);
#pragma unroll
  for (int tt = 0; tt < 3; tt++)
#pragma unroll
    for (int ct = 0; ct < 2; ct++)
#pragma unroll
      for (int qd = 0; qd < 4; qd++)
        *(LDSP f4*)(sm + (tt * 4 + (r >> 3)) * 8192 + w * 2048 + (r & 7) * 256 + (((ct * 8 + qd * 2 + hh) ^ (r & 7)) << 4)) =
            f4{acc[tt * 2 + ct][qd * 4], acc[tt * 2 + ct][qd * 4 + 1], acc[tt * 2 + ct][qd * 4 + 2], acc[tt * 2 + ct][qd * 4 + 3]};
  WAITL0(); BAR();
  p2_out(p, sm, item, xv, go);
}

template <int type>
DI void loader(const Params& p, LDSP char* sm, rsrc_t rws, int lw, int lane, int rt, int cg, int row0, int row2, int cg2) {
  const int b = blockIdx.x, t = lw * 64 + lane;
  constexpr bool gu = type == 0;
  unsigned vo[13];
  {
    const int x0 = gu ? lw * 5 : lw * 8;
    const unsigned xr = (unsigned)((row0 + x0 * 8 + (lane >> 3)) * 1280), xs = (unsigned)(((lane & 7) ^ (lane >> 4) ^ ((x0 & 1) << 2)) << 4);
#pragma unroll
    for (int i = 0; i < 8; i++) vo[i] = xr + (xs ^ ((i & 1) << 6)) + i * 10240;
    if (gu) {
      const unsigned g0 = (unsigned)(((lw & 1) * 32 + (lane >> 4)) * 3072 + (((lane & 15) ^ ((lane >> 4) << 2) ^ (lw & 2)) << 4));
#pragma unroll
      for (int i = 5; i < 13; i++) vo[i] = g0 + (i - 5) * 12288;
    } else {
#pragma unroll
      for (int i = 0; i < 5; i++) {
        const int e = i * 64 + lane, rq = (e * 205) >> 12, pos = e - rq * 20;
        const int adj = type == 1 ? 0 : (pos < 10 ? 0 : (pos < 12 ? 1248 : 1088));
        vo[8 + i] = (unsigned)((lw * 16 + rq) * 3840 + pos * 16 + adj);
      }
    }
  }
  const unsigned wofs = gu ? cg * 256 : (type == 1 ? cg * 320 : 1280 + cg * 160);
  const char* wbase = (gu ? ((lw >> 1) ? p.wu : p.wg) : p.wqkv) + wofs;
  const unsigned wstep = gu ? 64 * 3072 : 64 * 3840, wlen = wstep * 10 - wofs, mstep = gu ? wstep : 128;
  const rsrc_t rx = mkrs(p.x, S * D * 2), rw = mkrs(wbase, wlen), rm = mkrs(gu ? wbase : p.x, gu ? wlen : S * D * 2);
  const int dx = gu ? lw * 5120 : lw * 8192, dm = gu ? 20480 + lw * 8192 : dx + 5120, dw = gu ? dm + 3072 : 32768 + lw * 5120;
  CS ck;
  const int task = type == 1 ? (rt * 4 + cg) * 4 + lw : min(156 + lw * 64 + cg * 8 + rt, 234);
  const bool act = !gu && task < 234;
  auto issue = [&](int s, int a0 = 0, int a1 = 0) {
    LDSP char* d = sm + (s % 3) * SLOT;
#pragma unroll
    for (int i = 0; i < 13; i++) {
      if (i < 5) blds16(rx, vo[i], s * 128, d + dx + i * 1024);
      else if (i < 8) blds16(rm, vo[i], s * mstep, d + dm + (i - 5) * 1024);
      else blds16(rw, vo[i], s * wstep, d + dw + (i - 8) * 1024);
      if (i >= 1 && a0 + i - 1 < a1 && (type == 1 || act)) {
        FENCE(); cs_add(ck, a0 + i - 1);
        asm volatile("" : "+v"(ck.c[0]), "+v"(ck.c[1]), "+v"(ck.c[2]), "+v"(ck.c[3]), "+v"(ck.c[4]), "+v"(ck.c[5]), "+v"(ck.c[6]), "+v"(ck.c[7]));
        FENCE();
      }
    }
    FENCE();
  };
  auto csl = [&](int i0, int i1) {
    if (type == 1 || act) {
#pragma unroll
      for (int i = i0; i < i1; i++) cs_load(ck, i);
      FENCE();
    }
  };
  issue(0); issue(1);
  if (!gu) cs_init(p, ck, task, lane);
  constexpr bool q1 = type == 1;
  WAITV(13); BAR();
  issue(2);
  if (type == 2) {
    bst128(u4{EMPTY, EMPTY, EMPTY, EMPTY}, rws, t * 16, WS_KVP + (rt * 8 + cg) * 7168);
    bst128(u4{EMPTY, EMPTY, EMPTY, EMPTY}, rws, min(t + 256, 419) * 16, WS_KVP + (rt * 8 + cg) * 7168);
    FENCE();
  }
  const rsrc_t rd_ = mkrs(p.wd, type == 1 ? 0 : F * D * 2), ro_ = mkrs(p.wo, type == 1 ? 0 : D * D * 2);
  const unsigned bo = (unsigned)((lane >> 3) * 1280 + cg2 * 128 + (((lane & 7) ^ (((lane >> 4) & 1) << 2)) << 4));
  auto bdma = [&](int i) {
#pragma unroll
    for (int h = 0; h < 2; h++) blds16(rd_, bo, (lw * 384 + i * 16 + h * 8) * 1280, sm + (17 - i) * 8192 + lw * 2048 + h * 1024);
  };
  u4 br[46];
  auto breg = [&](int i0, int i1) {
#pragma unroll
    for (int i = i0; i < i1; i++) {
      const int st = 11 + (i >> 1);
      br[i] = st < 24 ? bld128<0>(rd_, bo, (lw * 384 + st * 16 + (i & 1) * 8) * 1280) : bld128<0>(ro_, bo, (lw * 160 + (st - 24) * 16 + (i & 1) * 8) * 1280);
    }
    FENCE();
  };
  auto bput = [&](int i0, int i1) {
#pragma unroll
    for (int i = i0; i < i1; i++) {
      const int st = 11 + (i >> 1), sl = st < 18 ? st : st - 18;
      *(LDSP u4*)(sm + (17 - sl) * 8192 + lw * 2048 + (i & 1) * 1024 + lane * 16) = br[i];
    }
  };
  auto bgu = [&](int i0, int i1) { if (gu) breg(i0, i1); };
  auto bkv = [&](int i0, int i1) { if (type == 2) breg(i0, i1); };
#define WAITG(a, b, c) do { if (gu) WAITV(a); else if (type == 1) WAITV(b); else WAITV(c); } while (0)
#define WAITA(a, b, c, d) do { if (type == 2 && act) WAITV(d); else WAITG(a, b, c); } while (0)
  csl(0, q1 ? 11 : 8); WAITA(13, 24, 15, 23); BAR();
  issue(3); csl(q1 ? 11 : 8, q1 ? 22 : 16); WAITA(13, 35, 13, 29); BAR();
  const u64 tag = lds_tag(sm);
  if (t < (type == 2 ? 2 : 1)) st_flag(p.ws, t ? WS_FKV + (cg * 8 + rt) * 8 : WS_F0 + b * 8, tag);
  FENCE();
  issue(4, 0, q1 ? 11 : 0); bgu(0, 4); csl(22, q1 ? 32 : 0); WAITA(17, 34, 13, 21); BAR();
  issue(5, q1 ? 11 : 0, q1 ? 22 : 8); bgu(4, 8); WAITG(21, 23, 13); BAR();
  issue(6, q1 ? 22 : 8, q1 ? 32 : 16); bgu(8, 12);
  if (type == 1 || act) cs_store(ck, rws, lane, (unsigned)tag);
  WAITA(21, 14, 13, 14); BAR();
  issue(7); bgu(12, 16); bkv(0, 4);
  unsigned sto = min(row0 + t, S - 1) * 16, cso;
  {
    const int tc = gu ? t : min(t, 159);
    cso = 8 * (gu ? 1920 + (tc >> 7) * 1536 + cg * 128 + (tc & 127)
                  : (type == 1 ? cg * 160 + tc : cg * 80 + tc + (tc < 80 ? 640 : (tc < 96 ? 1264 : 1184))));
  }
  asm volatile("" : "+v"(sto), "+v"(cso));
  WAITA(21, 14, 17, 18); BAR();
  issue(8); bgu(16, 20); bkv(4, 8); WAITG(21, 13, 21); BAR();
  issue(9); bgu(20, 24); bkv(8, 12); WAITG(21, 13, 21); BAR();
  u4 stv; u2 csv[3];
  auto aux = [&]() {
    stv = bld128<SC1>(rws, sto, WS_STATS);
#pragma unroll
    for (int i = 0; i < 3; i++) csv[i] = bld64<SC1>(rws, cso, WS_CS + i * NCOL * 8);
    FENCE();
  };
  aux();
#pragma unroll
  for (int i = 5; i < 11; i++) bdma(i);
  FENCE();
  if (type == 1) *(LDSP u2*)(sm + IMG + t * 8) = u2{0, 0};
  WAITG(20, 16, 20); BAR();
  if (type != 1) {
#pragma unroll
    for (int i = 0; i < 5; i++) bdma(i);
    FENCE();
  }
  auto fresh = [&]() {
    const unsigned tg = (unsigned)tag;
    return __all(stv[1] == tg && stv[3] == tg && csv[0][1] == tg && csv[1][1] == tg && csv[2][1] == tg);
  };
  if (!fresh()) {
#pragma unroll 1
    for (int it = 0; it < (1 << 18); it++) { aux(); if (fresh()) break; }
  }
  *(LDSP unsigned*)(sm + AUX + t * 4) = stv[0];
  *(LDSP unsigned*)(sm + AUX + 1024 + t * 4) = stv[2];
  *(LDSP float*)(sm + AUX + 2048 + t * 4) = (__uint_as_float(csv[0][0]) + __uint_as_float(csv[1][0])) + __uint_as_float(csv[2][0]);
  if (type == 1) WAITV(0);
  FENCE();
  bgu(24, 28);
  WAITL0(); BAR();
  if (type == 1) {
    const unsigned fk = WS_FKV + (cg * 16 + (lane & 15)) * 8;
    wait_flag(p.ws, fk, tag, ld_flag(p.ws, fk));
    u4 pv[4][8];
    unsigned off[4], da[4];
#pragma unroll
    for (int m = 0; m < 4; m++) {
      const int g = min(t + m * 256, 999), dd = g / 25, e5 = g - dd * 25, hg = dd >= 20, dq = dd - hg * 20;
      const int eg = (dq / 5) * 20 + (e5 < 20 ? e5 : (e5 - 20) * 4);
      off[m] = WS_KVP + (cg * 2 + hg) * 7168 + (dq * 21 + min(e5, 20)) * 16;
      da[m] = IMG + hg * 25600 + (dq >> 2) * 1024 + (dq & 1) * 512 + ((dq >> 1) & 1) * 8 + (eg >> 4) * 5120 + (eg & 15) * 16 + (e5 < 20 ? 0 : 256);
      asm volatile("" : "+v"(off[m]), "+v"(da[m]));
#pragma unroll
      for (int i = 0; i < 8; i++) asm volatile("" : "=v"(pv[m][i]));
    }
    const u4 rk = {(unsigned)(size_t)p.ws, (unsigned)((size_t)p.ws >> 32) & 0xffffu, WS_END, 0x00020000u};
    auto fetch = [&](unsigned set) {
#pragma unroll
      for (int i = 0; i < 8; i++)
        if (set >> i & 1)
          asm volatile("buffer_load_dwordx4 %0, %4, %8, %9 offen sc1\n\tbuffer_load_dwordx4 %1, %5, %8, %9 offen sc1\n\t"
                       "buffer_load_dwordx4 %2, %6, %8, %9 offen sc1\n\tbuffer_load_dwordx4 %3, %7, %8, %9 offen sc1"
                       : "+v"(pv[0][i]), "+v"(pv[1][i]), "+v"(pv[2][i]), "+v"(pv[3][i])
                       : "v"(off[0]), "v"(off[1]), "v"(off[2]), "v"(off[3]), "s"(rk), "s"(i * 8 * 7168) : "memory");
    };
    auto landed = [&]() {
      WAITV(0);
#pragma unroll
      for (int i = 0; i < 8; i++) asm volatile("" : "+v"(pv[0][i]), "+v"(pv[1][i]), "+v"(pv[2][i]), "+v"(pv[3][i]));
    };
    {
      const unsigned ho = WS_KVP + ((lane & 7) * 8 + cg * 2 + ((lane >> 3) & 1)) * 7168 + 419 * 16;
      unsigned got = 0;
#pragma unroll 1
      for (int it = 0; it < (1 << 18) && got != 255; it++) {
        const unsigned mk = (unsigned)__ballot(bld32<SC1>(rws, ho, 0) != EMPTY), now = mk & (mk >> 8) & 255 & ~got;
        fetch(now);
        got |= now;
      }
    }
    f4 sum[4];
#pragma unroll 1
    for (int it = 0; it < (1 << 18); it++) {
      landed();
      bool nan = false;
#pragma unroll
      for (int m = 0; m < 4; m++) {
        sum[m] = __builtin_bit_cast(f4, pv[m][0]);
#pragma unroll
        for (int i = 1; i < 8; i++) sum[m] += __builtin_bit_cast(f4, pv[m][i]);
        nan = nan || __builtin_isunordered(sum[m][0], sum[m][1]) || __builtin_isunordered(sum[m][2], sum[m][3]);
      }
      if (!__any(nan)) break;
      unsigned mx = 0;
#pragma unroll
      for (int i = 0; i < 8; i++)
#pragma unroll
        for (int m = 0; m < 4; m++) mx = max(max(mx, max(pv[m][i][0], pv[m][i][1])), max(pv[m][i][2], pv[m][i][3]));
      if (!__any(mx == EMPTY)) break;
      fetch(255);
    }
#pragma unroll
    for (int m = 0; m < 4; m++) *(LDSP u2*)(sm + da[m]) = u2{pk2(sum[m][0], sum[m][1]), pk2(sum[m][2], sum[m][3])};
    WAITL0(); BAR();
    BAR();
    return;
  }
  __builtin_amdgcn_s_setprio(0);
  if (gu) {
    BAR();
    breg(28, 38);
    WAITV(14);
  } else {
    breg(12, 30);
    BAR(); kv_publish(sm, rws, rt, cg); WAITL0();
    WAITV(18);
  }
  BAR();
  bput(0, 14);
  FENCE();
  bkv(30, 46); bgu(38, 46);
  WAITL0(); BAR();
  bput(14, 30);
  WAITL0(); BAR();
  bput(30, 46);
  WAITL0(); BAR();
  unsigned go[2]; u4 xv[2];
#pragma unroll
  for (int i = 0; i < 2; i++) {
    const int item = i * 256 + t;
    go[i] = (unsigned)((row2 + (item >> 3)) * 1280 + cg2 * 128 + (item & 7) * 16);
    xv[i] = *(const GLBP u4*)(p.x + go[i]);
  }
  BAR();
#pragma unroll
  for (int i = 0; i < 2; i++) p2_out(p, sm, i * 256 + t, xv[i], go[i]);
}

__global__ __launch_bounds__(512, 2) void fused_kernel(Params) {
  typedef unsigned long long ka_t __attribute__((ext_vector_type(8)));
  ka_t ka;
  asm volatile("s_load_dwordx16 %0, %1, 0x0" : "=s"(ka) : "s"(__builtin_amdgcn_kernarg_segment_ptr()));
  extern __shared__ __attribute__((aligned(16))) char smem[];
  LDSP char* sm = (LDSP char*)smem;
  const int lane = threadIdx.x & 63, w = __builtin_amdgcn_readfirstlane(threadIdx.x >> 6), b = blockIdx.x;
  u64 pc0 = __builtin_amdgcn_s_getpc();
  if (w < 2) {
    const u4 rc = {(unsigned)pc0, (unsigned)(pc0 >> 32) & 0xffffu, CODE_BYTES, 0x00020000u};
    asm volatile("s_mov_b32 m0, %0\n\ts_nop 0\n\tbuffer_load_dword %1, %2, 0 offen lds"
                 :: "s"((unsigned)(size_t)(sm + AUX + 3328)), "v"(min(threadIdx.x * 128 + ((blockIdx.x >> 3) & 3) * 16384, CODE_BYTES - 128)), "s"(rc) : "memory", "m0");
  }
  FENCE();
  int type, rt, cg, n2;
  {
    const int c = b & 7, j = b >> 3, a = c & 1, q = c >> 1;
    int lo = (j - 12) >> 31;
    asm volatile("" : "+s"(lo));
    const int n = j + ((a * 19 - 12) & ~lo), n3 = (n * 43) >> 7, m = n - n3 * 3, t1 = min(m, 1) & lo;
    type = ((t1 + 1) & lo) + ((((b | 6) + 2) >> 8) & ~a) * 3;
    rt = n3 + (4 * a & lo);
    cg = q * (3 + ((t1 - 2) & lo)) + m - t1;
    n2 = j - 4 + ((3 - n3) & lo);
    n2 = max(n2 + q * 55 + a * 27, 0);
  }
  int row0 = min(rt * (b < 96 ? 256 : 160), S - 160);
  const int rt2 = (n2 * 205) >> 11;
  int cg2 = n2 - rt2 * 10, row2 = min(rt2 * 96, S - 96);
  asm volatile("s_waitcnt lgkmcnt(0)" : "+s"(ka), "+s"(type), "+s"(rt), "+s"(cg), "+s"(row0), "+s"(row2), "+s"(cg2));
  const Params p = {(const char*)ka[0], (const char*)ka[1], (const char*)ka[2], (const char*)ka[3], (const char*)ka[4], (const char*)ka[5], (char*)ka[6], (char*)ka[7]};
  const rsrc_t rws = mkrs(p.ws, WS_END);
  if (type == 3) {
    u2 xs[5];
    const u64 tag = row_load(p, xs, w & 3, lane) + 1;
    const int task = 128 + ((b >> 1) & 3) + w * 4;
    CS ck;
    cs_init(p, ck, task < 156 ? task : 234, lane);
#pragma unroll
    for (int i = 0; i < 32; i++) cs_load(ck, i);
    FENCE();
    if (w < 4) row_stats(xs, rws, w, lane, (unsigned)tag);
#pragma unroll
    for (int i = 0; i < 32; i++) cs_add(ck, i);
    if (task < 156) cs_store(ck, rws, lane, (unsigned)tag);
    BAR();
    if (w != 0) return;
    if (lane == 0) st_flag(p.ws, WS_F0 + b * 8, tag);
    if (b != 248) return;
#pragma unroll 1
    for (int i = 0; i < 4; i++) wait_flag(p.ws, WS_F0 + (i * 64 + lane) * 8, tag, tag - 1);
    if (lane == 0) st_flag(p.ws, WS_GEN, tag);
    return;
  }
  if (w >= 4) {
    __builtin_amdgcn_s_setprio(3);
    if (type == 2) loader<2>(p, sm, rws, w - 4, lane, rt, cg, row0, row2, cg2);
    else if (type == 0) loader<0>(p, sm, rws, w - 4, lane, rt, cg, row0, row2, cg2);
    else loader<1>(p, sm, rws, w - 4, lane, rt, cg, row0, row2, cg2);
  } else {
    if (type == 0) k1_mfma<0>(p, sm, rws, w, lane, rt, cg, row0);
    else if (type == 1) k1_mfma<1>(p, sm, rws, w, lane, rt, cg, row0);
    else k1_mfma<2>(p, sm, rws, w, lane, rt, cg, row0);
    if (type != 1) p2_mfma(p, sm, rws, w, lane, row2, cg2);
  }
}

extern "C" void kernel_launch(void* const* d_in, const int* in_sizes, int n_in, void* d_out, int out_size,
                              void* d_ws, size_t ws_size, hipStream_t stream) {
  Params p;
  p.x = (const char*)d_in[0]; p.wqkv = (const char*)d_in[1]; p.wo = (const char*)d_in[2];
  p.wg = (const char*)d_in[4]; p.wu = (const char*)d_in[5]; p.wd = (const char*)d_in[6];
  p.out = (char*)d_out; p.ws = (char*)d_ws;
  (void)hipFuncSetAttribute((const void*)fused_kernel, hipFuncAttributeMaxDynamicSharedMemorySize, LDS_BYTES);
  fused_kernel<<<256, 512, LDS_BYTES, stream>>>(p);
}
```
